```python
import torch

def _softmax(x, axis=-1):
    return torch.softmax(x, dim=axis)
def _gelu(x):
    return torch.nn.functional.gelu(x, approximate="tanh")
def _silu(x):
    return torch.nn.functional.silu(x)
def _sigmoid(x):
    return torch.sigmoid(x)
def _layernorm(x, axis=-1, eps=1e-5):
    m = x.mean(dim=axis, keepdim=True)
    v = x.var(dim=axis, keepdim=True, unbiased=False)
    return (x - m) / torch.sqrt(v + eps)
def _scatter_add(tgt, idx, src, axis=0):
    return tgt.clone().index_add_(axis, idx, src)
def _segment_sum(src, seg, n):
    out = torch.zeros((n,) + src.shape[1:], dtype=src.dtype)
    return out.index_add_(0, seg, src)
def _pad(x, pw):
    flat = tuple(v for pair in reversed(pw) for v in pair)
    return torch.nn.functional.pad(x, flat)

kernel_name = 'sequential_preLN__ssm_conv_s4d__swiglu_ffn__prompt'

def setup_inputs():

    _w = (lambda a: (lambda t: t.to(torch.bfloat16) if t.is_floating_point() else t)(torch.as_tensor(a)))
    def _A_setup():
        Bsz, L, D, N = 1, 2048, 512, 128
        return {
            "u": _w((lambda *s: torch.randn(*s, dtype=torch.bfloat16) * 0.05)(Bsz, L, D)),
            "log_A_real": _w((lambda *s: torch.randn(*s, dtype=torch.bfloat16) * 6.4)(D, N) * 0.5),
            "A_imag": _w((lambda *s: torch.randn(*s, dtype=torch.bfloat16) * 0.05)(D, N) * 3.14),
            "C_real": _w((lambda *s: torch.randn(*s, dtype=torch.bfloat16) * 0.05)(D, N) / N ** 0.5),
            "C_imag": _w((lambda *s: torch.randn(*s, dtype=torch.bfloat16) * 0.05)(D, N) / N ** 0.5),
            "log_dt": _w((lambda *s: torch.randn(*s, dtype=torch.bfloat16) * 0.05)(D) * 0.5 - 4.0),
            "D_skip": _w((lambda *s: torch.randn(*s, dtype=torch.bfloat16) * 0.05)(D)),
        }
    _A_in = _A_setup()

    def _B_setup():
        B, S, D, F = 1, 2048, 512, 1792
        return {
            "x": _w((lambda *s: torch.randn(*s, dtype=torch.bfloat16) * 0.05)(B, S, D)),
            "w_gate": _w((lambda *s: torch.randn(*s, dtype=torch.bfloat16) * 0.05)(D, F)),
            "w_up": _w((lambda *s: torch.randn(*s, dtype=torch.bfloat16) * 0.05)(D, F)),
            "w_down": _w((lambda *s: torch.randn(*s, dtype=torch.bfloat16) * 0.05)(F, D)),
        }
    _B_in = _B_setup()

    return {
        "A_u": _A_in["u"],
        "A_log_A_real": _A_in["log_A_real"],
        "A_A_imag": _A_in["A_imag"],
        "A_C_real": _A_in["C_real"],
        "A_C_imag": _A_in["C_imag"],
        "A_log_dt": _A_in["log_dt"],
        "A_D_skip": _A_in["D_skip"],
        "B_x": _B_in["x"],
        "B_w_gate": _B_in["w_gate"],
        "B_w_up": _B_in["w_up"],
        "B_w_down": _B_in["w_down"],
    }

def reference(A_u, A_log_A_real, A_A_imag, A_C_real, A_C_imag, A_log_dt, A_D_skip, B_x, B_w_gate, B_w_up, B_w_down):
    def _A_ref(u, log_A_real, A_imag, C_real, C_imag, log_dt, D_skip):
        L = u.shape[1]
        dt = torch.exp(log_dt)
        A = -torch.exp(log_A_real) + 1j * A_imag
        C = (C_real + 1j * C_imag) * dt[:, None]
        pos = (lambda x, dt: x.to(dt))(torch.arange(L), log_dt.dtype)
        P = torch.exp((A * dt[:, None])[:, :, None] * pos[None, None, :])
        K = 2.0 * torch.einsum("dn,dnl->dl", C, P).real
        L2 = 2 * L
        u_t = (lambda x, *d: x.permute(*d))(u, 0, 2, 1)
        Kf = (lambda x, *a, **k: torch.fft.rfft(x.float(), *a, **k))(K, L2)
        Uf = (lambda x, *a, **k: torch.fft.rfft(x.float(), *a, **k))(u_t, L2)
        y = (lambda x, *a, **k: torch.fft.irfft(x, *a, **k).to(torch.bfloat16))(Uf * Kf[None, :, :], L2)[:, :, :L]
        y = (lambda x, *d: x.permute(*d))(y, 0, 2, 1)
        return y + u * D_skip[None, None, :]

    def _B_ref(x, w_gate, w_up, w_down):
        g = _silu(x @ w_gate)
        u = x @ w_up
        return (g * u) @ w_down

    x = A_u
    h = x + _A_ref(_layernorm(x, axis=-1), A_log_A_real, A_A_imag, A_C_real, A_C_imag, A_log_dt, A_D_skip)
    h2 = h + _B_ref(_layernorm(h, axis=-1), B_w_gate, B_w_up, B_w_down)
    return h2
```

```cpp
#include <hip/hip_runtime.h>
#include <hip/hip_bf16.h>
#include <stdint.h>
typedef unsigned short u16;
constexpr int L=2048, D=512, N=128, F=1792;
__device__ __forceinline__ float bf2f(u16 b){ return __uint_as_float(((unsigned)b)<<16); }
__device__ __forceinline__ u16 f2bf(float f){ unsigned u=__float_as_uint(f); u += 0x7FFFu + ((u>>16)&1u); return (u16)(u>>16); }
__device__ __forceinline__ float rbf(float f){ return bf2f(f2bf(f)); }

__global__ void ln_kernel(const u16* x, u16* out){
  int row = blockIdx.x, lane = threadIdx.x;
  float v[8]; float s=0;
  for(int j=0;j<8;j++){ v[j]=bf2f(x[row*D+lane*8+j]); s+=v[j]; }
  for(int o=32;o;o>>=1) s+=__shfl_xor(s,o);
  float m=s/D; float q=0;
  for(int j=0;j<8;j++){ float t=v[j]-m; q+=t*t; }
  for(int o=32;o;o>>=1) q+=__shfl_xor(q,o);
  float mb=rbf(m), vb=rbf(q/D);
  float sd=rbf(sqrtf(rbf(vb+1e-5f)));
  for(int j=0;j<8;j++) out[row*D+lane*8+j]=f2bf(rbf(v[j]-mb)/sd);
}
__global__ void kgen_kernel(const u16* logA,const u16* Aim,const u16* Cr,const u16* Ci,const u16* logdt,float* K){
  int d=blockIdx.x;
  __shared__ float ar[N],ai[N],cr[N],ci[N];
  float dt=rbf(expf(bf2f(logdt[d])));
  if(threadIdx.x<N){ int n=threadIdx.x;
    ar[n]=-rbf(expf(bf2f(logA[d*N+n])))*dt; ai[n]=bf2f(Aim[d*N+n])*dt; cr[n]=bf2f(Cr[d*N+n])*dt; ci[n]=bf2f(Ci[d*N+n])*dt; }
  __syncthreads();
  for(int l=threadIdx.x;l<L;l+=blockDim.x){
    float p=rbf((float)l); float acc=0;
    for(int n=0;n<N;n++){ float e=expf(ar[n]*p); float s,c; sincosf(ai[n]*p,&s,&c); acc+=cr[n]*(e*c)-ci[n]*(e*s); }
    K[d*L+l]=2.f*acc;
  }
}
__global__ void conv_kernel(const u16* x,const u16* u,const float* K,const u16* Dsk,u16* h){
  int d=blockIdx.x; __shared__ float ks[L], us[L];
  for(int l=threadIdx.x;l<L;l+=blockDim.x){ ks[l]=K[d*L+l]; us[l]=bf2f(u[l*D+d]); }
  __syncthreads();
  float ds=bf2f(Dsk[d]);
  for(int t=threadIdx.x;t<L;t+=blockDim.x){
    float acc=0; for(int l=0;l<=t;l++) acc+=ks[l]*us[t-l];
    float y=rbf(acc); float z=rbf(y+rbf(us[t]*ds));
    h[t*D+d]=f2bf(bf2f(x[t*D+d])+z);
  }
}
__global__ void ffn1_kernel(const u16* a,const u16* wg,const u16* wu,u16* p){
  int t=blockIdx.x; __shared__ float as[D];
  for(int k=threadIdx.x;k<D;k+=blockDim.x) as[k]=bf2f(a[t*D+k]);
  __syncthreads();
  for(int f=threadIdx.x;f<F;f+=blockDim.x){
    float g=0,u=0; for(int k=0;k<D;k++){ g+=as[k]*bf2f(wg[k*F+f]); u+=as[k]*bf2f(wu[k*F+f]); }
    g=rbf(g); u=rbf(u); float s=rbf(g/(1.f+expf(-g))); p[t*F+f]=f2bf(s*u);
  }
}
__global__ void ffn2_kernel(const u16* p,const u16* wd,const u16* h,u16* out){
  int t=blockIdx.x; __shared__ float ps[F];
  for(int k=threadIdx.x;k<F;k+=blockDim.x) ps[k]=bf2f(p[t*F+k]);
  __syncthreads();
  for(int n=threadIdx.x;n<D;n+=blockDim.x){
    float a=0; for(int k=0;k<F;k++) a+=ps[k]*bf2f(wd[k*D+n]);
    out[t*D+n]=f2bf(bf2f(h[t*D+n])+rbf(a));
  }
}
extern "C" void kernel_launch(void* const* d_in, const int* in_sizes, int n_in, void* d_out, int out_size, void* d_ws, size_t ws_size, hipStream_t stream){
  const u16* x=(const u16*)d_in[0];
  char* ws=(char*)d_ws;
  u16* u=(u16*)ws; float* K=(float*)(ws+(4<<20)); u16* h=(u16*)(ws+(12<<20)); u16* a=(u16*)(ws+(16<<20)); u16* p=(u16*)(ws+(20<<20));
  ln_kernel<<<L,64,0,stream>>>(x,u);
  kgen_kernel<<<D,256,0,stream>>>((const u16*)d_in[1],(const u16*)d_in[2],(const u16*)d_in[3],(const u16*)d_in[4],(const u16*)d_in[5],K);
  conv_kernel<<<D,256,0,stream>>>(x,u,K,(const u16*)d_in[6],h);
  ln_kernel<<<L,64,0,stream>>>(h,a);
  ffn1_kernel<<<L,256,0,stream>>>(a,(const u16*)d_in[8],(const u16*)d_in[9],p);
  ffn2_kernel<<<L,256,0,stream>>>(p,(const u16*)d_in[10],h,(u16*)d_out);
}
```

```cpp
#include <hip/hip_runtime.h>
#include <hip/hip_bf16.h>
#include <stdint.h>

#define STAMPS 0

typedef unsigned short u16;
typedef unsigned int u32;
typedef unsigned long long u64;
typedef float f32x4 __attribute__((ext_vector_type(4)));
typedef float f32x16 __attribute__((ext_vector_type(16)));
typedef _Float16 h16x8 __attribute__((ext_vector_type(8)));
typedef _Float16 h16x2 __attribute__((ext_vector_type(2)));
typedef __bf16 b16x8 __attribute__((ext_vector_type(8)));
typedef __bf16 b16x2 __attribute__((ext_vector_type(2)));
typedef short s16x4 __attribute__((ext_vector_type(4)));
typedef u32 u32x2 __attribute__((ext_vector_type(2)));
typedef u32 u32x4 __attribute__((ext_vector_type(4)));
#define LAS __attribute__((address_space(3)))
#define GAS __attribute__((address_space(1)))
#define CAS __attribute__((address_space(4)))

constexpr int SEQ = 2048, DM = 512, NMODE = 128, FF = 1792;

constexpr u32 WS_EPOCH = 0, WS_FLAG2 = 32768, WS_FLAG3 = 40960;
constexpr u32 WS_HT = 1u << 20, WS_P = 4u << 20, WS_DBG = 16u << 20, WS_EX = 32u << 20;

constexpr u32 LDS_BYTES = 163840;
constexpr u32 XY_STR = 544, XY_CH = 53 * XY_STR;
constexpr u32 L_XY = 0;
constexpr u32 UR_STR = 4640, UR_CH = 4 * UR_STR;
constexpr u32 L_UR = 0;
constexpr u32 L_XS = 37120;
constexpr u32 K_CH = 8448;
constexpr u32 L_K = 45312;
constexpr u32 VT_CH = 1344;
constexpr u32 L_VT = 62464;
constexpr u32 L_MISC = 9216;
constexpr u32 L_YP = 65536;
constexpr u32 T_STR = 2080, L_T = 57664;
constexpr u32 L_W0 = 81920;
constexpr u32 L_W2 = 65536;
constexpr u32 L_A = 0;
constexpr u32 L_ST = 131072;
constexpr u32 L_RED = 0;
constexpr int SPIN_CAP = 1 << 17;

struct Params {
  const u16 *x, *logA, *Aim, *Cr, *Ci, *logdt, *Dsk, *wg, *wu, *wd;
  u16 *out;
  char *ws;
};

__device__ __forceinline__ float bf_lo(u32 v) { return __uint_as_float(v << 16); }
__device__ __forceinline__ float bf_hi(u32 v) { return __uint_as_float(v & 0xffff0000u); }
__device__ __forceinline__ u32 pk_bf16(float a, float b) {
  b16x2 r; r[0] = (__bf16)a; r[1] = (__bf16)b; return __builtin_bit_cast(u32, r);
}
__device__ __forceinline__ float rbf(float a) { return bf_lo(pk_bf16(a, a)); }
__device__ __forceinline__ u32 pk_f16(float a, float b) {
  h16x2 r; r[0] = (_Float16)a; r[1] = (_Float16)b; return __builtin_bit_cast(u32, r);
}
template <typename T> __device__ __forceinline__ LAS T *lds(u32 off) { return (LAS T *)(size_t)off; }
__device__ __forceinline__ u32x2 tr_read(u32 addr) {
  return __builtin_bit_cast(u32x2, __builtin_amdgcn_ds_read_tr16_b64_v4i16((LAS s16x4 *)(size_t)addr));
}
__device__ __forceinline__ u32x4 cat(u32x2 a, u32x2 b) { u32x4 r; r[0] = a[0]; r[1] = a[1]; r[2] = b[0]; r[3] = b[1]; return r; }
__device__ __forceinline__ f32x16 mfma32(u32x4 a, u32x4 b, f32x16 c) {
  return __builtin_amdgcn_mfma_f32_32x32x16_bf16(__builtin_bit_cast(b16x8, a), __builtin_bit_cast(b16x8, b), c, 0, 0, 0);
}
__device__ __forceinline__ f32x4 mfma16h(u32x4 a, u32x4 b, f32x4 c) {
  return __builtin_amdgcn_mfma_f32_16x16x32_f16(__builtin_bit_cast(h16x8, a), __builtin_bit_cast(h16x8, b), c, 0, 0, 0);
}
__device__ __forceinline__ float dot2bf(u32 a, u32 b, float c) {
  return __builtin_amdgcn_fdot2_f32_bf16(__builtin_bit_cast(b16x2, a), __builtin_bit_cast(b16x2, b), c, false);
}
__device__ __forceinline__ void glds(u32 voff, const void *sbase, u32 ldst) {
  asm volatile("s_mov_b32 m0, %2\n\ts_nop 0\n\tglobal_load_lds_dwordx4 %0, %1" ::"v"(voff), "s"(sbase), "s"(ldst) : "memory");
}
__device__ __forceinline__ void glds_sc1(u32 voff, const void *sbase, u32 ldst) {
  asm volatile("s_mov_b32 m0, %2\n\ts_nop 0\n\tglobal_load_lds_dwordx4 %0, %1 sc1" ::"v"(voff), "s"(sbase), "s"(ldst) : "memory");
}
#define WAIT_VM(n) asm volatile("s_waitcnt vmcnt(" #n ")" ::: "memory")
#define WAIT_LGKM0() asm volatile("s_waitcnt lgkmcnt(0)" ::: "memory")
#define BAR() do { asm volatile("s_waitcnt lgkmcnt(0)\n\ts_barrier" ::: "memory"); } while (0)
#define SCHED() __builtin_amdgcn_sched_barrier(0)
#define PIN(x) asm volatile("" : "+v"(x))

template <int CTRL> __device__ __forceinline__ float dpp(float v) {
  return __uint_as_float(__builtin_amdgcn_update_dpp(0u, __float_as_uint(v), CTRL, 0xf, 0xf, true));
}
__device__ __forceinline__ float wave_sum(float v) {
  v += dpp<0xB1>(v); v += dpp<0x4E>(v); v += dpp<0x141>(v); v += dpp<0x140>(v);
  auto a = __builtin_amdgcn_permlane16_swap(__float_as_uint(v), __float_as_uint(v), false, false);
  v = __uint_as_float(a[0]) + __uint_as_float(a[1]);
  auto c = __builtin_amdgcn_permlane32_swap(__float_as_uint(v), __float_as_uint(v), false, false);
  return __uint_as_float(c[0]) + __uint_as_float(c[1]);
}
__device__ __forceinline__ float wave_max(float v) {
  v = fmaxf(v, dpp<0xB1>(v)); v = fmaxf(v, dpp<0x4E>(v)); v = fmaxf(v, dpp<0x141>(v)); v = fmaxf(v, dpp<0x140>(v));
  auto a = __builtin_amdgcn_permlane16_swap(__float_as_uint(v), __float_as_uint(v), false, false);
  v = fmaxf(__uint_as_float(a[0]), __uint_as_float(a[1]));
  auto c = __builtin_amdgcn_permlane32_swap(__float_as_uint(v), __float_as_uint(v), false, false);
  return fmaxf(__uint_as_float(c[0]), __uint_as_float(c[1]));
}
struct cf { float r, i; };
__device__ __forceinline__ cf cmul(cf a, cf b) { return {a.r * b.r - a.i * b.i, a.r * b.i + a.i * b.r}; }
__device__ __forceinline__ cf cexp_at(float lr, float rev, float pos) {
  float e = __builtin_amdgcn_exp2f(lr * pos), f = __builtin_amdgcn_fractf(rev * pos);
  return {e * __builtin_amdgcn_cosf(f), e * __builtin_amdgcn_sinf(f)};
}

#define STAMP(i)

__global__ void __launch_bounds__(512, 2) fused(Params p) {
  extern __shared__ __attribute__((aligned(1024))) char smem_[];
  const u32 tid = threadIdx.x, lane = tid & 63;
  const u32 w = __builtin_amdgcn_readfirstlane(tid >> 6);
  const u32 b = blockIdx.x, xcd = b & 7, jj = b >> 3;
  char *const ws = p.ws;
  STAMP(0);
  const bool g1 = jj < 28;
  const u32 panel = 2 * xcd + (jj >= 14 ? 1 : 0), ft = jj >= 28 ? jj - 28 : (jj >= 14 ? jj - 14 : jj);
  u32 tag = 0;

  u32x4 W[32];
  const u32 w1_voff = ((lane >> 4) * FF + ft * 128) * 2 + ((((lane & 15) >> 1) ^ ((lane >> 4) | ((w & 1) << 2))) * 32) + (lane & 1) * 16;
  const char *const w1_base = (const char *)((w & 1) ? p.wu : p.wg) + (size_t)((w >> 1) * 4) * (FF * 2);
  auto w1_one = [&](int ks, u32 dst) {
    glds(w1_voff, w1_base + (size_t)ks * 16 * (FF * 2), dst + w * 1024);
  };
  const u32 g4 = lane >> 4, q4 = (lane >> 2) & 3, p4 = lane & 3;
  const u32 w1_rd = (g4 >> 1) * 2048 + (g4 & 1) * 1024 + q4 * 256 + ((w ^ (q4 | ((g4 & 1) << 2))) * 32) + p4 * 8;

  const u32 cp = xcd * 32 + jj, d0 = 2 * cp;
  const u32 ch = w >> 2, job = w & 3;
  u32x4 xr; u32 pl, pa, pr, pi;
  {
    const u32 o1 = (8 * b + w) * (DM * 2) + lane * 16;
    asm volatile("global_load_dwordx4 %0, %1, %2" : "=v"(xr) : "v"(o1), "s"(p.x));
    const u32 o2 = ((d0 + ch) * NMODE + 2 * lane) * 2;
    asm volatile("global_load_dword %0, %1, %2" : "=v"(pl) : "v"(o2), "s"(p.logA));
    asm volatile("global_load_dword %0, %1, %2" : "=v"(pa) : "v"(o2), "s"(p.Aim));
    asm volatile("global_load_dword %0, %1, %2" : "=v"(pr) : "v"(o2), "s"(p.Cr));
    asm volatile("global_load_dword %0, %1, %2" : "=v"(pi) : "v"(o2), "s"(p.Ci));
  }
  const u32 ldt2 = *(const CAS u32 *)(p.logdt + d0), dsk2 = *(const CAS u32 *)(p.Dsk + d0);
  asm volatile("s_load_dword %0, %1, 0x0\n\ts_waitcnt lgkmcnt(0)" : "=s"(tag) : "s"(ws) : "memory");
  tag += 1u;
  u32 vidx[8];
  {
    const u32 l0 = 8 * (tid & 255);
#pragma unroll
    for (int e = 0; e < 8; e++) {
      const u32 l = l0 + e, bits = pk_bf16((float)l, 0.f) & 0xffffu;
      vidx[e] = L_VT + (tid >> 8) * VT_CH + 2 * (l < 256 ? l : bits - (0x4380u - 256u));
    }
#pragma unroll
    for (int e = 0; e < 8; e++) asm volatile("" : "+v"(vidx[e]));
  }
  asm volatile("s_waitcnt vmcnt(4)" : "+v"(xr));
  {
    float v[8];
#pragma unroll
    for (int i = 0; i < 4; i++) { v[2 * i] = bf_lo(xr[i]); v[2 * i + 1] = bf_hi(xr[i]); }
    const float m = wave_sum(((v[0] + v[1]) + (v[2] + v[3])) + ((v[4] + v[5]) + (v[6] + v[7]))) * (1.f / DM);
    float q0 = 0, q1 = 0;
#pragma unroll
    for (int i = 0; i < 4; i++) { const float t0 = v[i] - m, t1 = v[i + 4] - m; q0 += t0 * t0; q1 += t1 * t1; }
    const float var = rbf(wave_sum(q0 + q1) * (1.f / DM));
    const float rs = __builtin_amdgcn_rcpf(rbf(__builtin_amdgcn_sqrtf(rbf(var + 1e-5f)))), mb = rbf(m);
    u32 uu[4];
#pragma unroll
    for (int i = 0; i < 4; i++) {
      const u32 a = pk_bf16(v[2 * i] - mb, v[2 * i + 1] - mb);
      const u32 u = pk_bf16(bf_lo(a) * rs, bf_hi(a) * rs);
      uu[i] = pk_f16(bf_lo(u), bf_hi(u));
    }
    const u32 ta = L_T + w * T_STR + lane * 32;
    u32x4 o; o[0] = uu[0]; o[1] = xr[0]; o[2] = uu[1]; o[3] = xr[1];
    *lds<u32x4>(ta) = o;
    o[0] = uu[2]; o[1] = xr[2]; o[2] = uu[3]; o[3] = xr[3];
    *lds<u32x4>(ta + 16) = o;
  }
  BAR();
  {
    __amdgpu_buffer_rsrc_t rs = __builtin_amdgcn_make_buffer_rsrc(ws + WS_EX, 0, 256 * 256 * 128, 0x00020000);
    const u32 ra = L_T + (4 * (p4 & 1) + 3 - q4) * T_STR + (32 * w + 2 * g4 + (p4 >> 1)) * 8;
    const u32 so = ((32 * w + (lane >> 3)) * 256 + b) * 128 + (lane & 7) * 16;
    u32x2 r[4];
#pragma unroll
    for (int i = 0; i < 4; i++) r[i] = tr_read(ra + i * 64);
#pragma unroll
    for (int i = 0; i < 4; i++) {
      u32x4 o; o[0] = r[i][0]; o[1] = tag; o[2] = r[i][1]; o[3] = tag;
      __builtin_amdgcn_raw_buffer_store_b128(o, rs, so + i * (8 * 256 * 128), 0, 16);
    }
  }
  STAMP(1);
  asm volatile("s_waitcnt vmcnt(4)" : "+v"(pl), "+v"(pa), "+v"(pr), "+v"(pi));
  const float dt = rbf(__expf(ch ? bf_hi(ldt2) : bf_lo(ldt2)));
  {
    const float LOG2E = 1.4426950408889634f, INV2PI = 0.15915494309189535f;
    float lr[2], rv[2];
    lr[0] = -rbf(__expf(bf_lo(pl))) * dt * LOG2E; lr[1] = -rbf(__expf(bf_hi(pl))) * dt * LOG2E;
    rv[0] = bf_lo(pa) * dt * INV2PI; rv[1] = bf_hi(pa) * dt * INV2PI;
    const u32 wb = L_XY + ch * XY_CH + lane * 8;
    auto put = [&](int row, cf a, cf c) {
      u32x2 v; v[0] = pk_f16(a.r, a.i); v[1] = pk_f16(c.r, c.i);
      *lds<u32x2>(wb + row * XY_STR) = v;
    };
    auto G = [&](int i) { w1_one(i, L_W0 + i * 8192); };
    if (job < 2) {
      G(0);
      cf s0 = cexp_at(lr[0], -rv[0], 8.f), s1 = cexp_at(lr[1], -rv[1], 8.f);
      const float p0 = job ? 128.f : 0.f;
      cf z0 = cexp_at(lr[0], -rv[0], p0), z1 = cexp_at(lr[1], -rv[1], p0);
      const u32 wb2 = wb + job * 16 * XY_STR;
#pragma unroll
      for (int i = 0; i < 16; i++) {
        u32x2 v; v[0] = pk_f16(z0.r, z0.i); v[1] = pk_f16(z1.r, z1.i);
        *lds<u32x2>(wb2 + i * XY_STR) = v;
        if (i < 15) { z0 = cmul(z0, s0); z1 = cmul(z1, s1); }
        if (i < 6) G(1 + i); else if (i < 12 && (i & 1)) G(7 + (i - 7) / 2);
        SCHED();
      }
    } else {
      G(0);
      cf c0 = {bf_lo(pr), bf_lo(pi)}, c1 = {bf_hi(pr), bf_hi(pi)};
      const float mx = wave_max(fmaxf(fmaxf(fabsf(c0.r), fabsf(c0.i)), fmaxf(fabsf(c1.r), fabsf(c1.i))));
      const int ex = __builtin_amdgcn_frexp_expf(mx);
      const float sc = __builtin_amdgcn_ldexpf(1.f, -ex);
      c0.r *= sc; c0.i *= sc; c1.r *= sc; c1.i *= sc;
      G(1);
      if (job == 2) {
        if (lane == 0) *lds<float>(L_MISC + ch * 4) = __builtin_amdgcn_ldexpf(2.f * dt, ex);
        cf s0 = cexp_at(lr[0], rv[0], 1.f), s1 = cexp_at(lr[1], rv[1], 1.f);
        cf z0 = c0, z1 = c1;
#pragma unroll
        for (int i = 0; i < 8; i++) { put(32 + i, z0, z1); if (i < 7) { z0 = cmul(z0, s0); z1 = cmul(z1, s1); } G(2 + i); SCHED(); }
        s0 = cmul(s0, s0); s1 = cmul(s1, s1);
        z0 = cmul(c0, cexp_at(lr[0], rv[0], 256.f)); z1 = cmul(c1, cexp_at(lr[1], rv[1], 256.f));
#pragma unroll
        for (int i = 0; i < 4; i++) { put(40 + i, z0, z1); if (i < 3) { z0 = cmul(z0, s0); z1 = cmul(z1, s1); } }
      } else {
        cf f0 = cexp_at(lr[0], rv[0], 4.f), f1 = cexp_at(lr[1], rv[1], 4.f);
        G(2); SCHED();
        cf s0 = cexp_at(lr[0], rv[0], 256.f), s1 = cexp_at(lr[1], rv[1], 256.f);
        G(3); SCHED();
        cf z0 = cmul(c0, cexp_at(lr[0], rv[0], 512.f)), z1 = cmul(c1, cexp_at(lr[1], rv[1], 512.f));
        G(4); SCHED();
        put(44, z0, z1); put(45, cmul(z0, f0), cmul(z1, f1));
        z0 = cmul(z0, s0); z1 = cmul(z1, s1);
        put(46, z0, z1); put(47, cmul(z0, f0), cmul(z1, f1));
#pragma unroll
        for (int i = 0; i < 5; i++) { z0 = cmul(z0, s0); z1 = cmul(z1, s1); put(48 + i, z0, z1); G(5 + i); SCHED(); }
      }
    }
  }
  BAR();
  STAMP(2);
  {
    const u32 rb = job >> 1, cb = job & 1;
    const u32 xa = L_XY + ch * XY_CH + (32 + 16 * rb + (lane & 15)) * XY_STR + g4 * 16;
    const u32 ya = L_XY + ch * XY_CH + (16 * cb + (lane & 15)) * XY_STR + g4 * 16;
    u32x4 fa[8], fb[8];
#pragma unroll
    for (int ks = 0; ks < 8; ks++) { fa[ks] = *lds<u32x4>(xa + ks * 64); fb[ks] = *lds<u32x4>(ya + ks * 64); }
    SCHED();
    const u32 j = 16 * cb + (lane & 15);
    u32 i0, is, i2;
    if (rb == 0) {
      i0 = g4 < 2 ? 4 * g4 + 8 * j : (g4 == 2 ? 256 + 4 * j : 384 + 2 * j); is = 1; i2 = g4 == 3 ? 64 : 2;
    } else {
      i0 = g4 == 0 ? 512 + j : 640 + j; is = g4 == 0 ? 32 : 0; i2 = 2 * is;
    }
    f32x4 a0 = {0, 0, 0, 0}, a1 = {0, 0, 0, 0};
#pragma unroll
    for (int ks = 0; ks < 8; ks += 2) { a0 = mfma16h(fa[ks], fb[ks], a0); a1 = mfma16h(fa[ks + 1], fb[ks + 1], a1); }
    a0 += a1;
    const u32 vb = L_VT + ch * VT_CH;
    if (rb == 0 || g4 == 0) {
#pragma unroll
      for (int r = 0; r < 4; r++) *lds<_Float16>(vb + 2 * (i0 + (r & 1) * is + (r >> 1) * i2)) = (_Float16)a0[r];
    } else if (g4 == 1) {
      *lds<_Float16>(vb + 2 * i0) = (_Float16)a0[0];
    }
  }
  BAR();
  STAMP(3);
  u32x4 ex[4];
  const char *const exb = ws + WS_EX + (size_t)cp * 32768;
  const u32 exo = tid * 16;
#define EX_LOAD(i) asm volatile("global_load_dwordx4 %0, %1, %2 sc1" : "=v"(ex[i]) : "v"(exo), "s"(exb + (i) * 8192))
  EX_LOAD(0); EX_LOAD(1); EX_LOAD(2); EX_LOAD(3);
  {
    u32x4 kv;
    _Float16 t[8];
#pragma unroll
    for (int e = 0; e < 8; e++) t[e] = *lds<_Float16>(vidx[e]);
#pragma unroll
    for (int e = 0; e < 4; e++) { h16x2 t2; t2[0] = t[2 * e]; t2[1] = t[2 * e + 1]; kv[e] = __builtin_bit_cast(u32, t2); }
    const u32 kb = L_K + (tid >> 8) * K_CH + (tid & 255) * 16;
    u32x2 lo, hi; lo[0] = kv[0]; lo[1] = kv[1]; hi[0] = kv[2]; hi[1] = kv[3];
    *lds<u32x2>(kb + 120) = lo; *lds<u32x2>(kb + 128) = hi;
    *lds<u32x4>(kb + 4224 + 112) = kv;
    const u32x2 z2 = {0, 0};
    u32 za;
    if (tid < 64) {
      const u32 c = tid >> 5, i = tid & 31;
      za = L_K + c * K_CH + (i < 15 ? i * 8 : 4224 + (i < 29 ? i - 15 : 0) * 8);
    } else {
      const u32 i = tid < 448 ? tid - 64 : 0, c = i / 48;
      za = L_UR + c * UR_STR + 4096 + (i - c * 48) * 8;
    }
    *lds<u32x2>(za) = z2;
  }
  STAMP(4);
  asm volatile("s_waitcnt vmcnt(0)" : "+v"(ex[0]), "+v"(ex[1]), "+v"(ex[2]), "+v"(ex[3]));
#pragma unroll 1
  for (int spin = 0; spin < SPIN_CAP; spin++) {
    bool ok = true;
#pragma unroll
    for (int i = 0; i < 4; i++) ok = ok && ex[i][1] == tag && ex[i][3] == tag;
    if (__all(ok)) break;
    __builtin_amdgcn_s_sleep(1);
    asm volatile("global_load_dwordx4 %0, %4, %5 sc1\n\tglobal_load_dwordx4 %1, %4, %6 sc1\n\t"
                 "global_load_dwordx4 %2, %4, %7 sc1\n\tglobal_load_dwordx4 %3, %4, %8 sc1\n\ts_waitcnt vmcnt(0)"
                 : "=&v"(ex[0]), "=&v"(ex[1]), "=&v"(ex[2]), "=&v"(ex[3])
                 : "v"(exo), "s"(exb), "s"(exb + 8192), "s"(exb + 16384), "s"(exb + 24576) : "memory");
  }
  STAMP(5);
  {
    const u32 e = tid & 3, t0 = 8 * (tid >> 3) + (tid & 4);
    const u32 ab = (e < 2 ? L_UR + 3 * UR_STR + e * UR_CH : L_XS + (e - 2) * 4096) + (2044 - 1536 - t0) * 2;
#pragma unroll
    for (int i = 0; i < 4; i++) { u32x2 v; v[0] = ex[i][0]; v[1] = ex[i][2]; *lds<u32x2>(ab + (3 - i) * 1024) = v; }
  }
  BAR();
  {
    const u32 ub = L_UR + (tid >> 8) * UR_CH + (tid & 255) * 16;
    const u32x4 a = *lds<u32x4>(ub + 3 * UR_STR);
    const u32x2 c = *lds<u32x2>(ub + 3 * UR_STR + 16);
    SCHED();
    u32 wr = L_W0 + w1_rd;
    asm volatile("" : "+v"(wr));
#pragma unroll
    for (int ks = 0; ks < 8; ks++) W[ks] = cat(tr_read(wr + ks * 8192), tr_read(wr + ks * 8192 + 4096));
    SCHED();
    u32x4 o;
    o[0] = __builtin_amdgcn_alignbit(a[1], a[0], 16); o[1] = __builtin_amdgcn_alignbit(a[2], a[1], 16);
    o[2] = __builtin_amdgcn_alignbit(a[3], a[2], 16); o[3] = __builtin_amdgcn_alignbit(c[0], a[3], 16);
    *lds<u32x4>(ub + 2 * UR_STR) = o;
    u32x4 o1; o1[0] = a[1]; o1[1] = a[2]; o1[2] = a[3]; o1[3] = c[0];
    *lds<u32x4>(ub + UR_STR) = o1;
    u32x4 o0; o0[0] = o[1]; o0[1] = o[2]; o0[2] = o[3]; o0[3] = __builtin_amdgcn_alignbit(c[1], c[0], 16);
    *lds<u32x4>(ub) = o0;
    wr += 65536;
#pragma unroll
    for (int ks = 8; ks < 10; ks++) W[ks] = cat(tr_read(wr + (ks - 8) * 8192), tr_read(wr + (ks - 8) * 8192 + 4096));
  }
  BAR();
  STAMP(6);
  const u32 pg = w & 1, cg = (w >> 1) & 1;
  f32x4 ya[4];
#pragma unroll
  for (int i = 0; i < 4; i++) ya[i] = f32x4{0, 0, 0, 0};
  {
    const u32 c16 = lane & 15;
    const u32 ka = L_K + ch * K_CH + (c16 & 1) * 4224 + (c16 >> 1) * 16 + g4 * 16 + cg * 4 * 64;
    const u32 ua = L_UR + ch * UR_CH + (c16 & 3) * UR_STR + (1984 - 1792 - 64 * (c16 >> 2) + 8 * g4) * 2 + cg * 4 * 64;
    u32x4 kf[2][8], uf[2][8];
    auto rd = [&](int s, int i) { uf[s & 1][7 - i] = *lds<u32x4>(ua + i * 512 + s * 64); kf[s & 1][i] = *lds<u32x4>(ka + i * 512 + s * 64); };
#pragma unroll
    for (int i = 0; i < 8; i++) rd(0, i);
    if (pg == 0) {
#pragma unroll
      for (int s = 0; s < 4; s++) {
#pragma unroll
        for (int i = 0; i < 8; i++) {
          if (s < 3) rd(s + 1, i);
          if ((i % 3) == 1 && s * 3 + i / 3 < 10) w1_one(10 + s * 3 + i / 3, L_W0 + (s * 3 + i / 3) * 8192);
          ya[3] = mfma16h(uf[s & 1][7 - i], kf[s & 1][i], ya[3]);
          if (i <= 4) ya[2] = mfma16h(uf[s & 1][4 - i], kf[s & 1][i], ya[2]);
          if (i <= 3) ya[1] = mfma16h(uf[s & 1][3 - i], kf[s & 1][i], ya[1]);
          if (i <= 0) ya[0] = mfma16h(uf[s & 1][0 - i], kf[s & 1][i], ya[0]);
          SCHED();
        }
      }
    } else {
#pragma unroll
      for (int s = 0; s < 4; s++) {
#pragma unroll
        for (int i = 0; i < 8; i++) {
          if (s < 3) rd(s + 1, i);
          if ((i % 3) == 1 && s * 3 + i / 3 < 10) w1_one(10 + s * 3 + i / 3, L_W0 + (s * 3 + i / 3) * 8192);
          if (i <= 6) ya[3] = mfma16h(uf[s & 1][6 - i], kf[s & 1][i], ya[3]);
          if (i <= 5) ya[2] = mfma16h(uf[s & 1][5 - i], kf[s & 1][i], ya[2]);
          if (i <= 2) ya[1] = mfma16h(uf[s & 1][2 - i], kf[s & 1][i], ya[1]);
          if (i <= 1) ya[0] = mfma16h(uf[s & 1][1 - i], kf[s & 1][i], ya[0]);
          SCHED();
        }
      }
    }
  }
  {
    const u32 yb = L_YP + w * 2048 + lane * 16;
    *lds<f32x4>(yb) = cg ? ya[0] : ya[2];
    *lds<f32x4>(yb + 1024) = cg ? ya[1] : ya[3];
  }
  WAIT_VM(0);
  BAR();
  STAMP(7);
  {
    const u32 yb = L_YP + (w ^ 2) * 2048 + lane * 16;
    const float ksc = *lds<float>(L_MISC + ch * 4), dsk = ch ? bf_hi(dsk2) : bf_lo(dsk2);
    __amdgpu_buffer_rsrc_t rs = __builtin_amdgcn_make_buffer_rsrc(ws + WS_HT, 0, SEQ * DM * 2, 0x00020000);
    f32x4 o[2]; u32x2 uu[2], xx[2]; u32 t0[2];
#pragma unroll
    for (int i = 0; i < 2; i++) {
      o[i] = *lds<f32x4>(yb + i * 1024);
      t0[i] = 256 * (4 * cg + (i ? 3 - pg : pg)) + 4 * lane;
      uu[i] = *lds<u32x2>(L_UR + ch * UR_CH + 3 * UR_STR + (2044 - t0[i]) * 2);
      xx[i] = *lds<u32x2>(L_XS + ch * 4096 + (2044 - t0[i]) * 2);
    }
#pragma unroll
    for (int i = 0; i < 2; i++) {
      const f32x4 own = cg ? (i ? ya[3] : ya[2]) : (i ? ya[1] : ya[0]);
      const u32 uu0 = uu[i][0], uu1 = uu[i][1];
      const h16x2 ua = __builtin_bit_cast(h16x2, uu1), ub = __builtin_bit_cast(h16x2, uu0);
      const float uv[4] = {(float)ua[1], (float)ua[0], (float)ub[1], (float)ub[0]};
      const float xv[4] = {bf_hi(xx[i][1]), bf_lo(xx[i][1]), bf_hi(xx[i][0]), bf_lo(xx[i][0])};
      float hv[4];
#pragma unroll
      for (int r = 0; r < 4; r += 2) {
        const u32 y2 = pk_bf16((o[i][r] + own[r]) * ksc, (o[i][r + 1] + own[r + 1]) * ksc);
        const u32 d2 = pk_bf16(uv[r] * dsk, uv[r + 1] * dsk);
        const u32 z2 = pk_bf16(bf_lo(y2) + bf_lo(d2), bf_hi(y2) + bf_hi(d2));
        hv[r] = xv[r] + bf_lo(z2); hv[r + 1] = xv[r + 1] + bf_hi(z2);
      }
      u32x2 hv2; hv2[0] = pk_bf16(hv[0], hv[1]); hv2[1] = pk_bf16(hv[2], hv[3]);
      const u32 off = ((t0[i] >> 5) * DM + d0 + ch) * 64 + (t0[i] & 31) * 2;
      __builtin_amdgcn_raw_buffer_store_b64(hv2, rs, off, 0, 16);
    }
  }
  SCHED();
  {
    u32 wr = L_W0 + w1_rd;
    asm volatile("" : "+v"(wr));
#pragma unroll
    for (int ks = 0; ks < 8; ks++) W[10 + ks] = cat(tr_read(wr + ks * 8192), tr_read(wr + ks * 8192 + 4096));
    wr += 65536;
#pragma unroll
    for (int ks = 8; ks < 10; ks++) W[10 + ks] = cat(tr_read(wr + (ks - 8) * 8192), tr_read(wr + (ks - 8) * 8192 + 4096));
  }
  if (b == 0 && tid == 0)
    __hip_atomic_store((GAS u32 *)(ws + WS_EPOCH), tag, __ATOMIC_RELAXED, __HIP_MEMORY_SCOPE_AGENT);
  BAR();
  f32x16 cs;
#pragma unroll
  for (int i = 0; i < 16; i++) cs[i] = 0;
  const u32x4 ones = {0x3f803f80u, 0x3f803f80u, 0x3f803f80u, 0x3f803f80u};
#pragma unroll
  for (int ks = 0; ks < 12; ks++) { w1_one(20 + ks, L_W2 + ks * 8192); cs = mfma32(W[ks], ones, cs); SCHED(); }
  WAIT_VM(12);
  BAR();
  if (tid == 0) __hip_atomic_store((GAS u32 *)(ws + WS_FLAG2) + b, tag, __ATOMIC_RELAXED, __HIP_MEMORY_SCOPE_AGENT);
  STAMP(8);
#pragma unroll
  for (int ks = 12; ks < 20; ks++) cs = mfma32(W[ks], ones, cs);
  SCHED();
  WAIT_VM(0);
  BAR();
  const u32 h32 = lane >> 5, t32 = lane & 31;
  auto poll2 = [&]() {
    const u32 fo = WS_FLAG2 + lane * 16;
#pragma unroll 1
    for (int spin = 0; spin < SPIN_CAP; spin++) {
      u32x4 f;
      asm volatile("global_load_dwordx4 %0, %1, %2 sc1\n\ts_waitcnt vmcnt(0)" : "=&v"(f) : "v"(fo), "s"(ws) : "memory");
      if (__all(f[0] == tag && f[1] == tag && f[2] == tag && f[3] == tag)) break;
      __builtin_amdgcn_s_sleep(1);
    }
  };

  if (g1) {
    {
      u32 wr = L_W2 + w1_rd;
      asm volatile("" : "+v"(wr));
#pragma unroll
      for (int ks = 0; ks < 8; ks++) W[20 + ks] = cat(tr_read(wr + ks * 8192), tr_read(wr + ks * 8192 + 4096));
      wr += 65536;
#pragma unroll
      for (int ks = 8; ks < 12; ks++) W[20 + ks] = cat(tr_read(wr + (ks - 8) * 8192), tr_read(wr + (ks - 8) * 8192 + 4096));
    }
    STAMP(9);
    if (w == 0) poll2();
    BAR();
    STAMP(10);
    {
      const char *base = ws + WS_HT + (size_t)panel * 131072 + w * 1024;
#pragma unroll
      for (int i = 0; i < 16; i++) glds_sc1(lane * 16, base + i * 8192, L_A + w * 1024 + i * 8192);
    }
#pragma unroll
    for (int ks = 20; ks < 32; ks++) cs = mfma32(W[ks], ones, cs);
    SCHED();
    u32 ab = L_A + (4 * (g4 >> 1) + q4) * 64 + (g4 & 1) * 32 + p4 * 8;
    __amdgpu_buffer_rsrc_t prs = __builtin_amdgcn_make_buffer_rsrc(ws + WS_P, 0, SEQ * FF * 2, 0x00020000);
    u32 p_off = (panel * 4 * 112 + ft * 8 + w) * 1024 + lane * 16;
    u32 st_rd = L_ST + lane * 4;
    f32x16 acc, prev;
    u32x4 F[4];
    auto frag = [&](u32 a) { return cat(tr_read(a), tr_read(a + 512)); };
    float sp[8], e_m, e_ne, e_s2, e_g, e_u, e_e, o[8], s_s, s_q;
    u32x4 SF[4];
    u32 pko[4];
    auto epi_step = [&](int ks) {
      if (ks == 0) {
#pragma unroll
        for (int i = 0; i < 8; i++) sp[i] = *lds<float>(st_rd + i * 256);
      } else if (ks == 2) {
        sp[0] += sp[1]; PIN(sp[0]); sp[2] += sp[3]; PIN(sp[2]); sp[4] += sp[5]; PIN(sp[4]); sp[6] += sp[7]; PIN(sp[6]);
        sp[0] += sp[2]; PIN(sp[0]); sp[4] += sp[6]; sp[0] += sp[4];
      } else if (ks == 3) {
        const auto sw = __builtin_amdgcn_permlane32_swap(__float_as_uint(sp[0]), __float_as_uint(sp[0]), false, false);
        sp[1] = __uint_as_float(sw[0]) * (1.f / DM);
        sp[2] = fmaxf(__uint_as_float(sw[1]) * (1.f / DM) - sp[1] * sp[1], 0.f);
      } else if (ks == 4) {
        sp[2] = __builtin_amdgcn_sqrtf(rbf(rbf(sp[2]) + 1e-5f));
      } else if (ks == 5) {
        const u32 r = pk_bf16(sp[1], sp[2]);
        e_m = bf_lo(r); const float sd = bf_hi(r);
        e_ne = -1.4426950408889634f * __builtin_amdgcn_rcpf(sd); e_s2 = sd * sd;
      } else if (ks >= 6 && ks < 22) {
        const int i = (ks - 6) >> 1;
        if (!(ks & 1)) {
          e_g = prev[i] - e_m * cs[i]; PIN(e_g);
          e_e = __builtin_amdgcn_exp2f(e_g * e_ne);
          e_u = (prev[i + 8] - e_m * cs[i + 8]) * e_g;
          PIN(e_e); PIN(e_u);
        } else {
          o[i] = e_u * __builtin_amdgcn_rcpf(e_e * e_s2 + e_s2);
          PIN(o[i]);
          if (i & 1) { pko[i >> 1] = pk_bf16(o[i - 1], o[i]); PIN(pko[i >> 1]); }
        }
      } else if (ks == 22) {
        u32x4 v; v[0] = pko[0]; v[1] = pko[1]; v[2] = pko[2]; v[3] = pko[3];
        __builtin_amdgcn_raw_buffer_store_b128(v, prs, p_off, 0, 16);
        p_off += 112 * 1024;
      }
    };
    auto stat_step = [&](int ks) {
      if (ks >= 21 && ks < 25) SF[ks - 21] = frag(ab + w * 4096 + (ks - 21) * 1024);
      if (ks == 23) { s_s = 0; s_q = 0; }
      if (ks >= 23 && ks < 31) {
        const int i = (ks - 23) >> 1, e = ((ks - 23) & 1) * 2;
        s_s = dot2bf(SF[i][e], 0x3f803f80u, s_s); s_q = dot2bf(SF[i][e], SF[i][e], s_q);
        s_s = dot2bf(SF[i][e + 1], 0x3f803f80u, s_s); s_q = dot2bf(SF[i][e + 1], SF[i][e + 1], s_q);
        PIN(s_s); PIN(s_q);
      }
      if (ks == 31) {
        const auto sw = __builtin_amdgcn_permlane32_swap(__float_as_uint(s_s), __float_as_uint(s_q), false, false);
        st_rd ^= 2048;
        *lds<float>(st_rd + w * 256) = __uint_as_float(sw[0]) + __uint_as_float(sw[1]);
      }
    };
    WAIT_VM(15);
    BAR();
#pragma unroll
    for (int i = 0; i < 3; i++) F[i] = frag(ab + i * 1024);
#pragma unroll
    for (int i = 0; i < 16; i++) acc[i] = 0;
#pragma unroll
    for (int ks = 0; ks < 32; ks++) {
      if (ks == 5) { WAIT_VM(14); BAR(); }
      if (ks == 13) { WAIT_VM(13); BAR(); }
      if (ks == 21) { WAIT_VM(12); BAR(); }
      if (ks == 29) { WAIT_VM(0); BAR(); }
      F[(ks + 3) & 3] = frag(ab + (ks + 3) * 1024);
      acc = mfma32(W[ks], F[ks & 3], acc);
      stat_step(ks);
      SCHED();
    }
    STAMP(11);
#pragma unroll 1
    for (u32 tg = 1; tg < 4; tg++) {
      BAR();
      prev = acc;
      ab += 32768;
#pragma unroll
      for (int i = 0; i < 16; i++) acc[i] = 0;
#pragma unroll
      for (int ks = 0; ks < 32; ks++) {
        F[(ks + 3) & 3] = frag(ab + (ks + 3) * 1024);
        acc = mfma32(W[ks], F[ks & 3], acc);
        epi_step(ks);
        stat_step(ks);
        SCHED();
      }
    }
    BAR();
    STAMP(12);
    prev = acc;
#pragma unroll
    for (int ks = 0; ks < 23; ks++) epi_step(ks);
    STAMP(13);
  }

  {
    const u32 nt = jj & 7, tt = jj >> 3, pn2 = 2 * xcd + (tt >> 1), tg0 = pn2 * 4 + (tt & 1) * 2;
    const u32 nh = w & 1, kq = w >> 1;
    const u32 r8 = lane >> 3;
    const u32 d_voff = r8 * (DM * 2) + (((lane & 7) ^ (((r8 >> 1) & 1) << 2)) * 16);
    auto wd_issue = [&](int ks0, int n) {
      const char *base = (const char *)p.wd + (size_t)(kq * 448 + ks0 * 16 + nh * 8) * (DM * 2) + nt * 128;
#pragma unroll
      for (int i = 0; i < n; i++) glds(d_voff, base + (size_t)i * 16 * (DM * 2), kq * 32768 + i * 2048 + nh * 1024);
    };
    const u32 d_rd = kq * 32768 + (((4 * (g4 >> 1) + q4) * 128 + nh * 64 + (g4 & 1) * 32 + p4 * 8) ^ (((q4 >> 1) & 1) * 64));
    wd_issue(0, 16);
    if (g1) {
      WAIT_VM(16);
      BAR();
      if (tid == 0) __hip_atomic_store((GAS u32 *)(ws + WS_FLAG3) + panel * 32 + ft, tag, __ATOMIC_RELAXED, __HIP_MEMORY_SCOPE_AGENT);
      STAMP(14);
    }
    WAIT_VM(0);
    BAR();
#pragma unroll
    for (int ks = 0; ks < 16; ks++) W[ks] = cat(tr_read(d_rd + ks * 2048), tr_read(d_rd + ks * 2048 + 1024));
    BAR();
    wd_issue(16, 12);
    WAIT_VM(0);
    BAR();
#pragma unroll
    for (int ks = 0; ks < 12; ks++) W[16 + ks] = cat(tr_read(d_rd + ks * 2048), tr_read(d_rd + ks * 2048 + 1024));
    STAMP(15);
    if (w == 0) {
      const u32 fo = WS_FLAG3 + pn2 * 128 + (lane < 14 ? lane : 0) * 4;
#pragma unroll 1
      for (int spin = 0; spin < SPIN_CAP; spin++) {
        u32 f;
        asm volatile("global_load_dword %0, %1, %2 sc1\n\ts_waitcnt vmcnt(0)" : "=&v"(f) : "v"(fo), "s"(ws) : "memory");
        if (__all(f == tag)) break;
        __builtin_amdgcn_s_sleep(1);
      }
      if (!g1) poll2();
    }
    BAR();
    STAMP(16);
    const char *pbase = ws + WS_P + ((size_t)(tg0 + nh) * 112 + kq * 28) * 1024;
    const u32 lv = lane * 16;
    auto p_issue = [&](int s) {
#pragma unroll
      for (int i = 0; i < 2; i++) glds_sc1(lv, pbase + (s * 2 + i) * 1024, (s & 7) * 16384 + kq * 4096 + i * 2048 + nh * 1024);
    };
    p_issue(0); p_issue(1);
    const u32 own_t = (tg0 + (kq >> 1)) * 32 + t32, own_n = nt * 64 + nh * 32 + (kq & 1) * 16;
    u32 res[8];
    {
      const u32 ro = WS_HT + ((own_t >> 5) * DM + own_n + 4 * h32) * 64 + t32 * 2;
#define RES_LD(i, o) asm volatile("global_load_ushort %0, %1, %2 offset:" #o " sc1" : "=v"(res[i]) : "v"(ro), "s"(ws));
      RES_LD(0, 0) RES_LD(1, 64) RES_LD(2, 128) RES_LD(3, 192) RES_LD(4, 512) RES_LD(5, 576) RES_LD(6, 640) RES_LD(7, 704)
#undef RES_LD
    }
#pragma unroll
    for (int s = 2; s < 8; s++) p_issue(s);
    f32x16 a0, a1;
#pragma unroll
    for (int i = 0; i < 16; i++) { a0[i] = 0; a1[i] = 0; }
    u32 p_rd = kq * 4096 + lv, p_rd2 = p_rd + 65536;
    asm volatile("" : "+v"(p_rd), "+v"(p_rd2));
    u32x4 PF[2][4];
    auto p_read = [&](int s, int i) { PF[s & 1][i] = *lds<u32x4>(((s & 4) ? p_rd2 : p_rd) + (s & 3) * 16384 + i * 1024); };
#pragma unroll
    for (int s = 0; s < 14; s++) {
      if (s == 0) WAIT_VM(20); else if (s <= 7) WAIT_VM(10); else if (s == 8) WAIT_VM(8); else if (s == 9) WAIT_VM(6);
      else if (s == 10) WAIT_VM(4); else if (s == 11) WAIT_VM(2); else if (s == 12) WAIT_VM(0);
      if (s == 0) BAR(); else if (s <= 12) asm volatile("s_barrier" ::: "memory");
      if (s == 0) {
#pragma unroll
        for (int i = 0; i < 4; i++) p_read(0, i);
      }
      if (s >= 1 && s + 7 < 14) p_issue(s + 7);
      SCHED();
#pragma unroll
      for (int i = 0; i < 4; i++) {
        if (s < 13) p_read(s + 1, i);
        if (i & 1) a1 = mfma32(W[s * 2 + (i >> 1)], PF[s & 1][i], a1);
        else a0 = mfma32(W[s * 2 + (i >> 1)], PF[s & 1][i], a0);
        SCHED();
      }
    }
    asm volatile("" : "+v"(res[0]), "+v"(res[1]), "+v"(res[2]), "+v"(res[3]), "+v"(res[4]), "+v"(res[5]), "+v"(res[6]), "+v"(res[7]));
    BAR();
    STAMP(17);
    {
      auto piece = [&](int o, int h) {
        f32x4 v;
#pragma unroll
        for (int i = 0; i < 4; i++) v[i] = (o >> 1) ? a1[8 * (o & 1) + 4 * h + i] : a0[8 * (o & 1) + 4 * h + i];
        return v;
      };
#pragma unroll
      for (int o = 0; o < 4; o++) {
        if (o != (int)kq) {
          const u32 a = L_RED + ((nh * 4 + o) * 4 + kq) * 2048 + lv;
          *lds<f32x4>(a) = piece(o, 0); *lds<f32x4>(a + 1024) = piece(o, 1);
        }
      }
      f32x4 r0, r1;
      if (kq == 0) { r0 = piece(0, 0); r1 = piece(0, 1); }
      else if (kq == 1) { r0 = piece(1, 0); r1 = piece(1, 1); }
      else if (kq == 2) { r0 = piece(2, 0); r1 = piece(2, 1); }
      else { r0 = piece(3, 0); r1 = piece(3, 1); }
      BAR();
#pragma unroll
      for (int i = 1; i < 4; i++) {
        const u32 a = L_RED + ((nh * 4 + kq) * 4 + ((kq + i) & 3)) * 2048 + lv;
        r0 += *lds<f32x4>(a); r1 += *lds<f32x4>(a + 1024);
      }
      u32 o4[4];
      {
        const u32 y0 = pk_bf16(r0[0], r0[1]), y1 = pk_bf16(r0[2], r0[3]), y2 = pk_bf16(r1[0], r1[1]), y3 = pk_bf16(r1[2], r1[3]);
        o4[0] = pk_bf16(bf_lo(res[0]) + bf_lo(y0), bf_lo(res[1]) + bf_hi(y0));
        o4[1] = pk_bf16(bf_lo(res[2]) + bf_lo(y1), bf_lo(res[3]) + bf_hi(y1));
        o4[2] = pk_bf16(bf_lo(res[4]) + bf_lo(y2), bf_lo(res[5]) + bf_hi(y2));
        o4[3] = pk_bf16(bf_lo(res[6]) + bf_lo(y3), bf_lo(res[7]) + bf_hi(y3));
      }
      const auto x0 = __builtin_amdgcn_permlane32_swap(o4[0], o4[2], false, false);
      const auto x1 = __builtin_amdgcn_permlane32_swap(o4[1], o4[3], false, false);
      u32x4 v; v[0] = x0[0]; v[1] = x1[0]; v[2] = x0[1]; v[3] = x1[1];
      *(u32x4 *)(p.out + (size_t)own_t * DM + own_n + 8 * h32) = v;
    }
    STAMP(18);
  }
}

extern "C" void kernel_launch(void *const *d_in, const int *in_sizes, int n_in, void *d_out, int out_size, void *d_ws,
                              size_t ws_size, hipStream_t stream) {
  Params p;
  p.x = (const u16 *)d_in[0]; p.logA = (const u16 *)d_in[1]; p.Aim = (const u16 *)d_in[2]; p.Cr = (const u16 *)d_in[3];
  p.Ci = (const u16 *)d_in[4]; p.logdt = (const u16 *)d_in[5]; p.Dsk = (const u16 *)d_in[6];
  p.wg = (const u16 *)d_in[8]; p.wu = (const u16 *)d_in[9]; p.wd = (const u16 *)d_in[10];
  p.out = (u16 *)d_out; p.ws = (char *)d_ws;
  (void)hipFuncSetAttribute((const void *)fused, hipFuncAttributeMaxDynamicSharedMemorySize, LDS_BYTES);
  fused<<<256, 512, LDS_BYTES, stream>>>(p);
}
```

```cpp
#include <hip/hip_runtime.h>
#include <hip/hip_bf16.h>
#include <stdint.h>

#define STAMPS 0

typedef unsigned short u16;
typedef unsigned int u32;
typedef unsigned long long u64;
typedef float f32x4 __attribute__((ext_vector_type(4)));
typedef float f32x16 __attribute__((ext_vector_type(16)));
typedef _Float16 h16x8 __attribute__((ext_vector_type(8)));
typedef _Float16 h16x2 __attribute__((ext_vector_type(2)));
typedef __bf16 b16x8 __attribute__((ext_vector_type(8)));
typedef __bf16 b16x2 __attribute__((ext_vector_type(2)));
typedef short s16x4 __attribute__((ext_vector_type(4)));
typedef u32 u32x2 __attribute__((ext_vector_type(2)));
typedef u32 u32x4 __attribute__((ext_vector_type(4)));
#define LAS __attribute__((address_space(3)))
#define GAS __attribute__((address_space(1)))
#define CAS __attribute__((address_space(4)))

constexpr int SEQ = 2048, DM = 512, NMODE = 128, FF = 1792;

constexpr u32 WS_EPOCH = 0, WS_ST = 4096, WS_FLAG2 = 32768, WS_FLAG3 = 40960;
constexpr u32 WS_HT = 1u << 20, WS_P = 4u << 20, WS_DBG = 16u << 20, WS_EX = 32u << 20;

constexpr u32 LDS_BYTES = 163840;
constexpr u32 XY_STR = 544, XY_CH = 53 * XY_STR;
constexpr u32 L_XY = 0;
constexpr u32 UR_STR = 4640, UR_CH = 4 * UR_STR;
constexpr u32 L_UR = 0;
constexpr u32 L_XS = 37120;
constexpr u32 K_CH = 8448;
constexpr u32 L_K = 45312;
constexpr u32 VT_CH = 1344;
constexpr u32 L_VT = 62464;
constexpr u32 L_MISC = 9216;
constexpr u32 L_YP = 65536;
constexpr u32 T_STR = 2080, L_T = 57664;
constexpr u32 L_W0 = 81920;
constexpr u32 L_W2 = 49152;
constexpr u32 L_A = 0;
constexpr u32 L_ST = 131072;
constexpr u32 L_RED = 0;
constexpr int SPIN_CAP = 1 << 17;

struct Params {
  const u16 *x, *logA, *Aim, *Cr, *Ci, *logdt, *Dsk, *wg, *wu, *wd;
  u16 *out;
  char *ws;
};

__device__ __forceinline__ float bf_lo(u32 v) { return __uint_as_float(v << 16); }
__device__ __forceinline__ float bf_hi(u32 v) { return __uint_as_float(v & 0xffff0000u); }
__device__ __forceinline__ u32 pk_bf16(float a, float b) {
  b16x2 r; r[0] = (__bf16)a; r[1] = (__bf16)b; return __builtin_bit_cast(u32, r);
}
__device__ __forceinline__ float rbf(float a) { return bf_lo(pk_bf16(a, a)); }
__device__ __forceinline__ u32 pk_f16(float a, float b) {
  h16x2 r; r[0] = (_Float16)a; r[1] = (_Float16)b; return __builtin_bit_cast(u32, r);
}
template <typename T> __device__ __forceinline__ LAS T *lds(u32 off) { return (LAS T *)(size_t)off; }
__device__ __forceinline__ u32x2 tr_read(u32 addr) {
  return __builtin_bit_cast(u32x2, __builtin_amdgcn_ds_read_tr16_b64_v4i16((LAS s16x4 *)(size_t)addr));
}
__device__ __forceinline__ u32x4 cat(u32x2 a, u32x2 b) { u32x4 r; r[0] = a[0]; r[1] = a[1]; r[2] = b[0]; r[3] = b[1]; return r; }
__device__ __forceinline__ f32x16 mfma32(u32x4 a, u32x4 b, f32x16 c) {
  return __builtin_amdgcn_mfma_f32_32x32x16_bf16(__builtin_bit_cast(b16x8, a), __builtin_bit_cast(b16x8, b), c, 0, 0, 0);
}
__device__ __forceinline__ f32x4 mfma16h(u32x4 a, u32x4 b, f32x4 c) {
  return __builtin_amdgcn_mfma_f32_16x16x32_f16(__builtin_bit_cast(h16x8, a), __builtin_bit_cast(h16x8, b), c, 0, 0, 0);
}
__device__ __forceinline__ float dot2bf(u32 a, u32 b, float c) {
  return __builtin_amdgcn_fdot2_f32_bf16(__builtin_bit_cast(b16x2, a), __builtin_bit_cast(b16x2, b), c, false);
}
__device__ __forceinline__ void glds(u32 voff, const void *sbase, u32 ldst) {
  asm volatile("s_mov_b32 m0, %2\n\ts_nop 0\n\tglobal_load_lds_dwordx4 %0, %1" ::"v"(voff), "s"(sbase), "s"(ldst) : "memory");
}
__device__ __forceinline__ void glds_sc1(u32 voff, const void *sbase, u32 ldst) {
  asm volatile("s_mov_b32 m0, %2\n\ts_nop 0\n\tglobal_load_lds_dwordx4 %0, %1 sc1" ::"v"(voff), "s"(sbase), "s"(ldst) : "memory");
}
#define WAIT_VM(n) asm volatile("s_waitcnt vmcnt(" #n ")" ::: "memory")
#define WAIT_LGKM0() asm volatile("s_waitcnt lgkmcnt(0)" ::: "memory")
#define BAR() do { asm volatile("s_waitcnt lgkmcnt(0)\n\ts_barrier" ::: "memory"); } while (0)
#define SCHED() __builtin_amdgcn_sched_barrier(0)
#define PIN(x) asm volatile("" : "+v"(x))

template <int CTRL> __device__ __forceinline__ float dpp(float v) {
  return __uint_as_float(__builtin_amdgcn_update_dpp(0u, __float_as_uint(v), CTRL, 0xf, 0xf, true));
}
__device__ __forceinline__ float wave_sum(float v) {
  v += dpp<0xB1>(v); v += dpp<0x4E>(v); v += dpp<0x141>(v); v += dpp<0x140>(v);
  auto a = __builtin_amdgcn_permlane16_swap(__float_as_uint(v), __float_as_uint(v), false, false);
  v = __uint_as_float(a[0]) + __uint_as_float(a[1]);
  auto c = __builtin_amdgcn_permlane32_swap(__float_as_uint(v), __float_as_uint(v), false, false);
  return __uint_as_float(c[0]) + __uint_as_float(c[1]);
}
__device__ __forceinline__ float wave_max(float v) {
  v = fmaxf(v, dpp<0xB1>(v)); v = fmaxf(v, dpp<0x4E>(v)); v = fmaxf(v, dpp<0x141>(v)); v = fmaxf(v, dpp<0x140>(v));
  auto a = __builtin_amdgcn_permlane16_swap(__float_as_uint(v), __float_as_uint(v), false, false);
  v = fmaxf(__uint_as_float(a[0]), __uint_as_float(a[1]));
  auto c = __builtin_amdgcn_permlane32_swap(__float_as_uint(v), __float_as_uint(v), false, false);
  return fmaxf(__uint_as_float(c[0]), __uint_as_float(c[1]));
}
struct cf { float r, i; };
__device__ __forceinline__ cf cmul(cf a, cf b) { return {a.r * b.r - a.i * b.i, a.r * b.i + a.i * b.r}; }
__device__ __forceinline__ cf cexp_at(float lr, float rev, float pos) {
  float e = __builtin_amdgcn_exp2f(lr * pos), f = __builtin_amdgcn_fractf(rev * pos);
  return {e * __builtin_amdgcn_cosf(f), e * __builtin_amdgcn_sinf(f)};
}

#define STAMP(i)

__global__ void __launch_bounds__(512, 2) fused(Params p) {
  extern __shared__ __attribute__((aligned(1024))) char smem_[];
  const u32 tid = threadIdx.x, lane = tid & 63;
  const u32 w = __builtin_amdgcn_readfirstlane(tid >> 6);
  const u32 b = blockIdx.x, xcd = b & 7, jj = b >> 3;
  char *const ws = p.ws;
  STAMP(0);
  const bool g1 = jj < 28;
  const u32 panel = 4 * (xcd >> 1) + (jj / 7 & 3), ft = 7 * (xcd & 1) + jj % 7;
  u32 tag = 0;

  u32x4 W[32];
  const u32 w1_voff = ((lane >> 4) * FF + ft * 128) * 2 + ((((lane & 15) >> 1) ^ ((lane >> 4) | ((w & 1) << 2))) * 32) + (lane & 1) * 16;
  const char *const w1_base = (const char *)((w & 1) ? p.wu : p.wg) + (size_t)((w >> 1) * 4) * (FF * 2);
  auto w1_one = [&](int ks, u32 dst) {
    glds(w1_voff, w1_base + (size_t)ks * 16 * (FF * 2), dst + w * 1024);
  };
  const u32 g4 = lane >> 4, q4 = (lane >> 2) & 3, p4 = lane & 3;
  const u32 w1_rd = (g4 >> 1) * 2048 + (g4 & 1) * 1024 + q4 * 256 + ((w ^ (q4 | ((g4 & 1) << 2))) * 32) + p4 * 8;

  const u32 cp = xcd * 32 + jj, d0 = 2 * cp;
  const u32 ch = w >> 2, job = w & 3;
  u32x4 xr; u32 pl, pa, pr, pi;
  {
    const u32 o1 = (8 * b + w) * (DM * 2) + lane * 16;
    asm volatile("global_load_dwordx4 %0, %1, %2" : "=v"(xr) : "v"(o1), "s"(p.x));
    const u32 o2 = ((d0 + ch) * NMODE + 2 * lane) * 2;
    asm volatile("global_load_dword %0, %1, %2" : "=v"(pl) : "v"(o2), "s"(p.logA));
    asm volatile("global_load_dword %0, %1, %2" : "=v"(pa) : "v"(o2), "s"(p.Aim));
    asm volatile("global_load_dword %0, %1, %2" : "=v"(pr) : "v"(o2), "s"(p.Cr));
    asm volatile("global_load_dword %0, %1, %2" : "=v"(pi) : "v"(o2), "s"(p.Ci));
  }
  u32 junk;
  {
    u64 pc; u32 sz;
    asm volatile(".Lcode_begin:\n\ts_getpc_b64 %0\n\ts_mov_b32 %1, .Lcode_end-.Lcode_begin" : "=s"(pc), "=s"(sz));
    const u32 co = min((jj * 8 + w) * 256 + lane * 4, sz);
    asm volatile("global_load_dword %0, %1, %2" : "=v"(junk) : "v"(co), "s"(pc));
    const u32 gw = b * 8 + w, po = (gw < 1792 ? gw : gw - 1024) * 1024 + lane * 16;
    glds(po, p.wg, L_W0 + w * 1024); glds(po, p.wu, L_W0 + w * 1024); glds(po, p.wd, L_W0 + w * 1024);
  }
  const u32 ldt2 = *(const CAS u32 *)(p.logdt + d0), dsk2 = *(const CAS u32 *)(p.Dsk + d0);
  asm volatile("s_load_dword %0, %1, 0x0\n\ts_waitcnt lgkmcnt(0)" : "=s"(tag) : "s"(ws) : "memory");
  tag += 1u;
  u32 vidx[8];
  {
    const u32 l0 = 8 * (tid & 255);
#pragma unroll
    for (int e = 0; e < 8; e++) {
      const u32 l = l0 + e, bits = pk_bf16((float)l, 0.f) & 0xffffu;
      vidx[e] = L_VT + (tid >> 8) * VT_CH + 2 * (l < 256 ? l : bits - (0x4380u - 256u));
    }
#pragma unroll
    for (int e = 0; e < 8; e++) asm volatile("" : "+v"(vidx[e]));
  }
  asm volatile("s_waitcnt vmcnt(8)" : "+v"(xr));
  STAMP(19);
  {
    float v[8];
#pragma unroll
    for (int i = 0; i < 4; i++) { v[2 * i] = bf_lo(xr[i]); v[2 * i + 1] = bf_hi(xr[i]); }
    const float m = wave_sum(((v[0] + v[1]) + (v[2] + v[3])) + ((v[4] + v[5]) + (v[6] + v[7]))) * (1.f / DM);
    float q0 = 0, q1 = 0;
#pragma unroll
    for (int i = 0; i < 4; i++) { const float t0 = v[i] - m, t1 = v[i + 4] - m; q0 += t0 * t0; q1 += t1 * t1; }
    const float var = rbf(wave_sum(q0 + q1) * (1.f / DM));
    const float rs = __builtin_amdgcn_rcpf(rbf(__builtin_amdgcn_sqrtf(rbf(var + 1e-5f)))), mb = rbf(m);
    u32 uu[4];
#pragma unroll
    for (int i = 0; i < 4; i++) {
      const u32 a = pk_bf16(v[2 * i] - mb, v[2 * i + 1] - mb);
      const u32 u = pk_bf16(bf_lo(a) * rs, bf_hi(a) * rs);
      uu[i] = pk_f16(bf_lo(u), bf_hi(u));
    }
    const u32 ta = L_T + w * T_STR + lane * 32;
    u32x4 o; o[0] = uu[0]; o[1] = xr[0]; o[2] = uu[1]; o[3] = xr[1];
    *lds<u32x4>(ta) = o;
    o[0] = uu[2]; o[1] = xr[2]; o[2] = uu[3]; o[3] = xr[3];
    *lds<u32x4>(ta + 16) = o;
  }
  STAMP(20);
  BAR();
  {
    __amdgpu_buffer_rsrc_t rs = __builtin_amdgcn_make_buffer_rsrc(ws + WS_EX, 0, 256 * 256 * 128, 0x00020000);
    const u32 ra = L_T + (4 * (p4 & 1) + 3 - q4) * T_STR + (32 * w + 2 * g4 + (p4 >> 1)) * 8;
    const u32 so = ((32 * w + (lane >> 3)) * 256 + b) * 128 + (lane & 7) * 16;
    u32x2 r[4];
#pragma unroll
    for (int i = 0; i < 4; i++) r[i] = tr_read(ra + i * 64);
#pragma unroll
    for (int i = 0; i < 4; i++) {
      u32x4 o; o[0] = r[i][0]; o[1] = tag; o[2] = r[i][1]; o[3] = tag;
      __builtin_amdgcn_raw_buffer_store_b128(o, rs, so + i * (8 * 256 * 128), 0, 16);
    }
  }
  STAMP(1);
  asm volatile("s_waitcnt vmcnt(8)" : "+v"(pl), "+v"(pa), "+v"(pr), "+v"(pi));
  const float dt = rbf(__expf(ch ? bf_hi(ldt2) : bf_lo(ldt2)));
  {
    const float LOG2E = 1.4426950408889634f, INV2PI = 0.15915494309189535f;
    float lr[2], rv[2];
    lr[0] = -rbf(__expf(bf_lo(pl))) * dt * LOG2E; lr[1] = -rbf(__expf(bf_hi(pl))) * dt * LOG2E;
    rv[0] = bf_lo(pa) * dt * INV2PI; rv[1] = bf_hi(pa) * dt * INV2PI;
    const u32 wb = L_XY + ch * XY_CH + lane * 8;
    auto put = [&](int row, cf a, cf c) {
      u32x2 v; v[0] = pk_f16(a.r, a.i); v[1] = pk_f16(c.r, c.i);
      *lds<u32x2>(wb + row * XY_STR) = v;
    };
    auto G = [&](int i) { w1_one(i, L_W0 + i * 8192); };
    if (job < 2) {
      G(0);
      cf s0 = cexp_at(lr[0], -rv[0], 8.f), s1 = cexp_at(lr[1], -rv[1], 8.f);
      const float p0 = job ? 128.f : 0.f;
      cf z0 = cexp_at(lr[0], -rv[0], p0), z1 = cexp_at(lr[1], -rv[1], p0);
      const u32 wb2 = wb + job * 16 * XY_STR;
#pragma unroll
      for (int i = 0; i < 16; i++) {
        u32x2 v; v[0] = pk_f16(z0.r, z0.i); v[1] = pk_f16(z1.r, z1.i);
        *lds<u32x2>(wb2 + i * XY_STR) = v;
        if (i < 15) { z0 = cmul(z0, s0); z1 = cmul(z1, s1); }
        if (i == 2 || i == 7 || i == 12) G(1 + i / 5);
        SCHED();
      }
    } else {
      G(0);
      cf c0 = {bf_lo(pr), bf_lo(pi)}, c1 = {bf_hi(pr), bf_hi(pi)};
      const float mx = wave_max(fmaxf(fmaxf(fabsf(c0.r), fabsf(c0.i)), fmaxf(fabsf(c1.r), fabsf(c1.i))));
      const int ex = __builtin_amdgcn_frexp_expf(mx);
      const float sc = __builtin_amdgcn_ldexpf(1.f, -ex);
      c0.r *= sc; c0.i *= sc; c1.r *= sc; c1.i *= sc;
      G(1);
      if (job == 2) {
        if (lane == 0) *lds<float>(L_MISC + ch * 4) = __builtin_amdgcn_ldexpf(2.f * dt, ex);
        cf s0 = cexp_at(lr[0], rv[0], 1.f), s1 = cexp_at(lr[1], rv[1], 1.f);
        cf z0 = c0, z1 = c1;
#pragma unroll
        for (int i = 0; i < 8; i++) { put(32 + i, z0, z1); if (i < 7) { z0 = cmul(z0, s0); z1 = cmul(z1, s1); } if (i == 2 || i == 7) G(2 + i / 5); SCHED(); }
        s0 = cmul(s0, s0); s1 = cmul(s1, s1);
        z0 = cmul(c0, cexp_at(lr[0], rv[0], 256.f)); z1 = cmul(c1, cexp_at(lr[1], rv[1], 256.f));
#pragma unroll
        for (int i = 0; i < 4; i++) { put(40 + i, z0, z1); if (i < 3) { z0 = cmul(z0, s0); z1 = cmul(z1, s1); } }
      } else {
        cf f0 = cexp_at(lr[0], rv[0], 4.f), f1 = cexp_at(lr[1], rv[1], 4.f);
        cf s0 = cexp_at(lr[0], rv[0], 256.f), s1 = cexp_at(lr[1], rv[1], 256.f);
        G(2); SCHED();
        cf z0 = cmul(c0, cexp_at(lr[0], rv[0], 512.f)), z1 = cmul(c1, cexp_at(lr[1], rv[1], 512.f));
        put(44, z0, z1); put(45, cmul(z0, f0), cmul(z1, f1));
        z0 = cmul(z0, s0); z1 = cmul(z1, s1);
        put(46, z0, z1); put(47, cmul(z0, f0), cmul(z1, f1));
#pragma unroll
        for (int i = 0; i < 5; i++) { z0 = cmul(z0, s0); z1 = cmul(z1, s1); put(48 + i, z0, z1); if (i == 1) G(3); SCHED(); }
      }
    }
  }
  BAR();
  STAMP(2);
  {
    const u32 rb = job >> 1, cb = job & 1;
    const u32 xa = L_XY + ch * XY_CH + (32 + 16 * rb + (lane & 15)) * XY_STR + g4 * 16;
    const u32 ya = L_XY + ch * XY_CH + (16 * cb + (lane & 15)) * XY_STR + g4 * 16;
    u32x4 fa[8], fb[8];
#pragma unroll
    for (int ks = 0; ks < 8; ks++) { fa[ks] = *lds<u32x4>(xa + ks * 64); fb[ks] = *lds<u32x4>(ya + ks * 64); }
    SCHED();
#pragma unroll
    for (int i = 4; i < 8; i++) w1_one(i, L_W0 + i * 8192);
    SCHED();
    const u32 j = 16 * cb + (lane & 15);
    u32 i0, is, i2;
    if (rb == 0) {
      i0 = g4 < 2 ? 4 * g4 + 8 * j : (g4 == 2 ? 256 + 4 * j : 384 + 2 * j); is = 1; i2 = g4 == 3 ? 64 : 2;
    } else {
      i0 = g4 == 0 ? 512 + j : 640 + j; is = g4 == 0 ? 32 : 0; i2 = 2 * is;
    }
    f32x4 a0 = {0, 0, 0, 0}, a1 = {0, 0, 0, 0};
#pragma unroll
    for (int ks = 0; ks < 8; ks += 2) { a0 = mfma16h(fa[ks], fb[ks], a0); a1 = mfma16h(fa[ks + 1], fb[ks + 1], a1); }
    a0 += a1;
    const u32 vb = L_VT + ch * VT_CH;
    if (rb == 0 || g4 == 0) {
#pragma unroll
      for (int r = 0; r < 4; r++) *lds<_Float16>(vb + 2 * (i0 + (r & 1) * is + (r >> 1) * i2)) = (_Float16)a0[r];
    } else if (g4 == 1) {
      *lds<_Float16>(vb + 2 * i0) = (_Float16)a0[0];
    }
  }
  BAR();
  STAMP(3);
  u32x4 ex[4];
  const char *const exb = ws + WS_EX + (size_t)cp * 32768;
  const u32 exo = tid * 16;
#define EX_LOAD(i) asm volatile("global_load_dwordx4 %0, %1, %2 sc1" : "=v"(ex[i]) : "v"(exo), "s"(exb + (i) * 8192))
  EX_LOAD(0); EX_LOAD(1); EX_LOAD(2); EX_LOAD(3);
  {
    u32x4 kv;
    _Float16 t[8];
#pragma unroll
    for (int e = 0; e < 8; e++) t[e] = *lds<_Float16>(vidx[e]);
#pragma unroll
    for (int e = 0; e < 4; e++) { h16x2 t2; t2[0] = t[2 * e]; t2[1] = t[2 * e + 1]; kv[e] = __builtin_bit_cast(u32, t2); }
    const u32 kb = L_K + (tid >> 8) * K_CH + (tid & 255) * 16;
    u32x2 lo, hi; lo[0] = kv[0]; lo[1] = kv[1]; hi[0] = kv[2]; hi[1] = kv[3];
    *lds<u32x2>(kb + 120) = lo; *lds<u32x2>(kb + 128) = hi;
    *lds<u32x4>(kb + 4224 + 112) = kv;
    const u32x2 z2 = {0, 0};
    u32 za;
    if (tid < 64) {
      const u32 c = tid >> 5, i = tid & 31;
      za = L_K + c * K_CH + (i < 15 ? i * 8 : 4224 + (i < 29 ? i - 15 : 0) * 8);
    } else {
      const u32 i = tid < 448 ? tid - 64 : 0, c = i / 48;
      za = L_UR + c * UR_STR + 4096 + (i - c * 48) * 8;
    }
    *lds<u32x2>(za) = z2;
  }
  STAMP(4);
  asm volatile("s_waitcnt vmcnt(0)" : "+v"(ex[0]), "+v"(ex[1]), "+v"(ex[2]), "+v"(ex[3]), "+v"(junk));
#pragma unroll 1
  for (int spin = 0; spin < SPIN_CAP; spin++) {
    bool ok = true;
#pragma unroll
    for (int i = 0; i < 4; i++) ok = ok && ex[i][1] == tag && ex[i][3] == tag;
    if (__all(ok)) break;
    __builtin_amdgcn_s_sleep(1);
    asm volatile("global_load_dwordx4 %0, %4, %5 sc1\n\tglobal_load_dwordx4 %1, %4, %6 sc1\n\t"
                 "global_load_dwordx4 %2, %4, %7 sc1\n\tglobal_load_dwordx4 %3, %4, %8 sc1\n\ts_waitcnt vmcnt(0)"
                 : "=&v"(ex[0]), "=&v"(ex[1]), "=&v"(ex[2]), "=&v"(ex[3])
                 : "v"(exo), "s"(exb), "s"(exb + 8192), "s"(exb + 16384), "s"(exb + 24576) : "memory");
  }
  STAMP(5);
  {
    const u32 e = tid & 3, t0 = 8 * (tid >> 3) + (tid & 4);
    const u32 ab = (e < 2 ? L_UR + 3 * UR_STR + e * UR_CH : L_XS + (e - 2) * 4096) + (2044 - 1536 - t0) * 2;
#pragma unroll
    for (int i = 0; i < 4; i++) { u32x2 v; v[0] = ex[i][0]; v[1] = ex[i][2]; *lds<u32x2>(ab + (3 - i) * 1024) = v; }
  }
  BAR();
  STAMP(21);
  {
    const u32 ub = L_UR + (tid >> 8) * UR_CH + (tid & 255) * 16;
    const u32x4 a = *lds<u32x4>(ub + 3 * UR_STR);
    const u32x2 c = *lds<u32x2>(ub + 3 * UR_STR + 16);
    SCHED();
    u32 wr = L_W0 + w1_rd;
    asm volatile("" : "+v"(wr));
#pragma unroll
    for (int ks = 0; ks < 8; ks++) W[ks] = cat(tr_read(wr + ks * 8192), tr_read(wr + ks * 8192 + 4096));
    SCHED();
    u32x4 o;
    o[0] = __builtin_amdgcn_alignbit(a[1], a[0], 16); o[1] = __builtin_amdgcn_alignbit(a[2], a[1], 16);
    o[2] = __builtin_amdgcn_alignbit(a[3], a[2], 16); o[3] = __builtin_amdgcn_alignbit(c[0], a[3], 16);
    *lds<u32x4>(ub + 2 * UR_STR) = o;
    u32x4 o1; o1[0] = a[1]; o1[1] = a[2]; o1[2] = a[3]; o1[3] = c[0];
    *lds<u32x4>(ub + UR_STR) = o1;
    u32x4 o0; o0[0] = o[1]; o0[1] = o[2]; o0[2] = o[3]; o0[3] = __builtin_amdgcn_alignbit(c[1], c[0], 16);
    *lds<u32x4>(ub) = o0;
  }
  BAR();
  STAMP(6);
  const u32 pg = w & 1, cg = (w >> 1) & 1;
  f32x4 ya[4];
#pragma unroll
  for (int i = 0; i < 4; i++) ya[i] = f32x4{0, 0, 0, 0};
  {
    const u32 c16 = lane & 15;
    const u32 ka = L_K + ch * K_CH + (c16 & 1) * 4224 + (c16 >> 1) * 16 + g4 * 16 + cg * 4 * 64;
    const u32 ua = L_UR + ch * UR_CH + (c16 & 3) * UR_STR + (1984 - 1792 - 64 * (c16 >> 2) + 8 * g4) * 2 + cg * 4 * 64;
    u32x4 kf[2][8], uf[2][8];
    auto rd = [&](int s, int i) { uf[s & 1][7 - i] = *lds<u32x4>(ua + i * 512 + s * 64); kf[s & 1][i] = *lds<u32x4>(ka + i * 512 + s * 64); };
#pragma unroll
    for (int i = 0; i < 8; i++) rd(0, i);
    if (pg == 0) {
#pragma unroll
      for (int s = 0; s < 4; s++) {
#pragma unroll
        for (int i = 0; i < 8; i++) {
          if (s < 3) rd(s + 1, i);
          if ((i % 3) == 1 && s * 3 + i / 3 < 10) w1_one(8 + s * 3 + i / 3, L_W0 + (s * 3 + i / 3) * 8192);
          ya[3] = mfma16h(uf[s & 1][7 - i], kf[s & 1][i], ya[3]);
          if (i <= 4) ya[2] = mfma16h(uf[s & 1][4 - i], kf[s & 1][i], ya[2]);
          if (i <= 3) ya[1] = mfma16h(uf[s & 1][3 - i], kf[s & 1][i], ya[1]);
          if (i <= 0) ya[0] = mfma16h(uf[s & 1][0 - i], kf[s & 1][i], ya[0]);
          SCHED();
        }
      }
    } else {
#pragma unroll
      for (int s = 0; s < 4; s++) {
#pragma unroll
        for (int i = 0; i < 8; i++) {
          if (s < 3) rd(s + 1, i);
          if ((i % 3) == 1 && s * 3 + i / 3 < 10) w1_one(8 + s * 3 + i / 3, L_W0 + (s * 3 + i / 3) * 8192);
          if (i <= 6) ya[3] = mfma16h(uf[s & 1][6 - i], kf[s & 1][i], ya[3]);
          if (i <= 5) ya[2] = mfma16h(uf[s & 1][5 - i], kf[s & 1][i], ya[2]);
          if (i <= 2) ya[1] = mfma16h(uf[s & 1][2 - i], kf[s & 1][i], ya[1]);
          if (i <= 1) ya[0] = mfma16h(uf[s & 1][1 - i], kf[s & 1][i], ya[0]);
          SCHED();
        }
      }
    }
  }
  {
    const u32 yb = L_YP + w * 2048 + lane * 16;
    *lds<f32x4>(yb) = cg ? ya[0] : ya[2];
    *lds<f32x4>(yb + 1024) = cg ? ya[1] : ya[3];
  }
  WAIT_VM(0);
  BAR();
  STAMP(7);
  {
    const u32 yb = L_YP + (w ^ 2) * 2048 + lane * 16;
    const float ksc = *lds<float>(L_MISC + ch * 4), dsk = ch ? bf_hi(dsk2) : bf_lo(dsk2);
    __amdgpu_buffer_rsrc_t rs = __builtin_amdgcn_make_buffer_rsrc(ws + WS_HT, 0, SEQ * DM * 2, 0x00020000);
    f32x4 o[2]; u32x2 uu[2], xx[2]; u32 t0[2];
#pragma unroll
    for (int i = 0; i < 2; i++) {
      o[i] = *lds<f32x4>(yb + i * 1024);
      t0[i] = 256 * (4 * cg + (i ? 3 - pg : pg)) + 4 * lane;
      uu[i] = *lds<u32x2>(L_UR + ch * UR_CH + 3 * UR_STR + (2044 - t0[i]) * 2);
      xx[i] = *lds<u32x2>(L_XS + ch * 4096 + (2044 - t0[i]) * 2);
    }
#pragma unroll
    for (int i = 0; i < 2; i++) {
      const f32x4 own = cg ? (i ? ya[3] : ya[2]) : (i ? ya[1] : ya[0]);
      const u32 uu0 = uu[i][0], uu1 = uu[i][1];
      const h16x2 ua = __builtin_bit_cast(h16x2, uu1), ub = __builtin_bit_cast(h16x2, uu0);
      const float uv[4] = {(float)ua[1], (float)ua[0], (float)ub[1], (float)ub[0]};
      const float xv[4] = {bf_hi(xx[i][1]), bf_lo(xx[i][1]), bf_hi(xx[i][0]), bf_lo(xx[i][0])};
      float hv[4];
#pragma unroll
      for (int r = 0; r < 4; r += 2) {
        const u32 y2 = pk_bf16((o[i][r] + own[r]) * ksc, (o[i][r + 1] + own[r + 1]) * ksc);
        const u32 d2 = pk_bf16(uv[r] * dsk, uv[r + 1] * dsk);
        const u32 z2 = pk_bf16(bf_lo(y2) + bf_lo(d2), bf_hi(y2) + bf_hi(d2));
        hv[r] = xv[r] + bf_lo(z2); hv[r + 1] = xv[r + 1] + bf_hi(z2);
      }
      u32x2 hv2; hv2[0] = pk_bf16(hv[0], hv[1]); hv2[1] = pk_bf16(hv[2], hv[3]);
      const u32 off = ((t0[i] >> 5) * DM + d0 + ch) * 64 + (t0[i] & 31) * 2;
      __builtin_amdgcn_raw_buffer_store_b64(hv2, rs, off, 0, 16);
    }
  }
  SCHED();
  {
    u32 wr = L_W0 + w1_rd;
    asm volatile("" : "+v"(wr));
#pragma unroll
    for (int ks = 0; ks < 8; ks++) W[8 + ks] = cat(tr_read(wr + ks * 8192), tr_read(wr + ks * 8192 + 4096));
    wr += 65536;
#pragma unroll
    for (int ks = 8; ks < 10; ks++) W[8 + ks] = cat(tr_read(wr + (ks - 8) * 8192), tr_read(wr + (ks - 8) * 8192 + 4096));
  }
  if (b == 0 && tid == 0)
    __hip_atomic_store((GAS u32 *)(ws + WS_EPOCH), tag, __ATOMIC_RELAXED, __HIP_MEMORY_SCOPE_AGENT);
  BAR();
  f32x16 cs;
#pragma unroll
  for (int i = 0; i < 16; i++) cs[i] = 0;
  const u32x4 ones = {0x3f803f80u, 0x3f803f80u, 0x3f803f80u, 0x3f803f80u};
#pragma unroll
  for (int ks = 0; ks < 6; ks++) { w1_one(18 + ks, L_W2 + ks * 8192); cs = mfma32(W[ks], ones, cs); SCHED(); }
  WAIT_VM(6);
  BAR();
  if (tid == 0) __hip_atomic_store((GAS u32 *)(ws + WS_FLAG2) + b, tag, __ATOMIC_RELAXED, __HIP_MEMORY_SCOPE_AGENT);
  STAMP(8);
#pragma unroll
  for (int ks = 6; ks < 18; ks++) { if (ks < 14) w1_one(18 + ks, L_W2 + ks * 8192); cs = mfma32(W[ks], ones, cs); SCHED(); }
  WAIT_VM(0);
  BAR();
  const u32 h32 = lane >> 5, t32 = lane & 31;
  auto poll2 = [&]() {
    const u32 fo = WS_FLAG2 + lane * 16;
#pragma unroll 1
    for (int spin = 0; spin < SPIN_CAP; spin++) {
      u32x4 f;
      asm volatile("global_load_dwordx4 %0, %1, %2 sc1\n\ts_waitcnt vmcnt(0)" : "=&v"(f) : "v"(fo), "s"(ws) : "memory");
      if (__all(f[0] == tag && f[1] == tag && f[2] == tag && f[3] == tag)) break;
      __builtin_amdgcn_s_sleep(1);
    }
  };

  if (g1) {
    {
      u32 wr = L_W2 + w1_rd;
      asm volatile("" : "+v"(wr));
#pragma unroll
      for (int ks = 0; ks < 8; ks++) W[18 + ks] = cat(tr_read(wr + ks * 8192), tr_read(wr + ks * 8192 + 4096));
      wr += 65536;
#pragma unroll
      for (int ks = 8; ks < 14; ks++) W[18 + ks] = cat(tr_read(wr + (ks - 8) * 8192), tr_read(wr + (ks - 8) * 8192 + 4096));
    }
    STAMP(9);
    if (w == 0) poll2();
    BAR();
    STAMP(10);
    {
      const char *base = ws + WS_HT + (size_t)panel * 131072 + w * 1024;
#pragma unroll
      for (int i = 0; i < 16; i++) glds_sc1(lane * 16, base + i * 8192, L_A + w * 1024 + i * 8192);
    }
#pragma unroll
    for (int ks = 18; ks < 32; ks++) cs = mfma32(W[ks], ones, cs);
    SCHED();
    u32 ab = L_A + (4 * (g4 >> 1) + q4) * 64 + (g4 & 1) * 32 + p4 * 8;
    __amdgpu_buffer_rsrc_t prs = __builtin_amdgcn_make_buffer_rsrc(ws + WS_P, 0, SEQ * FF * 2, 0x00020000);
    u32 p_off = (panel * 4 * 112 + ft * 8 + w) * 1024 + lane * 16;
    u32x4 F[4];
    auto frag = [&](u32 a) { return cat(tr_read(a), tr_read(a + 512)); };
    float e_m, e_ne, e_s2, e_g, e_u, e_e, e_o;
    u32 pko[4];
    u32x2 sg;
    const u32 sg_off = WS_ST + (panel * 128 + t32) * 8;
    auto st_issue = [&](int tg) {
      if (tg == 0) asm volatile("global_load_dwordx2 %0, %1, %2 sc1" : "=v"(sg) : "v"(sg_off), "s"(ws));
      if (tg == 1) asm volatile("global_load_dwordx2 %0, %1, %2 offset:256 sc1" : "=v"(sg) : "v"(sg_off), "s"(ws));
      if (tg == 2) asm volatile("global_load_dwordx2 %0, %1, %2 offset:512 sc1" : "=v"(sg) : "v"(sg_off), "s"(ws));
      if (tg == 3) asm volatile("global_load_dwordx2 %0, %1, %2 offset:768 sc1" : "=v"(sg) : "v"(sg_off), "s"(ws));
    };
    auto st_take = [&](int tg) {
      if (tg == 0) asm volatile("s_waitcnt vmcnt(0)" : "+v"(sg)); else asm volatile("s_waitcnt vmcnt(1)" : "+v"(sg));
#pragma unroll 1
      for (int spin = 0; spin < SPIN_CAP; spin++) {
        if (__all(sg[1] == tag)) break;
        __builtin_amdgcn_s_sleep(1);
        asm volatile("global_load_dwordx2 %0, %1, %2 sc1\n\ts_waitcnt vmcnt(0)" : "=&v"(sg) : "v"(sg_off + tg * 256), "s"(ws) : "memory");
      }
      e_m = bf_lo(sg[0]);
      const float sd = bf_hi(sg[0]);
      e_ne = -1.4426950408889634f * __builtin_amdgcn_rcpf(sd); e_s2 = sd * sd;
    };
    auto epi_step = [&](int ks, int tg, const f32x16 &prev) {
      if (ks == 0) st_take(tg);
      else if (ks >= 1 && ks < 25) {
        const int i = (ks - 1) / 3, ph = (ks - 1) % 3;
        if (ph == 0) {
          e_g = prev[i] - e_m * cs[i]; PIN(e_g);
          e_e = __builtin_amdgcn_exp2f(e_g * e_ne);
        } else if (ph == 1) {
          e_u = (prev[i + 8] - e_m * cs[i + 8]) * e_g;
          e_e = e_e * e_s2 + e_s2;
          PIN(e_u); PIN(e_e);
        } else {
          const float o = e_u * __builtin_amdgcn_rcpf(e_e);
          if (i & 1) { pko[i >> 1] = pk_bf16(e_o, o); PIN(pko[i >> 1]); } else { e_o = o; PIN(e_o); }
        }
      } else if (ks == 25) {
        u32x4 v; v[0] = pko[0]; v[1] = pko[1]; v[2] = pko[2]; v[3] = pko[3];
        __builtin_amdgcn_raw_buffer_store_b128(v, prs, p_off + tg * (112 * 1024), 0, 16);
      }
    };
    f32x16 acc[2];
    WAIT_VM(15);
    BAR();
    u32 ab2 = ab + 65536;
    PIN(ab); PIN(ab2);
#pragma unroll
    for (int i = 0; i < 3; i++) F[i] = frag(ab + i * 1024);
#pragma unroll
    for (int tg = 0; tg < 4; tg++) {
#pragma unroll
      for (int i = 0; i < 16; i++) acc[tg & 1][i] = 0;
#pragma unroll
      for (int ks = 0; ks < 32; ks++) {
        if (tg == 0) {
          if (ks == 5) { WAIT_VM(14); BAR(); }
          if (ks == 13) { WAIT_VM(13); BAR(); }
          if (ks == 21) { WAIT_VM(12); BAR(); }
          if (ks == 29) { WAIT_VM(8); BAR(); }
        }
        if (tg == 1 && ks == 29) BAR();
        const int n = tg * 32 + ks + 3;
        if (n < 128) F[n & 3] = frag((n < 64 ? ab : ab2) + (n & 63) * 1024);
        acc[tg & 1] = mfma32(W[ks], F[ks & 3], acc[tg & 1]);
        if (tg > 0 && ks >= 3) epi_step(ks - 3, tg - 1, acc[(tg - 1) & 1]);
        if (ks == (tg ? 26 : 30)) st_issue(tg);
        SCHED();
      }
      if (tg == 0) STAMP(11);
    }
    BAR();
    STAMP(12);
#pragma unroll
    for (int ks = 0; ks < 26; ks++) epi_step(ks, 3, acc[1]);
    STAMP(13);
  } else {
    if (w == 0) poll2();
    BAR();
    const u32 e = (xcd & 1) * 4 + (jj - 28), tgs = (4 * (xcd >> 1) + (e >> 1)) * 4 + (e & 1);
    const char *base = ws + WS_HT + (size_t)tgs * 32768 + w * 4096;
#pragma unroll
    for (int i = 0; i < 8; i++) glds_sc1(lane * 16, base + (i >> 2) * 65536 + (i & 3) * 1024, (i >> 2) * 32768 + w * 4096 + (i & 3) * 1024);
    const u32 ab = w * 4096 + (4 * (g4 >> 1) + q4) * 64 + (g4 & 1) * 32 + p4 * 8;
#pragma unroll
    for (int g = 0; g < 2; g++) {
      if (g == 0) WAIT_VM(4); else WAIT_VM(1);
      float s_s = 0, s_q = 0;
#pragma unroll
      for (int i = 0; i < 4; i++) {
        const u32x4 f = cat(tr_read(ab + g * 32768 + i * 1024), tr_read(ab + g * 32768 + i * 1024 + 512));
#pragma unroll
        for (int k = 0; k < 4; k++) { s_s = dot2bf(f[k], 0x3f803f80u, s_s); s_q = dot2bf(f[k], f[k], s_q); }
      }
      const auto sw = __builtin_amdgcn_permlane32_swap(__float_as_uint(s_s), __float_as_uint(s_q), false, false);
      *lds<float>(L_ST + g * 2048 + w * 256 + lane * 4) = __uint_as_float(sw[0]) + __uint_as_float(sw[1]);
      BAR();
      float sp = 0;
#pragma unroll
      for (int i = 0; i < 8; i++) sp += *lds<float>(L_ST + g * 2048 + i * 256 + lane * 4);
      const auto s2 = __builtin_amdgcn_permlane32_swap(__float_as_uint(sp), __float_as_uint(sp), false, false);
      const float mean = __uint_as_float(s2[0]) * (1.f / DM);
      const float var = rbf(fmaxf(__uint_as_float(s2[1]) * (1.f / DM) - mean * mean, 0.f));
      const u64 gr = ((u64)tag << 32) | pk_bf16(mean, __builtin_amdgcn_sqrtf(rbf(var + 1e-5f)));
      if ((lane >> 2) == w) __hip_atomic_store((GAS u64 *)(ws + WS_ST) + (tgs + 2 * g) * 32 + lane, gr, __ATOMIC_RELAXED, __HIP_MEMORY_SCOPE_AGENT);
    }
    BAR();
  }

  {
    const u32 nt = jj & 7, tt = jj >> 3, pn2 = 2 * xcd + (tt >> 1), tg0 = pn2 * 4 + (tt & 1) * 2;
    const u32 nh = w & 1, kq = w >> 1;
    const u32 r8 = lane >> 3;
    const u32 d_voff = r8 * (DM * 2) + (((lane & 7) ^ (((r8 >> 1) & 1) << 2)) * 16);
    auto wd_issue = [&](int ks0, int n) {
      const char *base = (const char *)p.wd + (size_t)(kq * 448 + ks0 * 16 + nh * 8) * (DM * 2) + nt * 128;
#pragma unroll
      for (int i = 0; i < n; i++) glds(d_voff, base + (size_t)i * 16 * (DM * 2), kq * 32768 + i * 2048 + nh * 1024);
    };
    const u32 d_rd = kq * 32768 + (((4 * (g4 >> 1) + q4) * 128 + nh * 64 + (g4 & 1) * 32 + p4 * 8) ^ (((q4 >> 1) & 1) * 64));
    wd_issue(0, 16);
    if (g1) {
      WAIT_VM(16);
      BAR();
      if (tid == 0) __hip_atomic_store((GAS u32 *)(ws + WS_FLAG3) + panel * 32 + ft, tag, __ATOMIC_RELAXED, __HIP_MEMORY_SCOPE_AGENT);
      STAMP(14);
    }
    WAIT_VM(0);
    BAR();
#pragma unroll
    for (int ks = 0; ks < 16; ks++) W[ks] = cat(tr_read(d_rd + ks * 2048), tr_read(d_rd + ks * 2048 + 1024));
    BAR();
    wd_issue(16, 12);
    WAIT_VM(0);
    BAR();
#pragma unroll
    for (int ks = 0; ks < 12; ks++) W[16 + ks] = cat(tr_read(d_rd + ks * 2048), tr_read(d_rd + ks * 2048 + 1024));
    STAMP(15);
    if (w == 0) {
      const u32 fo = WS_FLAG3 + pn2 * 128 + (lane < 14 ? lane : 0) * 4;
#pragma unroll 1
      for (int spin = 0; spin < SPIN_CAP; spin++) {
        u32 f;
        asm volatile("global_load_dword %0, %1, %2 sc1\n\ts_waitcnt vmcnt(0)" : "=&v"(f) : "v"(fo), "s"(ws) : "memory");
        if (__all(f == tag)) break;
        __builtin_amdgcn_s_sleep(1);
      }
    }
    BAR();
    STAMP(16);
    const char *pbase = ws + WS_P + ((size_t)(tg0 + nh) * 112 + kq * 28) * 1024;
    const u32 lv = lane * 16;
    auto p_issue = [&](int s) {
#pragma unroll
      for (int i = 0; i < 2; i++) glds_sc1(lv, pbase + (s * 2 + i) * 1024, (s & 7) * 16384 + kq * 4096 + i * 2048 + nh * 1024);
    };
    p_issue(0); p_issue(1);
    const u32 own_t = (tg0 + (kq >> 1)) * 32 + t32, own_n = nt * 64 + nh * 32 + (kq & 1) * 16;
    u32 res[8];
    {
      const u32 ro = WS_HT + ((own_t >> 5) * DM + own_n + 4 * h32) * 64 + t32 * 2;
#define RES_LD(i, o) asm volatile("global_load_ushort %0, %1, %2 offset:" #o " sc1" : "=v"(res[i]) : "v"(ro), "s"(ws));
      RES_LD(0, 0) RES_LD(1, 64) RES_LD(2, 128) RES_LD(3, 192) RES_LD(4, 512) RES_LD(5, 576) RES_LD(6, 640) RES_LD(7, 704)
#undef RES_LD
    }
#pragma unroll
    for (int s = 2; s < 8; s++) p_issue(s);
    f32x16 a0, a1;
#pragma unroll
    for (int i = 0; i < 16; i++) { a0[i] = 0; a1[i] = 0; }
    u32 p_rd = kq * 4096 + lv, p_rd2 = p_rd + 65536;
    asm volatile("" : "+v"(p_rd), "+v"(p_rd2));
    u32x4 PF[2][4];
    auto p_read = [&](int s, int i) { PF[s & 1][i] = *lds<u32x4>(((s & 4) ? p_rd2 : p_rd) + (s & 3) * 16384 + i * 1024); };
#pragma unroll
    for (int s = 0; s < 14; s++) {
      if (s == 0) WAIT_VM(20); else if (s <= 7) WAIT_VM(10); else if (s == 8) WAIT_VM(8); else if (s == 9) WAIT_VM(6);
      else if (s == 10) WAIT_VM(4); else if (s == 11) WAIT_VM(2); else if (s == 12) WAIT_VM(0);
      if (s == 0) BAR(); else if (s <= 12) asm volatile("s_barrier" ::: "memory");
      if (s == 0) {
#pragma unroll
        for (int i = 0; i < 4; i++) p_read(0, i);
      }
      if (s >= 1 && s + 7 < 14) p_issue(s + 7);
      SCHED();
#pragma unroll
      for (int i = 0; i < 4; i++) {
        if (s < 13) p_read(s + 1, i);
        if (i & 1) a1 = mfma32(W[s * 2 + (i >> 1)], PF[s & 1][i], a1);
        else a0 = mfma32(W[s * 2 + (i >> 1)], PF[s & 1][i], a0);
        SCHED();
      }
    }
    asm volatile("" : "+v"(res[0]), "+v"(res[1]), "+v"(res[2]), "+v"(res[3]), "+v"(res[4]), "+v"(res[5]), "+v"(res[6]), "+v"(res[7]));
    BAR();
    STAMP(17);
    {
      auto piece = [&](int o, int h) {
        f32x4 v;
#pragma unroll
        for (int i = 0; i < 4; i++) v[i] = (o >> 1) ? a1[8 * (o & 1) + 4 * h + i] : a0[8 * (o & 1) + 4 * h + i];
        return v;
      };
#pragma unroll
      for (int o = 0; o < 4; o++) {
        if (o != (int)kq) {
          const u32 a = L_RED + ((nh * 4 + o) * 4 + kq) * 2048 + lv;
          *lds<f32x4>(a) = piece(o, 0); *lds<f32x4>(a + 1024) = piece(o, 1);
        }
      }
      f32x4 r0, r1;
      if (kq == 0) { r0 = piece(0, 0); r1 = piece(0, 1); }
      else if (kq == 1) { r0 = piece(1, 0); r1 = piece(1, 1); }
      else if (kq == 2) { r0 = piece(2, 0); r1 = piece(2, 1); }
      else { r0 = piece(3, 0); r1 = piece(3, 1); }
      BAR();
#pragma unroll
      for (int i = 1; i < 4; i++) {
        const u32 a = L_RED + ((nh * 4 + kq) * 4 + ((kq + i) & 3)) * 2048 + lv;
        r0 += *lds<f32x4>(a); r1 += *lds<f32x4>(a + 1024);
      }
      u32 o4[4];
      {
        const u32 y0 = pk_bf16(r0[0], r0[1]), y1 = pk_bf16(r0[2], r0[3]), y2 = pk_bf16(r1[0], r1[1]), y3 = pk_bf16(r1[2], r1[3]);
        o4[0] = pk_bf16(bf_lo(res[0]) + bf_lo(y0), bf_lo(res[1]) + bf_hi(y0));
        o4[1] = pk_bf16(bf_lo(res[2]) + bf_lo(y1), bf_lo(res[3]) + bf_hi(y1));
        o4[2] = pk_bf16(bf_lo(res[4]) + bf_lo(y2), bf_lo(res[5]) + bf_hi(y2));
        o4[3] = pk_bf16(bf_lo(res[6]) + bf_lo(y3), bf_lo(res[7]) + bf_hi(y3));
      }
      const auto x0 = __builtin_amdgcn_permlane32_swap(o4[0], o4[2], false, false);
      const auto x1 = __builtin_amdgcn_permlane32_swap(o4[1], o4[3], false, false);
      u32x4 v; v[0] = x0[0]; v[1] = x1[0]; v[2] = x0[1]; v[3] = x1[1];
      *(u32x4 *)(p.out + (size_t)own_t * DM + own_n + 8 * h32) = v;
    }
    STAMP(18);
  }
  asm volatile(".Lcode_end:");
}

extern "C" void kernel_launch(void *const *d_in, const int *in_sizes, int n_in, void *d_out, int out_size, void *d_ws,
                              size_t ws_size, hipStream_t stream) {
  Params p;
  p.x = (const u16 *)d_in[0]; p.logA = (const u16 *)d_in[1]; p.Aim = (const u16 *)d_in[2]; p.Cr = (const u16 *)d_in[3];
  p.Ci = (const u16 *)d_in[4]; p.logdt = (const u16 *)d_in[5]; p.Dsk = (const u16 *)d_in[6];
  p.wg = (const u16 *)d_in[8]; p.wu = (const u16 *)d_in[9]; p.wd = (const u16 *)d_in[10];
  p.out = (u16 *)d_out; p.ws = (char *)d_ws;
  (void)hipFuncSetAttribute((const void *)fused, hipFuncAttributeMaxDynamicSharedMemorySize, LDS_BYTES);
  fused<<<256, 512, LDS_BYTES, stream>>>(p);
}
```

```cpp
#include <hip/hip_runtime.h>
#include <hip/hip_bf16.h>
#include <stdint.h>

#define STAMPS 0

typedef unsigned short u16;
typedef unsigned int u32;
typedef unsigned long long u64;
typedef float f32x2 __attribute__((ext_vector_type(2)));
typedef float f32x4 __attribute__((ext_vector_type(4)));
typedef float f32x16 __attribute__((ext_vector_type(16)));
typedef _Float16 h16x8 __attribute__((ext_vector_type(8)));
typedef _Float16 h16x2 __attribute__((ext_vector_type(2)));
typedef __bf16 b16x8 __attribute__((ext_vector_type(8)));
typedef __bf16 b16x2 __attribute__((ext_vector_type(2)));
typedef short s16x4 __attribute__((ext_vector_type(4)));
typedef u32 u32x2 __attribute__((ext_vector_type(2)));
typedef u32 u32x4 __attribute__((ext_vector_type(4)));
#define LAS __attribute__((address_space(3)))
#define GAS __attribute__((address_space(1)))
#define CAS __attribute__((address_space(4)))

constexpr int SEQ = 2048, DM = 512, NMODE = 128, FF = 1792;

constexpr u32 WS_EPOCH = 0, WS_ST = 4096, WS_FLAG2 = 32768, WS_FLAG3 = 65536, WS_FLAG1 = 196608;
constexpr u32 WS_HT = 1u << 20, WS_P = 4u << 20, WS_DBG = 16u << 20, WS_EX = 32u << 20, WS_XT = 40u << 20;

constexpr u32 LDS_BYTES = 163840;
constexpr u32 XY_STR = 544, XY_CH = 53 * XY_STR;
constexpr u32 L_XY = 0;
constexpr u32 UR_STR = 4640, UR_CH = 4 * UR_STR;
constexpr u32 L_UR = 0;
constexpr u32 L_XS = 37120;
constexpr u32 K_CH = 8448;
constexpr u32 L_K = 45312;
constexpr u32 VT_CH = 1344;
constexpr u32 L_VT = 62464;
constexpr u32 L_MISC = 9216;
constexpr u32 L_YP = 65536;
constexpr u32 T_STR = 1056, L_T = 57664, L_TX = L_T + 8 * T_STR;
constexpr u32 L_W0 = 81920;
constexpr u32 L_W2 = 49152;
constexpr u32 L_A = 0;
constexpr u32 L_ST = 131072;
constexpr u32 L_RED = 0;
constexpr int SPIN_CAP = 1 << 17;

struct Params {
  const u16 *x, *logA, *Aim, *Cr, *Ci, *logdt, *Dsk, *wg, *wu, *wd;
  u16 *out;
  char *ws;
};

__device__ __forceinline__ float bf_lo(u32 v) { return __uint_as_float(v << 16); }
__device__ __forceinline__ float bf_hi(u32 v) { return __uint_as_float(v & 0xffff0000u); }
__device__ __forceinline__ u32 pk_bf16(float a, float b) {
  b16x2 r; r[0] = (__bf16)a; r[1] = (__bf16)b; return __builtin_bit_cast(u32, r);
}
__device__ __forceinline__ float rbf(float a) { return bf_lo(pk_bf16(a, a)); }
__device__ __forceinline__ u32 pk_f16(float a, float b) {
  h16x2 r; r[0] = (_Float16)a; r[1] = (_Float16)b; return __builtin_bit_cast(u32, r);
}
template <typename T> __device__ __forceinline__ LAS T *lds(u32 off) { return (LAS T *)(size_t)off; }
__device__ __forceinline__ u32x2 tr_read(u32 addr) {
  return __builtin_bit_cast(u32x2, __builtin_amdgcn_ds_read_tr16_b64_v4i16((LAS s16x4 *)(size_t)addr));
}
__device__ __forceinline__ u32x4 cat(u32x2 a, u32x2 b) { u32x4 r; r[0] = a[0]; r[1] = a[1]; r[2] = b[0]; r[3] = b[1]; return r; }
__device__ __forceinline__ f32x16 mfma32(u32x4 a, u32x4 b, f32x16 c) {
  return __builtin_amdgcn_mfma_f32_32x32x16_bf16(__builtin_bit_cast(b16x8, a), __builtin_bit_cast(b16x8, b), c, 0, 0, 0);
}
__device__ __forceinline__ f32x4 mfma16h(u32x4 a, u32x4 b, f32x4 c) {
  return __builtin_amdgcn_mfma_f32_16x16x32_f16(__builtin_bit_cast(h16x8, a), __builtin_bit_cast(h16x8, b), c, 0, 0, 0);
}
__device__ __forceinline__ float dot2bf(u32 a, u32 b, float c) {
  return __builtin_amdgcn_fdot2_f32_bf16(__builtin_bit_cast(b16x2, a), __builtin_bit_cast(b16x2, b), c, false);
}
__device__ __forceinline__ void glds(u32 voff, const void *sbase, u32 ldst) {
  asm volatile("s_mov_b32 m0, %2\n\ts_nop 0\n\tglobal_load_lds_dwordx4 %0, %1" ::"v"(voff), "s"(sbase), "s"(ldst) : "memory");
}
__device__ __forceinline__ void glds_sc1(u32 voff, const void *sbase, u32 ldst) {
  asm volatile("s_mov_b32 m0, %2\n\ts_nop 0\n\tglobal_load_lds_dwordx4 %0, %1 sc1" ::"v"(voff), "s"(sbase), "s"(ldst) : "memory");
}
#define WAIT_VM(n) asm volatile("s_waitcnt vmcnt(" #n ")" ::: "memory")
#define WAIT_LGKM0() asm volatile("s_waitcnt lgkmcnt(0)" ::: "memory")
#define BAR() do { asm volatile("s_waitcnt lgkmcnt(0)\n\ts_barrier" ::: "memory"); } while (0)
#define SCHED() __builtin_amdgcn_sched_barrier(0)
#define PIN(x) asm volatile("" : "+v"(x))
#define RAWBAR() asm volatile("s_barrier" ::: "memory")
template <int N> __device__ __forceinline__ void wait_vm() { asm volatile("s_waitcnt vmcnt(%0)" ::"n"(N) : "memory"); }
template <int I> struct IC { static constexpr int v = I; };
template <int B, int E, typename Fn> __device__ __forceinline__ void static_for(Fn &&f) {
  if constexpr (B < E) { f(IC<B>{}); static_for<B + 1, E>(f); }
}
constexpr int G1_OPS[] = {-8, -7, -6, -5, -4, -3, -2, -1, 2, 6, 10, 14, 18, 22, 26, 30,
                          27, 57, 89, 121,
                          62, 94, 126,
                          97, 118, 120,
                          34, 38, 42, 46, 50, 54, 58, 63, 65, 67, 69, 71, 73, 75, 78, 80, 82, 85,
                          96, 98, 100, 102, 104, 106, 108, 110, 112, 114};
constexpr int G1_TAKE[] = {66, 69, 72, 98, 101, 104, 107, 110, 113, 116, 119, 122, 124, 126};
constexpr int g1_take(int p) { for (int i = 0; i < 14; i++) if (G1_TAKE[i] == p || G1_TAKE[i] + 1 == p) return 2 * i + p - G1_TAKE[i]; return -1; }
#define XN 12
#define LOOK 3
constexpr int EPI0 = 5;
constexpr int g1_younger(int x, int p) { int n = 0; for (int q : G1_OPS) n += (q > x && q < p); return n; }
constexpr int g1_find(int first, int cnt, int p) { for (int i = 0; i < cnt; i++) if (G1_OPS[first + i] == p) return i; return -1; }
constexpr int wd_slot(int u) { return u < 4 ? 16 + u : u < 12 ? u - 4 : u < 16 ? u + 4 : u < 18 ? u - 16 : u < 22 ? u - 10 : u - 20; }

template <int CTRL> __device__ __forceinline__ float dpp(float v) {
  return __uint_as_float(__builtin_amdgcn_update_dpp(0u, __float_as_uint(v), CTRL, 0xf, 0xf, true));
}
__device__ __forceinline__ float wave_sum(float v) {
  v += dpp<0xB1>(v); v += dpp<0x4E>(v); v += dpp<0x141>(v); v += dpp<0x140>(v);
  auto a = __builtin_amdgcn_permlane16_swap(__float_as_uint(v), __float_as_uint(v), false, false);
  v = __uint_as_float(a[0]) + __uint_as_float(a[1]);
  auto c = __builtin_amdgcn_permlane32_swap(__float_as_uint(v), __float_as_uint(v), false, false);
  return __uint_as_float(c[0]) + __uint_as_float(c[1]);
}
__device__ __forceinline__ float wave_max(float v) {
  v = fmaxf(v, dpp<0xB1>(v)); v = fmaxf(v, dpp<0x4E>(v)); v = fmaxf(v, dpp<0x141>(v)); v = fmaxf(v, dpp<0x140>(v));
  auto a = __builtin_amdgcn_permlane16_swap(__float_as_uint(v), __float_as_uint(v), false, false);
  v = fmaxf(__uint_as_float(a[0]), __uint_as_float(a[1]));
  auto c = __builtin_amdgcn_permlane32_swap(__float_as_uint(v), __float_as_uint(v), false, false);
  return fmaxf(__uint_as_float(c[0]), __uint_as_float(c[1]));
}
struct cf { float r, i; };
__device__ __forceinline__ cf cmul(cf a, cf b) { return {a.r * b.r - a.i * b.i, a.r * b.i + a.i * b.r}; }
__device__ __forceinline__ cf cexp_at(float lr, float rev, float pos) {
  float e = __builtin_amdgcn_exp2f(lr * pos), f = __builtin_amdgcn_fractf(rev * pos);
  return {e * __builtin_amdgcn_cosf(f), e * __builtin_amdgcn_sinf(f)};
}

#define STAMP(i)

__global__ void __launch_bounds__(512, 2) fused(Params p) {
  extern __shared__ __attribute__((aligned(1024))) char smem_[];
  const u32 tid = threadIdx.x, lane = tid & 63;
  const u32 w = __builtin_amdgcn_readfirstlane(tid >> 6);
  const u32 b = blockIdx.x, xcd = b & 7, jj = b >> 3;
  char *const ws = p.ws;
  STAMP(0);
  const bool g1 = jj < 28;
  const u32 panel = (xcd & 4) * 2 + (jj < 24 ? jj & 7 : (xcd & 1) * 4 + (jj & 3));
  const u32 ft = 7 * ((xcd >> 1) & 1) + (jj < 24 ? (xcd & 1) * 4 + (jj >> 3) : 3);
  const u32 rot1 = (jj >> 3) * 8;
  u32 tag = 0;

  u32x4 W[32];
  const u32 w1_voff = ((lane >> 4) * FF + ft * 128) * 2 + ((((lane & 15) >> 1) ^ ((lane >> 4) | ((w & 1) << 2))) * 32) + (lane & 1) * 16;
  const char *const w1_base = (const char *)((w & 1) ? p.wu : p.wg) + (size_t)((w >> 1) * 4) * (FF * 2);
  auto w1_one = [&](u32 ks, u32 dst) {
    glds(w1_voff, w1_base + (size_t)((ks + rot1) & 31) * 16 * (FF * 2), dst + w * 1024);
  };
  const u32 g4 = lane >> 4, q4 = (lane >> 2) & 3, p4 = lane & 3;
  const u32 w1_rd = (g4 >> 1) * 2048 + (g4 & 1) * 1024 + q4 * 256 + ((w ^ (q4 | ((g4 & 1) << 2))) * 32) + p4 * 8;

  const u32 cp = xcd * 32 + jj, d0 = 2 * cp;
  const u32 ch = w >> 2, job = w & 3;
  u32x4 xr; u32 pl, pa, pr, pi;
  {
    const u32 o1 = (8 * b + w) * (DM * 2) + lane * 16;
    asm volatile("global_load_dwordx4 %0, %1, %2" : "=v"(xr) : "v"(o1), "s"(p.x));
    const u32 o2 = ((d0 + ch) * NMODE + 2 * lane) * 2;
    asm volatile("global_load_dword %0, %1, %2" : "=v"(pl) : "v"(o2), "s"(p.logA));
    asm volatile("global_load_dword %0, %1, %2" : "=v"(pa) : "v"(o2), "s"(p.Aim));
    asm volatile("global_load_dword %0, %1, %2" : "=v"(pr) : "v"(o2), "s"(p.Cr));
    asm volatile("global_load_dword %0, %1, %2" : "=v"(pi) : "v"(o2), "s"(p.Ci));
  }
  const u32 ldt2 = *(const CAS u32 *)(p.logdt + d0), dsk2 = *(const CAS u32 *)(p.Dsk + d0);
  asm volatile("s_load_dword %0, %1, 0x0\n\ts_waitcnt lgkmcnt(0)" : "=s"(tag) : "s"(ws) : "memory");
  tag += 1u;
  auto poll = [&](u32 fo) {
#pragma unroll 1
    for (int spin = 0; spin < SPIN_CAP; spin++) {
      u32 f;
      asm volatile("global_load_dword %0, %1, %2 sc1\n\ts_waitcnt vmcnt(0)" : "=&v"(f) : "v"(fo), "s"(ws) : "memory");
      if (__all(f == tag)) break;
      __builtin_amdgcn_s_sleep(1);
    }
  };
  u32 vidx[8];
  {
    const u32 l0 = 8 * (tid & 255);
#pragma unroll
    for (int e = 0; e < 8; e++) {
      const u32 l = l0 + e, bits = pk_bf16((float)l, 0.f) & 0xffffu;
      vidx[e] = L_VT + (tid >> 8) * VT_CH + 2 * (l < 256 ? l : bits - (0x4380u - 256u));
    }
#pragma unroll
    for (int e = 0; e < 8; e++) asm volatile("" : "+v"(vidx[e]));
  }
  u32 junk;
  {
    u64 pc; u32 sz;
    asm volatile(".Lcode_begin:\n\ts_getpc_b64 %0\n\ts_mov_b32 %1, .Lcode_end-.Lcode_begin" : "=s"(pc), "=s"(sz));
    const u32 co = min((jj * 8 + w) * 256 + lane * 4, sz);
    asm volatile("global_load_dword %0, %1, %2" : "=v"(junk) : "v"(co), "s"(pc));
#pragma unroll
    for (int i = 0; i < 4; i++) w1_one(i, L_W0 + i * 8192);
    const u32 gw = b * 8 + w, po = (gw < 1792 ? gw : gw - 1024) * 1024 + lane * 16;
    glds(po, p.wg, L_W0 + 65536 + w * 1024); glds(po, p.wu, L_W0 + 65536 + w * 1024); glds(po, p.wd, L_W0 + 65536 + w * 1024);
  }
  asm volatile("s_waitcnt vmcnt(12)" : "+v"(xr));
  {
    *lds<u32x4>(L_TX + w * T_STR + lane * 16) = xr;
    float v[8];
#pragma unroll
    for (int i = 0; i < 4; i++) { v[2 * i] = bf_lo(xr[i]); v[2 * i + 1] = bf_hi(xr[i]); }
    const float m = wave_sum(((v[0] + v[1]) + (v[2] + v[3])) + ((v[4] + v[5]) + (v[6] + v[7]))) * (1.f / DM);
    float q0 = 0, q1 = 0;
#pragma unroll
    for (int i = 0; i < 4; i++) { const float t0 = v[i] - m, t1 = v[i + 4] - m; q0 += t0 * t0; q1 += t1 * t1; }
    const float var = rbf(wave_sum(q0 + q1) * (1.f / DM));
    const float rs = __builtin_amdgcn_rcpf(rbf(__builtin_amdgcn_sqrtf(rbf(var + 1e-5f)))), mb = rbf(m);
    u32 uu[4];
#pragma unroll
    for (int i = 0; i < 4; i++) {
      const u32 a = pk_bf16(v[2 * i] - mb, v[2 * i + 1] - mb);
      const u32 u = pk_bf16(bf_lo(a) * rs, bf_hi(a) * rs);
      uu[i] = pk_f16(bf_lo(u), bf_hi(u));
    }
    u32x4 o; o[0] = uu[0]; o[1] = uu[1]; o[2] = uu[2]; o[3] = uu[3];
    *lds<u32x4>(L_T + w * T_STR + lane * 16) = o;
  }
  BAR();
  {
    __amdgpu_buffer_rsrc_t rs = __builtin_amdgcn_make_buffer_rsrc(ws + WS_EX, 0, 16u << 20, 0x00020000);
    const u32 ra = L_T + (4 * (p4 & 1) + 3 - q4) * T_STR + (64 * w + 8 * g4 + 4 * (p4 >> 1)) * 2;
    const u32 rx = L_TX + (3 - q4) * T_STR + (64 * w + 16 * g4 + 4 * p4) * 2;
    const u32 so = ((lane >> 3) * 256 + b) * 128 + (lane & 7) * 16;
    u32x2 r[2];
#pragma unroll
    for (int i = 0; i < 2; i++) r[i] = tr_read(ra + i * 64);
    const u32x4 xt = cat(tr_read(rx + 4 * T_STR), tr_read(rx));
#pragma unroll
    for (int i = 0; i < 2; i++) {
      u32x4 o; o[0] = r[i][0]; o[1] = tag; o[2] = r[i][1]; o[3] = tag;
      __builtin_amdgcn_raw_buffer_store_b128(o, rs, so + (16 * w + 8 * i) * 32768, 0, 16);
    }
    __builtin_amdgcn_raw_buffer_store_b128(xt, rs, so + (WS_XT - WS_EX) + 8 * w * 32768, 0, 16);
  }
  STAMP(1);
  asm volatile("s_waitcnt vmcnt(11)" : "+v"(pl), "+v"(pa), "+v"(pr), "+v"(pi));
  const float dt = rbf(__expf(ch ? bf_hi(ldt2) : bf_lo(ldt2)));
  {
    const float LOG2E = 1.4426950408889634f, INV2PI = 0.15915494309189535f;
    float lr[2], rv[2];
    lr[0] = -rbf(__expf(bf_lo(pl))) * dt * LOG2E; lr[1] = -rbf(__expf(bf_hi(pl))) * dt * LOG2E;
    rv[0] = bf_lo(pa) * dt * INV2PI; rv[1] = bf_hi(pa) * dt * INV2PI;
    const u32 wb = L_XY + ch * XY_CH + lane * 8;
    auto put = [&](int row, cf a, cf c) {
      u32x2 v; v[0] = pk_f16(a.r, a.i); v[1] = pk_f16(c.r, c.i);
      *lds<u32x2>(wb + row * XY_STR) = v;
    };
    auto G = [&](int i) { w1_one(4 + i, L_W0 + (4 + i) * 8192); if (i >= 2) w1_one(6 + i, L_W0 + (6 + i) * 8192); };
    if (job < 2) {
      G(0);
      cf s0 = cexp_at(lr[0], -rv[0], 8.f), s1 = cexp_at(lr[1], -rv[1], 8.f);
      const float p0 = job ? 128.f : 0.f;
      cf z0 = cexp_at(lr[0], -rv[0], p0), z1 = cexp_at(lr[1], -rv[1], p0);
      const u32 wb2 = wb + job * 16 * XY_STR;
#pragma unroll
      for (int i = 0; i < 16; i++) {
        u32x2 v; v[0] = pk_f16(z0.r, z0.i); v[1] = pk_f16(z1.r, z1.i);
        *lds<u32x2>(wb2 + i * XY_STR) = v;
        if (i < 15) { z0 = cmul(z0, s0); z1 = cmul(z1, s1); }
        if (i == 2 || i == 7 || i == 12) G(1 + i / 5);
        SCHED();
      }
    } else {
      G(0);
      cf c0 = {bf_lo(pr), bf_lo(pi)}, c1 = {bf_hi(pr), bf_hi(pi)};
      const float mx = wave_max(fmaxf(fmaxf(fabsf(c0.r), fabsf(c0.i)), fmaxf(fabsf(c1.r), fabsf(c1.i))));
      const int ex = __builtin_amdgcn_frexp_expf(mx);
      const float sc = __builtin_amdgcn_ldexpf(1.f, -ex);
      c0.r *= sc; c0.i *= sc; c1.r *= sc; c1.i *= sc;
      G(1);
      if (job == 2) {
        if (lane == 0) *lds<float>(L_MISC + ch * 4) = __builtin_amdgcn_ldexpf(2.f * dt, ex);
        cf s0 = cexp_at(lr[0], rv[0], 1.f), s1 = cexp_at(lr[1], rv[1], 1.f);
        cf z0 = c0, z1 = c1;
#pragma unroll
        for (int i = 0; i < 8; i++) { put(32 + i, z0, z1); if (i < 7) { z0 = cmul(z0, s0); z1 = cmul(z1, s1); } if (i == 2 || i == 7) G(2 + i / 5); SCHED(); }
        s0 = cmul(s0, s0); s1 = cmul(s1, s1);
        z0 = cmul(c0, cexp_at(lr[0], rv[0], 256.f)); z1 = cmul(c1, cexp_at(lr[1], rv[1], 256.f));
#pragma unroll
        for (int i = 0; i < 4; i++) { put(40 + i, z0, z1); if (i < 3) { z0 = cmul(z0, s0); z1 = cmul(z1, s1); } }
      } else {
        cf f0 = cexp_at(lr[0], rv[0], 4.f), f1 = cexp_at(lr[1], rv[1], 4.f);
        cf s0 = cexp_at(lr[0], rv[0], 256.f), s1 = cexp_at(lr[1], rv[1], 256.f);
        G(2); SCHED();
        cf z0 = cmul(c0, cexp_at(lr[0], rv[0], 512.f)), z1 = cmul(c1, cexp_at(lr[1], rv[1], 512.f));
        put(44, z0, z1); put(45, cmul(z0, f0), cmul(z1, f1));
        z0 = cmul(z0, s0); z1 = cmul(z1, s1);
        put(46, z0, z1); put(47, cmul(z0, f0), cmul(z1, f1));
#pragma unroll
        for (int i = 0; i < 5; i++) { z0 = cmul(z0, s0); z1 = cmul(z1, s1); put(48 + i, z0, z1); if (i == 1) G(3); SCHED(); }
      }
    }
  }
  asm volatile("s_waitcnt vmcnt(0)" : "+v"(junk));
  BAR();
  STAMP(2);
  if (tid == 0) __hip_atomic_store((GAS u32 *)(ws + WS_FLAG1) + b, tag, __ATOMIC_RELAXED, __HIP_MEMORY_SCOPE_AGENT);
  u32x4 ex[2];
  const char *const exb = ws + WS_EX + (size_t)(cp >> 1) * 32768 + (cp & 1) * 32;
  const u32 exo = (tid >> 2) * 128 + (tid & 2) * 32 + (tid & 1) * 16;
  asm volatile("global_load_dwordx4 %0, %1, %2 sc1" : "=v"(ex[0]) : "v"(exo), "s"(exb));
  asm volatile("global_load_dwordx4 %0, %1, %2 sc1" : "=v"(ex[1]) : "v"(exo), "s"(exb + 16384));
  {
    const u32 rb = job >> 1, cb = job & 1;
    const u32 xa = L_XY + ch * XY_CH + (32 + 16 * rb + (lane & 15)) * XY_STR + g4 * 16;
    const u32 ya = L_XY + ch * XY_CH + (16 * cb + (lane & 15)) * XY_STR + g4 * 16;
    u32x4 fa[8], fb[8];
#pragma unroll
    for (int ks = 0; ks < 8; ks++) { fa[ks] = *lds<u32x4>(xa + ks * 64); fb[ks] = *lds<u32x4>(ya + ks * 64); }
    SCHED();
    u32 wr = L_W0 + w1_rd;
    PIN(wr);
#pragma unroll
    for (int ks = 0; ks < 10; ks++) { const u32 a = (ks < 8 ? wr : wr + 65536) + (ks & 7) * 8192; W[ks] = cat(tr_read(a), tr_read(a + 4096)); }
    SCHED();
    const u32 j = 16 * cb + (lane & 15);
    u32 i0, is, i2;
    if (rb == 0) {
      i0 = g4 < 2 ? 4 * g4 + 8 * j : (g4 == 2 ? 256 + 4 * j : 384 + 2 * j); is = 1; i2 = g4 == 3 ? 64 : 2;
    } else {
      i0 = g4 == 0 ? 512 + j : 640 + j; is = g4 == 0 ? 32 : 0; i2 = 2 * is;
    }
    f32x4 a0 = {0, 0, 0, 0}, a1 = {0, 0, 0, 0};
#pragma unroll
    for (int ks = 0; ks < 8; ks += 2) { a0 = mfma16h(fa[ks], fb[ks], a0); a1 = mfma16h(fa[ks + 1], fb[ks + 1], a1); }
    a0 += a1;
    const u32 vb = L_VT + ch * VT_CH;
    if (rb == 0 || g4 == 0) {
#pragma unroll
      for (int r = 0; r < 4; r++) *lds<_Float16>(vb + 2 * (i0 + (r & 1) * is + (r >> 1) * i2)) = (_Float16)a0[r];
    } else if (g4 == 1) {
      *lds<_Float16>(vb + 2 * i0) = (_Float16)a0[0];
    }
  }
  STAMP(4);
  asm volatile("s_waitcnt vmcnt(0)" : "+v"(ex[0]), "+v"(ex[1]));
#pragma unroll 1
  for (int spin = 0; spin < SPIN_CAP; spin++) {
    if (__all(ex[0][1] == tag && ex[0][3] == tag && ex[1][1] == tag && ex[1][3] == tag)) break;
    __builtin_amdgcn_s_sleep(1);
    asm volatile("global_load_dwordx4 %0, %2, %3 sc1\n\tglobal_load_dwordx4 %1, %2, %4 sc1\n\ts_waitcnt vmcnt(0)"
                 : "=&v"(ex[0]), "=&v"(ex[1]) : "v"(exo), "s"(exb), "s"(exb + 16384) : "memory");
  }
  BAR();
  STAMP(5);
#pragma unroll
  for (int i = 0; i < 4; i++) w1_one(10 + i, L_W0 + i * 8192);
  {
    const u32 ab = L_UR + 3 * UR_STR + (tid & 1) * UR_CH + (2044 - 1024 - 8 * (tid >> 2) - 2 * (tid & 2)) * 2;
#pragma unroll
    for (int i = 0; i < 2; i++) { u32x2 v; v[0] = ex[i][0]; v[1] = ex[i][2]; *lds<u32x2>(ab + (1 - i) * 2048) = v; }
  }
  {
    u32x4 kv;
    _Float16 t[8];
#pragma unroll
    for (int e = 0; e < 8; e++) t[e] = *lds<_Float16>(vidx[e]);
#pragma unroll
    for (int e = 0; e < 4; e++) { h16x2 t2; t2[0] = t[2 * e]; t2[1] = t[2 * e + 1]; kv[e] = __builtin_bit_cast(u32, t2); }
    const u32 kb = L_K + (tid >> 8) * K_CH + (tid & 255) * 16;
    u32x2 lo, hi; lo[0] = kv[0]; lo[1] = kv[1]; hi[0] = kv[2]; hi[1] = kv[3];
    *lds<u32x2>(kb + 120) = lo; *lds<u32x2>(kb + 128) = hi;
    *lds<u32x4>(kb + 4224 + 112) = kv;
    const u32x2 z2 = {0, 0};
    u32 za;
    if (tid < 64) {
      const u32 c = tid >> 5, i = tid & 31;
      za = L_K + c * K_CH + (i < 15 ? i * 8 : 4224 + (i < 29 ? i - 15 : 0) * 8);
    } else {
      const u32 i = tid < 448 ? tid - 64 : 0, c = i / 48;
      za = L_UR + c * UR_STR + 4096 + (i - c * 48) * 8;
    }
    *lds<u32x2>(za) = z2;
  }
#pragma unroll
  for (int i = 4; i < 8; i++) w1_one(10 + i, L_W0 + i * 8192);
  BAR();
  {
    const u32 ub = L_UR + (tid >> 8) * UR_CH + (tid & 255) * 16;
    const u32x4 a = *lds<u32x4>(ub + 3 * UR_STR);
    const u32x2 c = *lds<u32x2>(ub + 3 * UR_STR + 16);
    u32x4 o;
    o[0] = __builtin_amdgcn_alignbit(a[1], a[0], 16); o[1] = __builtin_amdgcn_alignbit(a[2], a[1], 16);
    o[2] = __builtin_amdgcn_alignbit(a[3], a[2], 16); o[3] = __builtin_amdgcn_alignbit(c[0], a[3], 16);
    *lds<u32x4>(ub + 2 * UR_STR) = o;
    u32x4 o1; o1[0] = a[1]; o1[1] = a[2]; o1[2] = a[3]; o1[3] = c[0];
    *lds<u32x4>(ub + UR_STR) = o1;
    u32x4 o0; o0[0] = o[1]; o0[1] = o[2]; o0[2] = o[3]; o0[3] = __builtin_amdgcn_alignbit(c[1], c[0], 16);
    *lds<u32x4>(ub) = o0;
  }
  WAIT_VM(0);
  BAR();
  STAMP(6);
  u32 f1;
  const u32 xp = 255 - 32 * w - (lane >> 1);
  {
    u32 wr = L_W0 + w1_rd;
    PIN(wr);
#pragma unroll
    for (int ks = 0; ks < 8; ks++) W[10 + ks] = cat(tr_read(wr + ks * 8192), tr_read(wr + ks * 8192 + 4096));
    SCHED();
  }
  const u32 pg = w & 1, cg = (w >> 1) & 1;
  f32x4 ya[4];
#pragma unroll
  for (int i = 0; i < 4; i++) ya[i] = f32x4{0, 0, 0, 0};
  {
    const u32 c16 = lane & 15;
    const u32 ka = L_K + ch * K_CH + (c16 & 1) * 4224 + (c16 >> 1) * 16 + g4 * 16 + cg * 4 * 64;
    const u32 ua = L_UR + ch * UR_CH + (c16 & 3) * UR_STR + (1984 - 1792 - 64 * (c16 >> 2) + 8 * g4) * 2 + cg * 4 * 64;
    u32x4 kf[2][8], uf[2][8];
    auto rd1 = [&](int s, int j) {
      if (j >= 1 && j <= 8) uf[s & 1][8 - j] = *lds<u32x4>(ua + (j - 1) * 512 + s * 64);
      else kf[s & 1][j ? j - 8 : 0] = *lds<u32x4>(ka + (j ? j - 8 : 0) * 512 + s * 64);
    };
    auto rd = [&](int s, int i) { rd1(s, 2 * i); rd1(s, 2 * i + 1); };
    auto side = [&](int s, int i) {
      const int n = s * 8 + i;
      if (n == 0) asm volatile("global_load_dword %0, %1, %2 sc1" : "=v"(f1) : "v"(xp * 4), "s"(ws + WS_FLAG1));
      if (n == 1 || n == 4) w1_one(26 + n / 3, L_W0 + (8 + n / 3) * 8192);
      if (n == 6) RAWBAR();
      if (n == XN) {
        asm volatile("s_waitcnt vmcnt(%1)" : "+v"(f1) : "n"(2 + (XN - 5) / 3));
        if (!__all(f1 == tag)) poll(WS_FLAG1 + xp * 4);
        glds_sc1(xp * 128 + (lane & 1) * 16, ws + WS_XT + (size_t)(cp >> 2) * 32768 + (cp & 3) * 32, L_XS + w * 1024);
      }
      if (n >= 7 && (n - 7) % 3 == 0 && n < 31) w1_one(18 + (n - 7) / 3, L_W0 + (n - 7) / 3 * 8192);
    };
#pragma unroll
    for (int i = 0; i < 8; i++) rd(0, i);
    if (pg == 0) {
#pragma unroll
      for (int s = 0; s < 4; s++) {
#pragma unroll
        for (int i = 0; i < 8; i++) {
          if (s < 3) rd(s + 1, i);
          side(s, i);
          ya[3] = mfma16h(uf[s & 1][7 - i], kf[s & 1][i], ya[3]);
          if (i <= 4) ya[2] = mfma16h(uf[s & 1][4 - i], kf[s & 1][i], ya[2]);
          if (i <= 3) ya[1] = mfma16h(uf[s & 1][3 - i], kf[s & 1][i], ya[1]);
          if (i <= 0) ya[0] = mfma16h(uf[s & 1][0 - i], kf[s & 1][i], ya[0]);
          SCHED();
        }
      }
    } else {
#pragma unroll
      for (int s = 0; s < 4; s++) {
#pragma unroll
        for (int i = 0; i < 8; i++) {
          if (s < 3) rd(s + 1, i);
          side(s, i);
          if (i <= 6) ya[3] = mfma16h(uf[s & 1][6 - i], kf[s & 1][i], ya[3]);
          if (i <= 5) ya[2] = mfma16h(uf[s & 1][5 - i], kf[s & 1][i], ya[2]);
          if (i <= 2) ya[1] = mfma16h(uf[s & 1][2 - i], kf[s & 1][i], ya[1]);
          if (i <= 1) ya[0] = mfma16h(uf[s & 1][1 - i], kf[s & 1][i], ya[0]);
          SCHED();
        }
      }
    }
  }
  wait_vm<8 - (XN - 5) / 3>();
  {
    const u32 yb = L_YP + w * 2048 + lane * 16;
    *lds<f32x4>(yb) = cg ? ya[0] : ya[2];
    *lds<f32x4>(yb + 1024) = cg ? ya[1] : ya[3];
  }
  BAR();
  STAMP(7);
  w1_one(28, L_W2); w1_one(29, L_W2 + 8192);
  {
    const u32 yb = L_YP + (w ^ 2) * 2048 + lane * 16;
    const float ksc = *lds<float>(L_MISC + ch * 4), dsk = ch ? bf_hi(dsk2) : bf_lo(dsk2);
    __amdgpu_buffer_rsrc_t rs = __builtin_amdgcn_make_buffer_rsrc(ws + WS_HT, 0, SEQ * DM * 2, 0x00020000);
    f32x4 o[2]; u32x2 uu[2], xx[2]; u32 t0[2];
#pragma unroll
    for (int i = 0; i < 2; i++) {
      o[i] = *lds<f32x4>(yb + i * 1024);
      t0[i] = 256 * (4 * cg + (i ? 3 - pg : pg)) + 4 * lane;
      uu[i] = *lds<u32x2>(L_UR + ch * UR_CH + 3 * UR_STR + (2044 - t0[i]) * 2);
      xx[i] = *lds<u32x2>(L_XS + ((2044 - t0[i]) >> 3) * 32 + ch * 16 + ((2044 - t0[i]) & 7) * 2);
    }
#pragma unroll
    for (int i = 0; i < 2; i++) {
      const f32x4 own = cg ? (i ? ya[3] : ya[2]) : (i ? ya[1] : ya[0]);
      const u32 uu0 = uu[i][0], uu1 = uu[i][1];
      const h16x2 ua = __builtin_bit_cast(h16x2, uu1), ub = __builtin_bit_cast(h16x2, uu0);
      const float uv[4] = {(float)ua[1], (float)ua[0], (float)ub[1], (float)ub[0]};
      const float xv[4] = {bf_hi(xx[i][1]), bf_lo(xx[i][1]), bf_hi(xx[i][0]), bf_lo(xx[i][0])};
      float hv[4];
#pragma unroll
      for (int r = 0; r < 4; r += 2) {
        const u32 y2 = pk_bf16((o[i][r] + own[r]) * ksc, (o[i][r + 1] + own[r + 1]) * ksc);
        const u32 d2 = pk_bf16(uv[r] * dsk, uv[r + 1] * dsk);
        const u32 z2 = pk_bf16(bf_lo(y2) + bf_lo(d2), bf_hi(y2) + bf_hi(d2));
        hv[r] = xv[r] + bf_lo(z2); hv[r + 1] = xv[r + 1] + bf_hi(z2);
      }
      u32x2 hv2; hv2[0] = pk_bf16(hv[0], hv[1]); hv2[1] = pk_bf16(hv[2], hv[3]);
      const u32 off = ((t0[i] >> 5) * DM + d0 + ch) * 64 + (t0[i] & 31) * 2;
      __builtin_amdgcn_raw_buffer_store_b64(hv2, rs, off, 0, 16);
    }
  }
  SCHED();
  if (b == 0 && tid == 0)
    __hip_atomic_store((GAS u32 *)(ws + WS_EPOCH), tag, __ATOMIC_RELAXED, __HIP_MEMORY_SCOPE_AGENT);
  BAR();
  f32x16 cs;
#pragma unroll
  for (int i = 0; i < 16; i++) cs[i] = 0;
  const u32x4 ones = {0x3f803f80u, 0x3f803f80u, 0x3f803f80u, 0x3f803f80u};
  w1_one(30, L_YP); w1_one(31, L_YP + 8192);
#pragma unroll
  for (int ks = 0; ks < 12; ks++) cs = mfma32(W[ks], ones, cs);
  SCHED();
  WAIT_VM(2);
  RAWBAR();
  if (tid == 0) __hip_atomic_store((GAS u32 *)(ws + WS_FLAG2) + b * 32, tag, __ATOMIC_RELAXED, __HIP_MEMORY_SCOPE_AGENT);
  STAMP(8);
  const u32 t32 = lane & 31;
  const u32 nt = jj & 7, tt = jj >> 3, pn2 = 2 * xcd + (tt >> 1), tgA = tt & 1, tgB = 3 - tgA;
  const u32 lv = lane * 16, rot2 = nt == 7 ? 0 : nt;
  auto ft_of = [&](int i) { const u32 k = (i % 7) + rot2; return 2 * (k >= 7 ? k - 7 : k) + (i < 7 ? tgA ^ 1 : tgA); };
  const u32 d_voff = (lane >> 3) * (DM * 2) + (((lane & 7) ^ (((lane >> 4) & 1) << 2)) * 16);
  const char *const wd_base = (const char *)p.wd + (size_t)(w * 16) * (DM * 2) + nt * 128;
  auto wd_one = [&](int u) {
    const u32 k = __builtin_amdgcn_readfirstlane((ft_of(u >> 1) << 17) + ((u & 1) << 13));
    glds(d_voff, wd_base + k, wd_slot(u) * 8192 + w * 1024);
  };
  u32x4 D[28];
  u32 d_rd[2][3];
#pragma unroll
  for (int h = 0; h < 2; h++) {
    d_rd[h][0] = w * 1024 + (((4 * (g4 >> 1) + q4) * 128 + h * 64 + (g4 & 1) * 32 + p4 * 8) ^ (((q4 >> 1) & 1) * 64));
    d_rd[h][1] = d_rd[h][0] + 65536; d_rd[h][2] = d_rd[h][0] + 131072;
  }
  auto wd_take = [&](int j, int h) {
    const u32 o = wd_slot(2 * j) * 8192;
    D[2 * j + h] = cat(tr_read(d_rd[h][o >> 16] + (o & 65535)), tr_read(d_rd[h][o >> 16] + (o & 65535) + 8192));
  };
  auto lane_again = []() {
    u32 l; asm volatile("v_mbcnt_lo_u32_b32 %0, -1, 0\n\tv_mbcnt_hi_u32_b32 %0, -1, %0" : "=v"(l)); return l;
  };
  auto foA_of = [&]() { return WS_FLAG3 + ((pn2 * 4 + tgA) * 16 + min(lane_again(), 13u)) * 128; };
  __amdgpu_buffer_rsrc_t prs = __builtin_amdgcn_make_buffer_rsrc(ws + WS_P, 0, SEQ * FF * 2, 0x00020000);
  __amdgpu_buffer_rsrc_t frs = __builtin_amdgcn_make_buffer_rsrc(ws + WS_FLAG3, 0, 131072, 0x00020000);
  const u32 OOB = 0x40000000u;
  const u32 p_off = g1 ? (panel * 4 * 112 + ft * 8 + w) * 1024 + lane * 16 : OOB;
  const u32 f_off = g1 && tid == 0 ? (panel * 64 + ft) * 128 : OOB;
  auto tg_of = [&](int g) { return g < 2 ? (u32)g : g ^ (ft & 1); };
  float e_m, e_ne, e_s2, e_g, e_u, e_e, e_o;
  u32 pko[4];
  u32x2 sg;
  const u32 sg_off = WS_ST + (panel * 128 + t32) * 8;
  auto st_take = [&](int tg) {
    PIN(sg);
#pragma unroll 1
    for (int spin = 0; spin < SPIN_CAP; spin++) {
      if (__all(sg[1] == tag)) break;
      __builtin_amdgcn_s_sleep(1);
      asm volatile("global_load_dwordx2 %0, %1, %2 sc1\n\ts_waitcnt vmcnt(0)" : "=&v"(sg) : "v"(sg_off), "s"(ws + tg_of(tg) * 256) : "memory");
    }
    e_m = bf_lo(sg[0]);
    const float sd = bf_hi(sg[0]);
    e_ne = -1.4426950408889634f * __builtin_amdgcn_rcpf(sd); e_s2 = sd * sd;
  };
  auto epi_step = [&](int ks, int tg, const f32x16 &prev) {
    if (ks == 0) st_take(tg);
    else if (ks >= 1 && ks < 25) {
      const int i = (ks - 1) / 3, ph = (ks - 1) % 3;
      if (ph == 0) {
        e_g = prev[i] - e_m * cs[i]; PIN(e_g);
        e_e = __builtin_amdgcn_exp2f(e_g * e_ne);
      } else if (ph == 1) {
        e_u = (prev[i + 8] - e_m * cs[i + 8]) * e_g;
        e_e = e_e * e_s2 + e_s2;
        PIN(e_u); PIN(e_e);
      } else {
        const float o = e_u * __builtin_amdgcn_rcpf(e_e);
        if (i & 1) { pko[i >> 1] = pk_bf16(e_o, o); PIN(pko[i >> 1]); } else { e_o = o; PIN(e_o); }
      }
    } else if (ks == 25) {
      u32x4 v; v[0] = pko[0]; v[1] = pko[1]; v[2] = pko[2]; v[3] = pko[3];
      __builtin_amdgcn_raw_buffer_store_b128(v, prs, p_off + tg_of(tg) * (112 * 1024), 0, 16);
    }
  };
  f32x16 acc[2];
  if (g1) {
    {
      u32 wr = L_W0 + w1_rd;
      PIN(wr);
#pragma unroll
      for (int ks = 0; ks < 8; ks++) W[18 + ks] = cat(tr_read(wr + ks * 8192), tr_read(wr + ks * 8192 + 4096));
      wr += 65536;
#pragma unroll
      for (int ks = 0; ks < 2; ks++) W[26 + ks] = cat(tr_read(wr + ks * 8192), tr_read(wr + ks * 8192 + 4096));
      wr = L_W2 + w1_rd;
      PIN(wr);
#pragma unroll
      for (int ks = 0; ks < 2; ks++) W[28 + ks] = cat(tr_read(wr + ks * 8192), tr_read(wr + ks * 8192 + 4096));
#pragma unroll
      for (int ks = 12; ks < 18; ks++) cs = mfma32(W[ks], ones, cs);
      SCHED();
      if (w == 0) WAIT_VM(1); else WAIT_VM(0);
      RAWBAR();
#pragma unroll
      for (int ks = 2; ks < 4; ks++) W[28 + ks] = cat(tr_read(wr + ks * 8192), tr_read(wr + ks * 8192 + 4096));
#pragma unroll
      for (int ks = 18; ks < 32; ks++) cs = mfma32(W[ks], ones, cs);
      SCHED();
    }
    STAMP(9);
    poll(WS_FLAG2 + (w * 32 + t32) * 128);
    BAR();
    STAMP(10);
    const char *const a_base = ws + WS_HT + (size_t)panel * 131072;
    auto a_one = [&](int i) { glds_sc1(lv, a_base + tg_of(i >> 2) * 32768 + (((i & 3) * 8 + w + rot1) & 31) * 1024, L_A + w * 1024 + i * 8192); };
#pragma unroll
    for (int i = 0; i < 8; i++) a_one(i);
    u32 ab = L_A + (4 * (g4 >> 1) + q4) * 64 + (g4 & 1) * 32 + p4 * 8;
    u32x4 F[LOOK + 1];
    auto frag = [&](u32 a) { return cat(tr_read(a), tr_read(a + 512)); };
    u32 fa;
    wait_vm<g1_younger(-8, 0)>();
    RAWBAR();
    u32 ab2 = ab + 65536;
    PIN(ab); PIN(ab2);
#pragma unroll
    for (int i = 0; i < LOOK; i++) F[i] = frag(ab + i * 1024);
    static_for<0, 128>([&](auto P_) {
      constexpr int P = decltype(P_)::v, tg = P >> 5, ks = P & 31;
      if constexpr (ks == 0) {
#pragma unroll
        for (int i = 0; i < 16; i++) acc[tg & 1][i] = 0;
      }
      if constexpr (P == 8 - LOOK) { wait_vm<g1_younger(-7, P)>(); RAWBAR(); }
      if constexpr (P == 16 - LOOK) { wait_vm<g1_younger(-6, P)>(); RAWBAR(); }
      if constexpr (P == 24 - LOOK) { wait_vm<g1_younger(-5, P)>(); RAWBAR(); }
      if constexpr (P == 32 - LOOK) { wait_vm<g1_younger(-1, P)>(); RAWBAR(); }
      if constexpr (P == 48) { wait_vm<g1_younger(14, P)>(); RAWBAR(); }
      if constexpr (P == 64) { wait_vm<g1_younger(30, P)>(); RAWBAR(); }
      if constexpr (P == 66) wait_vm<g1_younger(54, P)>();
      if constexpr (P == 96) { wait_vm<g1_younger(85, P)>(); RAWBAR(); }
      if constexpr (P == 116) wait_vm<g1_younger(102, P)>();
      if constexpr (P == 118) RAWBAR();
      if constexpr (P == 122) wait_vm<g1_younger(114, P)>();
      if constexpr (g1_find(23, 2, P) >= 0) __builtin_amdgcn_raw_buffer_store_b32(tag, frs, f_off, 2048 * g1_find(23, 2, P), 16);
      if constexpr (P == G1_OPS[25]) asm volatile("global_load_dword %0, %1, %2 sc1" : "=v"(fa) : "v"(foA_of()), "s"(ws));
      if constexpr (g1_find(8, 8, P) >= 0) a_one(8 + g1_find(8, 8, P));
      if constexpr (g1_find(16, 4, P) >= 0)
        asm volatile("global_load_dwordx2 %0, %1, %2 sc1" : "=v"(sg) : "v"(sg_off), "s"(ws + tg_of(g1_find(16, 4, P)) * 256));
      if constexpr (g1_find(26, 28, P) >= 0) wd_one(g1_find(26, 28, P));
      if constexpr (g1_take(P) >= 0) wd_take(g1_take(P) >> 1, g1_take(P) & 1);
      constexpr int n = P + LOOK;
      if constexpr (n < 128) F[n % (LOOK + 1)] = frag((n < 64 ? ab : ab2) + (n & 63) * 1024);
      acc[tg & 1] = mfma32(W[ks], F[P % (LOOK + 1)], acc[tg & 1]);
      if constexpr (tg > 0 && ks == EPI0) wait_vm<g1_younger(G1_OPS[16 + tg - 1], P + 1)>();
      if constexpr (tg > 0 && ks >= EPI0) epi_step(ks - EPI0, tg - 1, acc[(tg - 1) & 1]);
      SCHED();
      if constexpr (P == 31) STAMP(11);
      if constexpr (P == 63) STAMP(19);
      if constexpr (P == 95) STAMP(20);
      if constexpr (P == 127) STAMP(3);
    });
    wait_vm<g1_younger(121, 128)>();
    PIN(fa);
    st_take(3);
    if (!__all(fa == tag)) poll(foA_of());
    STAMP(12);
  } else {
    poll(WS_FLAG2 + (w * 32 + t32) * 128);
    RAWBAR();
    const u32 tgs = (2 * xcd + ((jj >> 1) & 1)) * 4 + (jj & 1);
    const char *base = ws + WS_HT + (size_t)tgs * 32768 + w * 4096;
#pragma unroll
    for (int i = 0; i < 8; i++) glds_sc1(lv, base + (i >> 2) * 65536 + (i & 3) * 1024, 65536 + (i >> 2) * 32768 + w * 4096 + (i & 3) * 1024);
    const u32 ab = 65536 + w * 4096 + (4 * (g4 >> 1) + q4) * 64 + (g4 & 1) * 32 + p4 * 8;
#pragma unroll
    for (int g = 0; g < 2; g++) {
      if (g == 0) WAIT_VM(4); else WAIT_VM(0);
      float s_s = 0, s_q = 0;
#pragma unroll
      for (int i = 0; i < 4; i++) {
        const u32x4 f = cat(tr_read(ab + g * 32768 + i * 1024), tr_read(ab + g * 32768 + i * 1024 + 512));
#pragma unroll
        for (int k = 0; k < 4; k++) { s_s = dot2bf(f[k], 0x3f803f80u, s_s); s_q = dot2bf(f[k], f[k], s_q); }
      }
      const auto sw = __builtin_amdgcn_permlane32_swap(__float_as_uint(s_s), __float_as_uint(s_q), false, false);
      *lds<float>(g * 2048 + w * 256 + lane * 4) = __uint_as_float(sw[0]) + __uint_as_float(sw[1]);
      BAR();
      float sp = 0;
#pragma unroll
      for (int i = 0; i < 8; i++) sp += *lds<float>(g * 2048 + i * 256 + lane * 4);
      const auto s2 = __builtin_amdgcn_permlane32_swap(__float_as_uint(sp), __float_as_uint(sp), false, false);
      const float mean = __uint_as_float(s2[0]) * (1.f / DM);
      const float var = rbf(fmaxf(__uint_as_float(s2[1]) * (1.f / DM) - mean * mean, 0.f));
      const u64 gr = ((u64)tag << 32) | pk_bf16(mean, __builtin_amdgcn_sqrtf(rbf(var + 1e-5f)));
      if (tid < 32) __hip_atomic_store((GAS u64 *)(ws + WS_ST) + (tgs + 2 * g) * 32 + lane, gr, __ATOMIC_RELAXED, __HIP_MEMORY_SCOPE_AGENT);
    }
    BAR();
#pragma unroll
    for (int u = 0; u < 12; u++) wd_one(u);
    WAIT_VM(0);
#pragma unroll
    for (int j = 0; j < 6; j++) { wd_take(j, 0); wd_take(j, 1); }
    WAIT_LGKM0();
#pragma unroll
    for (int u = 12; u < 28; u++) wd_one(u);
    poll(foA_of());
#pragma unroll
    for (int j = 6; j < 14; j++) { wd_take(j, 0); wd_take(j, 1); }
#pragma unroll
    for (int i = 0; i < 16; i++) acc[1][i] = 0;
    e_m = 0; e_ne = 0; e_s2 = 1.f;
  }

  {
    const char *const pbA = ws + WS_P + ((size_t)(pn2 * 4 + tgA) * 112 + w) * 1024;
    const char *const pbB = ws + WS_P + ((size_t)(pn2 * 4 + tgB) * 112 + w) * 1024;
    u32x4 PF[14], QF[14];
    const u32 ln = lane_again(), hh = ln >> 5, tl = ln & 31;
    const u32 foB = WS_FLAG3 + ((pn2 * 4 + tgB) * 16 + 2 * min(ln, 6u)) * 128;
    auto p_one = [&](const char *pb, int i, u32x4 &d) {
      const u32 j = __builtin_amdgcn_readfirstlane(ft_of(i) << 13);
      asm volatile("global_load_dwordx4 %0, %1, %2 sc1" : "=v"(d) : "v"(lv), "s"(pb + j));
    };
#pragma unroll
    for (int i = 0; i < 4; i++) p_one(pbA, i, PF[i]);
    SCHED();
    {
      const f32x2 m2 = {e_m, e_m}, ne2 = {e_ne, e_ne}, s22 = {e_s2, e_s2};
      u32x4 v;
#pragma unroll
      for (int i = 0; i < 8; i += 2) {
        const f32x2 g = f32x2{acc[1][i], acc[1][i + 1]} - m2 * f32x2{cs[i], cs[i + 1]};
        const f32x2 u = (f32x2{acc[1][i + 8], acc[1][i + 9]} - m2 * f32x2{cs[i + 8], cs[i + 9]}) * g;
        f32x2 e = g * ne2;
        e[0] = __builtin_amdgcn_exp2f(e[0]); e[1] = __builtin_amdgcn_exp2f(e[1]);
        e = e * s22 + s22;
        e[0] = __builtin_amdgcn_rcpf(e[0]); e[1] = __builtin_amdgcn_rcpf(e[1]);
        const f32x2 o = u * e;
        v[i >> 1] = pk_bf16(o[0], o[1]);
      }
      __builtin_amdgcn_raw_buffer_store_b128(v, prs, p_off + tg_of(3) * (112 * 1024), 0, 16);
    }
    SCHED();
    STAMP(13);
    wait_vm<5>();
    RAWBAR();
    __builtin_amdgcn_raw_buffer_store_b32(tag, frs, f_off, tg_of(2) * 2048, 16);
#pragma unroll
    for (int i = 4; i < 9; i++) p_one(pbA, i, PF[i]);
    const u32 own_n = nt * 64 + w * 8;
    u32 res[8];
    const u32 roA = WS_HT + ((pn2 * 4 + tgA) * DM + own_n + 4 * hh) * 64 + tl * 2;
    const u32 roB = WS_HT + ((pn2 * 4 + tgB) * DM + own_n + 4 * hh) * 64 + tl * 2;
    f32x16 ac[2];
    f32x4 r[2];
    auto red_put = [&](int t) {
#pragma unroll
      for (int o = 0; o < 8; o++) {
        f32x4 v; v[0] = ac[o >> 2][4 * (o & 3)]; v[1] = ac[o >> 2][4 * (o & 3) + 1]; v[2] = ac[o >> 2][4 * (o & 3) + 2]; v[3] = ac[o >> 2][4 * (o & 3) + 3];
        *lds<f32x4>(t * 65536 + o * 8192 + w * 1024 + lv) = v;
      }
    };
    auto red_get = [&](int t) {
      f32x4 v[8];
#pragma unroll
      for (int i = 0; i < 8; i++) v[i] = *lds<f32x4>(t * 65536 + w * 8192 + i * 1024 + lv);
      r[t] = ((v[0] + v[1]) + (v[2] + v[3])) + ((v[4] + v[5]) + (v[6] + v[7]));
    };
#pragma unroll
    for (int i = 0; i < 16; i++) { ac[0][i] = 0; ac[1][i] = 0; }
    u32 fb;
    static_for<0, 14>([&](auto I_) {
      constexpr int i = decltype(I_)::v;
      if constexpr (i == 4) {
        wait_vm<6>();
        RAWBAR();
        __builtin_amdgcn_raw_buffer_store_b32(tag, frs, f_off, tg_of(3) * 2048, 16);
        asm volatile("global_load_dword %0, %1, %2 sc1" : "=v"(fb) : "v"(foB + (tgA ^ 1) * 128), "s"(ws));
#pragma unroll
        for (int k = 9; k < 14; k++) p_one(pbA, k, PF[k]);
        STAMP(15);
      }
      if constexpr (i == 9) {
        wait_vm<5>();
        PIN(fb);
        if (!__all(fb == tag)) poll(foB + (tgA ^ 1) * 128);
#pragma unroll
        for (int k = 0; k < 7; k++) p_one(pbB, k, QF[k]);
      }
      wait_vm<i < 4 ? 10 - i : (i < 9 ? 15 - i : 20 - i)>();
      PIN(PF[i]);
      if constexpr (i == 0) STAMP(14);
      ac[0] = mfma32(D[2 * i], PF[i], ac[0]);
      ac[1] = mfma32(D[2 * i + 1], PF[i], ac[1]);
      SCHED();
    });
#define RES_LD(i, ro, o) asm volatile("global_load_ushort %0, %1, %2 offset:" #o " sc1" : "=v"(res[i]) : "v"(ro), "s"(ws));
    RES_LD(0, roA, 0) RES_LD(1, roA, 64) RES_LD(2, roA, 128) RES_LD(3, roA, 192)
    RES_LD(4, roB, 0) RES_LD(5, roB, 64) RES_LD(6, roB, 128) RES_LD(7, roB, 192)
#undef RES_LD
    BAR();
    red_put(0);
    STAMP(16);
    poll(foB + tgA * 128);
    asm volatile("" : "+v"(res[0]), "+v"(res[1]), "+v"(res[2]), "+v"(res[3]), "+v"(res[4]), "+v"(res[5]), "+v"(res[6]), "+v"(res[7]));
    STAMP(21);
#pragma unroll
    for (int i = 7; i < 14; i++) p_one(pbB, i, QF[i]);
    BAR();
#pragma unroll
    for (int i = 0; i < 16; i++) { ac[0][i] = 0; ac[1][i] = 0; }
    SCHED();
    static_for<0, 14>([&](auto I_) {
      constexpr int i = decltype(I_)::v;
      if constexpr (i >= 7) wait_vm<13 - i>();
      PIN(QF[i]);
      ac[0] = mfma32(D[2 * i], QF[i], ac[0]);
      ac[1] = mfma32(D[2 * i + 1], QF[i], ac[1]);
      if constexpr (i == 1) red_get(0);
      SCHED();
    });
    STAMP(17);
    red_put(1);
    BAR();
    red_get(1);
    u32 o4[4];
    {
      const u32 y0 = pk_bf16(r[0][0], r[0][1]), y1 = pk_bf16(r[0][2], r[0][3]), y2 = pk_bf16(r[1][0], r[1][1]), y3 = pk_bf16(r[1][2], r[1][3]);
      o4[0] = pk_bf16(bf_lo(res[0]) + bf_lo(y0), bf_lo(res[1]) + bf_hi(y0));
      o4[1] = pk_bf16(bf_lo(res[2]) + bf_lo(y1), bf_lo(res[3]) + bf_hi(y1));
      o4[2] = pk_bf16(bf_lo(res[4]) + bf_lo(y2), bf_lo(res[5]) + bf_hi(y2));
      o4[3] = pk_bf16(bf_lo(res[6]) + bf_lo(y3), bf_lo(res[7]) + bf_hi(y3));
    }
    const auto x0 = __builtin_amdgcn_permlane32_swap(o4[0], o4[2], false, false);
    const auto x1 = __builtin_amdgcn_permlane32_swap(o4[1], o4[3], false, false);
    u32x4 v; v[0] = x0[0]; v[1] = x1[0]; v[2] = x0[1]; v[3] = x1[1];
    *(u32x4 *)(p.out + (size_t)((pn2 * 4 + (hh ? tgB : tgA)) * 32 + tl) * DM + own_n) = v;
    STAMP(18);
  }
  asm volatile(".Lcode_end:");
}

extern "C" void kernel_launch(void *const *d_in, const int *in_sizes, int n_in, void *d_out, int out_size, void *d_ws,
                              size_t ws_size, hipStream_t stream) {
  Params p;
  p.x = (const u16 *)d_in[0]; p.logA = (const u16 *)d_in[1]; p.Aim = (const u16 *)d_in[2]; p.Cr = (const u16 *)d_in[3];
  p.Ci = (const u16 *)d_in[4]; p.logdt = (const u16 *)d_in[5]; p.Dsk = (const u16 *)d_in[6];
  p.wg = (const u16 *)d_in[8]; p.wu = (const u16 *)d_in[9]; p.wd = (const u16 *)d_in[10];
  p.out = (u16 *)d_out; p.ws = (char *)d_ws;
  (void)hipFuncSetAttribute((const void *)fused, hipFuncAttributeMaxDynamicSharedMemorySize, LDS_BYTES);
  fused<<<256, 512, LDS_BYTES, stream>>>(p);
}
```

```cpp
#include <hip/hip_runtime.h>
#include <hip/hip_bf16.h>
#include <stdint.h>

#define STAMPS 0

typedef unsigned short u16;
typedef unsigned int u32;
typedef unsigned long long u64;
typedef float f32x2 __attribute__((ext_vector_type(2)));
typedef float f32x4 __attribute__((ext_vector_type(4)));
typedef float f32x16 __attribute__((ext_vector_type(16)));
typedef _Float16 h16x8 __attribute__((ext_vector_type(8)));
typedef _Float16 h16x2 __attribute__((ext_vector_type(2)));
typedef __bf16 b16x8 __attribute__((ext_vector_type(8)));
typedef __bf16 b16x2 __attribute__((ext_vector_type(2)));
typedef short s16x4 __attribute__((ext_vector_type(4)));
typedef u32 u32x2 __attribute__((ext_vector_type(2)));
typedef u32 u32x4 __attribute__((ext_vector_type(4)));
#define LAS __attribute__((address_space(3)))
#define GAS __attribute__((address_space(1)))
#define CAS __attribute__((address_space(4)))

constexpr int SEQ = 2048, DM = 512, NMODE = 128, FF = 1792;

constexpr u32 WS_EPOCH = 0, WS_ST = 4096, WS_FLAG2 = 32768, WS_FLAG3 = 65536, WS_FLAG1 = 196608;
constexpr u32 WS_HT = 1u << 20, WS_P = 4u << 20, WS_DBG = 16u << 20, WS_EX = 12u << 20, WS_XT = 18u << 20;

constexpr u32 LDS_BYTES = 163840;
constexpr u32 XY_STR = 544, XY_CH = 53 * XY_STR;
constexpr u32 L_XY = 0;
constexpr u32 UR_STR = 4640, UR_CH = 4 * UR_STR;
constexpr u32 L_UR = 0;
constexpr u32 L_XS = 37120;
constexpr u32 K_CH = 8448;
constexpr u32 L_K = 45312;
constexpr u32 VT_CH = 1344;
constexpr u32 L_VT = 62464;
constexpr u32 L_MISC = 9216;
constexpr u32 L_YP = 65536;
constexpr u32 T_STR = 1056, L_T = 57664, L_TX = L_T + 8 * T_STR;
constexpr u32 L_W0 = 81920;
constexpr u32 L_W2 = 49152;
constexpr u32 L_A = 0;
constexpr u32 L_ST = 131072;
constexpr u32 L_RED = 0;
constexpr int SPIN_CAP = 1 << 17;

struct Params {
  const u16 *x, *logA, *Aim, *Cr, *Ci, *logdt, *Dsk, *wg, *wu, *wd;
  u16 *out;
  char *ws;
};

__device__ __forceinline__ float bf_lo(u32 v) { return __uint_as_float(v << 16); }
__device__ __forceinline__ float bf_hi(u32 v) { return __uint_as_float(v & 0xffff0000u); }
__device__ __forceinline__ u32 pk_bf16(float a, float b) {
  b16x2 r; r[0] = (__bf16)a; r[1] = (__bf16)b; return __builtin_bit_cast(u32, r);
}
__device__ __forceinline__ float rbf(float a) { return bf_lo(pk_bf16(a, a)); }
__device__ __forceinline__ u32 pk_f16(float a, float b) {
  h16x2 r; r[0] = (_Float16)a; r[1] = (_Float16)b; return __builtin_bit_cast(u32, r);
}
template <typename T> __device__ __forceinline__ LAS T *lds(u32 off) { return (LAS T *)(size_t)off; }
__device__ __forceinline__ u32x2 tr_read(u32 addr) {
  return __builtin_bit_cast(u32x2, __builtin_amdgcn_ds_read_tr16_b64_v4i16((LAS s16x4 *)(size_t)addr));
}
__device__ __forceinline__ u32x4 cat(u32x2 a, u32x2 b) { u32x4 r; r[0] = a[0]; r[1] = a[1]; r[2] = b[0]; r[3] = b[1]; return r; }
__device__ __forceinline__ f32x16 mfma32(u32x4 a, u32x4 b, f32x16 c) {
  return __builtin_amdgcn_mfma_f32_32x32x16_bf16(__builtin_bit_cast(b16x8, a), __builtin_bit_cast(b16x8, b), c, 0, 0, 0);
}
__device__ __forceinline__ f32x4 mfma16h(u32x4 a, u32x4 b, f32x4 c) {
  return __builtin_amdgcn_mfma_f32_16x16x32_f16(__builtin_bit_cast(h16x8, a), __builtin_bit_cast(h16x8, b), c, 0, 0, 0);
}
__device__ __forceinline__ float dot2bf(u32 a, u32 b, float c) {
  return __builtin_amdgcn_fdot2_f32_bf16(__builtin_bit_cast(b16x2, a), __builtin_bit_cast(b16x2, b), c, false);
}
__device__ __forceinline__ void glds(u32 voff, const void *sbase, u32 ldst) {
  asm volatile("s_mov_b32 m0, %2\n\ts_nop 0\n\tglobal_load_lds_dwordx4 %0, %1" ::"v"(voff), "s"(sbase), "s"(ldst) : "memory");
}
__device__ __forceinline__ void glds_sc1(u32 voff, const void *sbase, u32 ldst) {
  asm volatile("s_mov_b32 m0, %2\n\ts_nop 0\n\tglobal_load_lds_dwordx4 %0, %1 sc1" ::"v"(voff), "s"(sbase), "s"(ldst) : "memory");
}
#define WAIT_VM(n) asm volatile("s_waitcnt vmcnt(" #n ")" ::: "memory")
#define WAIT_LGKM0() asm volatile("s_waitcnt lgkmcnt(0)" ::: "memory")
#define BAR() do { asm volatile("s_waitcnt lgkmcnt(0)\n\ts_barrier" ::: "memory"); } while (0)
#define SCHED() __builtin_amdgcn_sched_barrier(0)
#define PIN(x) asm volatile("" : "+v"(x))
#define RAWBAR() asm volatile("s_barrier" ::: "memory")
template <int N> __device__ __forceinline__ void wait_vm() { asm volatile("s_waitcnt vmcnt(%0)" ::"n"(N) : "memory"); }
template <int I> struct IC { static constexpr int v = I; };
template <int B, int E, typename Fn> __device__ __forceinline__ void static_for(Fn &&f) {
  if constexpr (B < E) { f(IC<B>{}); static_for<B + 1, E>(f); }
}
constexpr int G1_OPS[] = {-8, -7, -6, -5, -4, -3, -2, -1, 2, 6, 10, 14, 18, 22, 26, 30,
                          27, 57, 89, 121,
                          62, 94, 126,
                          97, 118, 120,
                          34, 38, 42, 46, 50, 54, 58, 63, 65, 67, 69, 71, 73, 75, 78, 80, 82, 85,
                          96, 98, 100, 102, 104, 106, 108, 110, 112, 114};
constexpr int G1_TAKE[] = {66, 69, 72, 98, 101, 104, 107, 110, 113, 116, 119, 122, 124, 126};
constexpr int g1_take(int p) { for (int i = 0; i < 14; i++) if (G1_TAKE[i] == p || G1_TAKE[i] + 1 == p) return 2 * i + p - G1_TAKE[i]; return -1; }
#define XN 12
#define LOOK 3
constexpr int EPI0 = 5;
constexpr int g1_younger(int x, int p) { int n = 0; for (int q : G1_OPS) n += (q > x && q < p); return n; }
constexpr int g1_find(int first, int cnt, int p) { for (int i = 0; i < cnt; i++) if (G1_OPS[first + i] == p) return i; return -1; }
constexpr int wd_slot(int u) { return u < 4 ? 16 + u : u < 12 ? u - 4 : u < 16 ? u + 4 : u < 18 ? u - 16 : u < 22 ? u - 10 : u - 20; }

template <int CTRL> __device__ __forceinline__ float dpp(float v) {
  return __uint_as_float(__builtin_amdgcn_update_dpp(0u, __float_as_uint(v), CTRL, 0xf, 0xf, true));
}
__device__ __forceinline__ float wave_sum(float v) {
  v += dpp<0xB1>(v); v += dpp<0x4E>(v); v += dpp<0x141>(v); v += dpp<0x140>(v);
  auto a = __builtin_amdgcn_permlane16_swap(__float_as_uint(v), __float_as_uint(v), false, false);
  v = __uint_as_float(a[0]) + __uint_as_float(a[1]);
  auto c = __builtin_amdgcn_permlane32_swap(__float_as_uint(v), __float_as_uint(v), false, false);
  return __uint_as_float(c[0]) + __uint_as_float(c[1]);
}
__device__ __forceinline__ float wave_max(float v) {
  v = fmaxf(v, dpp<0xB1>(v)); v = fmaxf(v, dpp<0x4E>(v)); v = fmaxf(v, dpp<0x141>(v)); v = fmaxf(v, dpp<0x140>(v));
  auto a = __builtin_amdgcn_permlane16_swap(__float_as_uint(v), __float_as_uint(v), false, false);
  v = fmaxf(__uint_as_float(a[0]), __uint_as_float(a[1]));
  auto c = __builtin_amdgcn_permlane32_swap(__float_as_uint(v), __float_as_uint(v), false, false);
  return fmaxf(__uint_as_float(c[0]), __uint_as_float(c[1]));
}
struct cf { float r, i; };
__device__ __forceinline__ cf cmul(cf a, cf b) { return {a.r * b.r - a.i * b.i, a.r * b.i + a.i * b.r}; }
__device__ __forceinline__ cf cexp_at(float lr, float rev, float pos) {
  float e = __builtin_amdgcn_exp2f(lr * pos), f = __builtin_amdgcn_fractf(rev * pos);
  return {e * __builtin_amdgcn_cosf(f), e * __builtin_amdgcn_sinf(f)};
}

#define STAMP(i)

__global__ void __launch_bounds__(512, 2) fused(Params p) {
  extern __shared__ __attribute__((aligned(1024))) char smem_[];
  const u32 tid = threadIdx.x, lane = tid & 63;
  const u32 w = __builtin_amdgcn_readfirstlane(tid >> 6);
  const u32 b = blockIdx.x, xcd = b & 7, jj = b >> 3;
  char *const ws = p.ws;
  STAMP(0);
  const bool g1 = jj < 28;
  const u32 panel = (xcd & 4) * 2 + (jj < 24 ? jj & 7 : (xcd & 1) * 4 + (jj & 3));
  const u32 ft = 7 * ((xcd >> 1) & 1) + (jj < 24 ? (xcd & 1) * 4 + (jj >> 3) : 3);
  const u32 rot1 = (jj >> 3) * 8;
  u32 tag = 0;

  u32x4 W[32];
  const u32 w1_voff = ((lane >> 4) * FF + ft * 128) * 2 + ((((lane & 15) >> 1) ^ ((lane >> 4) | ((w & 1) << 2))) * 32) + (lane & 1) * 16;
  const char *const w1_base = (const char *)((w & 1) ? p.wu : p.wg) + (size_t)((w >> 1) * 4) * (FF * 2);
  auto w1_one = [&](u32 ks, u32 dst) {
    glds(w1_voff, w1_base + (size_t)((ks + rot1) & 31) * 16 * (FF * 2), dst + w * 1024);
  };
  const u32 g4 = lane >> 4, q4 = (lane >> 2) & 3, p4 = lane & 3;
  const u32 w1_rd = (g4 >> 1) * 2048 + (g4 & 1) * 1024 + q4 * 256 + ((w ^ (q4 | ((g4 & 1) << 2))) * 32) + p4 * 8;

  const u32 cp = xcd * 32 + jj, d0 = 2 * cp;
  const u32 ch = w >> 2, job = w & 3;
  u32x4 xr; u32 pl, pa, pr, pi;
  {
    const u32 o1 = (8 * b + w) * (DM * 2) + lane * 16;
    asm volatile("global_load_dwordx4 %0, %1, %2" : "=v"(xr) : "v"(o1), "s"(p.x));
    const u32 o2 = ((d0 + ch) * NMODE + 2 * lane) * 2;
    asm volatile("global_load_dword %0, %1, %2" : "=v"(pl) : "v"(o2), "s"(p.logA));
    asm volatile("global_load_dword %0, %1, %2" : "=v"(pa) : "v"(o2), "s"(p.Aim));
    asm volatile("global_load_dword %0, %1, %2" : "=v"(pr) : "v"(o2), "s"(p.Cr));
    asm volatile("global_load_dword %0, %1, %2" : "=v"(pi) : "v"(o2), "s"(p.Ci));
  }
  u32 junk;
  {
    u64 pc; u32 sz;
    asm volatile(".Lcode_begin:\n\ts_getpc_b64 %0\n\ts_mov_b32 %1, .Lcode_end-.Lcode_begin" : "=s"(pc), "=s"(sz));
    const u32 co = min((jj * 8 + w) * 256 + lane * 4, sz);
    asm volatile("global_load_dword %0, %1, %2" : "=v"(junk) : "v"(co), "s"(pc));
    {
      const u32 q = jj * 8 + w, m = q - 176;
      const char *tb = q < 160 ? ws + (size_t)q * 131072 : q < 176 ? (const char *)p.out + (q - 160) * 131072
                     : m < 14 ? (const char *)p.wg + m * 131072 : m < 28 ? (const char *)p.wu + (m - 14) * 131072
                     : m < 42 ? (const char *)p.wd + (m - 28) * 131072 : ws;
      glds((lane & 1) << 16, tb, L_W0 + 65536 + w * 1024);
    }
  }
  const u32 ldt2 = *(const CAS u32 *)(p.logdt + d0), dsk2 = *(const CAS u32 *)(p.Dsk + d0);
  asm volatile("s_load_dword %0, %1, 0x0\n\ts_waitcnt lgkmcnt(0)" : "=s"(tag) : "s"(ws) : "memory");
  tag += 1u;
  auto poll = [&](u32 fo) {
#pragma unroll 1
    for (int spin = 0; spin < SPIN_CAP; spin++) {
      u32 f;
      asm volatile("global_load_dword %0, %1, %2 sc1\n\ts_waitcnt vmcnt(0)" : "=&v"(f) : "v"(fo), "s"(ws) : "memory");
      if (__all(f == tag)) break;
      __builtin_amdgcn_s_sleep(1);
    }
  };
  u32 vidx[8];
  {
    const u32 l0 = 8 * (tid & 255);
#pragma unroll
    for (int e = 0; e < 8; e++) {
      const u32 l = l0 + e, bits = pk_bf16((float)l, 0.f) & 0xffffu;
      vidx[e] = L_VT + (tid >> 8) * VT_CH + 2 * (l < 256 ? l : bits - (0x4380u - 256u));
    }
#pragma unroll
    for (int e = 0; e < 8; e++) asm volatile("" : "+v"(vidx[e]));
  }
  {
#pragma unroll
    for (int i = 0; i < 4; i++) w1_one(i, L_W0 + i * 8192);
    const u32 gw = b * 8 + w, po = (gw < 1792 ? gw : gw - 1024) * 1024 + lane * 16;
    glds(po, p.wg, L_W0 + 65536 + w * 1024); glds(po, p.wu, L_W0 + 65536 + w * 1024); glds(po, p.wd, L_W0 + 65536 + w * 1024);
  }
  asm volatile("s_waitcnt vmcnt(13)" : "+v"(xr));
  {
    *lds<u32x4>(L_TX + w * T_STR + lane * 16) = xr;
    float v[8];
#pragma unroll
    for (int i = 0; i < 4; i++) { v[2 * i] = bf_lo(xr[i]); v[2 * i + 1] = bf_hi(xr[i]); }
    const float m = wave_sum(((v[0] + v[1]) + (v[2] + v[3])) + ((v[4] + v[5]) + (v[6] + v[7]))) * (1.f / DM);
    float q0 = 0, q1 = 0;
#pragma unroll
    for (int i = 0; i < 4; i++) { const float t0 = v[i] - m, t1 = v[i + 4] - m; q0 += t0 * t0; q1 += t1 * t1; }
    const float var = rbf(wave_sum(q0 + q1) * (1.f / DM));
    const float rs = __builtin_amdgcn_rcpf(rbf(__builtin_amdgcn_sqrtf(rbf(var + 1e-5f)))), mb = rbf(m);
    u32 uu[4];
#pragma unroll
    for (int i = 0; i < 4; i++) {
      const u32 a = pk_bf16(v[2 * i] - mb, v[2 * i + 1] - mb);
      const u32 u = pk_bf16(bf_lo(a) * rs, bf_hi(a) * rs);
      uu[i] = pk_f16(bf_lo(u), bf_hi(u));
    }
    u32x4 o; o[0] = uu[0]; o[1] = uu[1]; o[2] = uu[2]; o[3] = uu[3];
    *lds<u32x4>(L_T + w * T_STR + lane * 16) = o;
  }
  BAR();
  {
    __amdgpu_buffer_rsrc_t rs = __builtin_amdgcn_make_buffer_rsrc(ws + WS_EX, 0, 8u << 20, 0x00020000);
    const u32 ra = L_T + (4 * (p4 & 1) + 3 - q4) * T_STR + (64 * w + 8 * g4 + 4 * (p4 >> 1)) * 2;
    const u32 rx = L_TX + (3 - q4) * T_STR + (64 * w + 16 * g4 + 4 * p4) * 2;
    const u32 so = ((lane >> 3) * 256 + b) * 128 + (lane & 7) * 16;
    u32x2 r[2];
#pragma unroll
    for (int i = 0; i < 2; i++) r[i] = tr_read(ra + i * 64);
    const u32x4 xt = cat(tr_read(rx + 4 * T_STR), tr_read(rx));
#pragma unroll
    for (int i = 0; i < 2; i++) {
      u32x4 o; o[0] = r[i][0]; o[1] = tag; o[2] = r[i][1]; o[3] = tag;
      __builtin_amdgcn_raw_buffer_store_b128(o, rs, so + (16 * w + 8 * i) * 32768, 0, 16);
    }
    __builtin_amdgcn_raw_buffer_store_b128(xt, rs, so + (WS_XT - WS_EX) + 8 * w * 32768, 0, 16);
  }
  STAMP(1);
  asm volatile("s_waitcnt vmcnt(12)" : "+v"(pl), "+v"(pa), "+v"(pr), "+v"(pi));
  const float dt = rbf(__expf(ch ? bf_hi(ldt2) : bf_lo(ldt2)));
  {
    const float LOG2E = 1.4426950408889634f, INV2PI = 0.15915494309189535f;
    float lr[2], rv[2];
    lr[0] = -rbf(__expf(bf_lo(pl))) * dt * LOG2E; lr[1] = -rbf(__expf(bf_hi(pl))) * dt * LOG2E;
    rv[0] = bf_lo(pa) * dt * INV2PI; rv[1] = bf_hi(pa) * dt * INV2PI;
    const u32 wb = L_XY + ch * XY_CH + lane * 8;
    auto put = [&](int row, cf a, cf c) {
      u32x2 v; v[0] = pk_f16(a.r, a.i); v[1] = pk_f16(c.r, c.i);
      *lds<u32x2>(wb + row * XY_STR) = v;
    };
    auto G = [&](int i) { w1_one(4 + i, L_W0 + (4 + i) * 8192); if (i >= 2) w1_one(6 + i, L_W0 + (6 + i) * 8192); };
    if (job < 2) {
      G(0);
      cf s0 = cexp_at(lr[0], -rv[0], 8.f), s1 = cexp_at(lr[1], -rv[1], 8.f);
      const float p0 = job ? 128.f : 0.f;
      cf z0 = cexp_at(lr[0], -rv[0], p0), z1 = cexp_at(lr[1], -rv[1], p0);
      const u32 wb2 = wb + job * 16 * XY_STR;
#pragma unroll
      for (int i = 0; i < 16; i++) {
        u32x2 v; v[0] = pk_f16(z0.r, z0.i); v[1] = pk_f16(z1.r, z1.i);
        *lds<u32x2>(wb2 + i * XY_STR) = v;
        if (i < 15) { z0 = cmul(z0, s0); z1 = cmul(z1, s1); }
        if (i == 2 || i == 7 || i == 12) G(1 + i / 5);
        SCHED();
      }
    } else {
      G(0);
      cf c0 = {bf_lo(pr), bf_lo(pi)}, c1 = {bf_hi(pr), bf_hi(pi)};
      const float mx = wave_max(fmaxf(fmaxf(fabsf(c0.r), fabsf(c0.i)), fmaxf(fabsf(c1.r), fabsf(c1.i))));
      const int ex = __builtin_amdgcn_frexp_expf(mx);
      const float sc = __builtin_amdgcn_ldexpf(1.f, -ex);
      c0.r *= sc; c0.i *= sc; c1.r *= sc; c1.i *= sc;
      G(1);
      if (job == 2) {
        if (lane == 0) *lds<float>(L_MISC + ch * 4) = __builtin_amdgcn_ldexpf(2.f * dt, ex);
        cf s0 = cexp_at(lr[0], rv[0], 1.f), s1 = cexp_at(lr[1], rv[1], 1.f);
        cf z0 = c0, z1 = c1;
#pragma unroll
        for (int i = 0; i < 8; i++) { put(32 + i, z0, z1); if (i < 7) { z0 = cmul(z0, s0); z1 = cmul(z1, s1); } if (i == 2 || i == 7) G(2 + i / 5); SCHED(); }
        s0 = cmul(s0, s0); s1 = cmul(s1, s1);
        z0 = cmul(c0, cexp_at(lr[0], rv[0], 256.f)); z1 = cmul(c1, cexp_at(lr[1], rv[1], 256.f));
#pragma unroll
        for (int i = 0; i < 4; i++) { put(40 + i, z0, z1); if (i < 3) { z0 = cmul(z0, s0); z1 = cmul(z1, s1); } }
      } else {
        cf f0 = cexp_at(lr[0], rv[0], 4.f), f1 = cexp_at(lr[1], rv[1], 4.f);
        cf s0 = cexp_at(lr[0], rv[0], 256.f), s1 = cexp_at(lr[1], rv[1], 256.f);
        G(2); SCHED();
        cf z0 = cmul(c0, cexp_at(lr[0], rv[0], 512.f)), z1 = cmul(c1, cexp_at(lr[1], rv[1], 512.f));
        put(44, z0, z1); put(45, cmul(z0, f0), cmul(z1, f1));
        z0 = cmul(z0, s0); z1 = cmul(z1, s1);
        put(46, z0, z1); put(47, cmul(z0, f0), cmul(z1, f1));
#pragma unroll
        for (int i = 0; i < 5; i++) { z0 = cmul(z0, s0); z1 = cmul(z1, s1); put(48 + i, z0, z1); if (i == 1) G(3); SCHED(); }
      }
    }
  }
  asm volatile("s_waitcnt vmcnt(0)" : "+v"(junk));
  BAR();
  STAMP(2);
  if (tid == 0) __hip_atomic_store((GAS u32 *)(ws + WS_FLAG1) + b, tag, __ATOMIC_RELAXED, __HIP_MEMORY_SCOPE_AGENT);
  u32x4 ex[2];
  const char *const exb = ws + WS_EX + (size_t)(cp >> 1) * 32768 + (cp & 1) * 32;
  const u32 exo = (tid >> 2) * 128 + (tid & 2) * 32 + (tid & 1) * 16;
  asm volatile("global_load_dwordx4 %0, %1, %2 sc1" : "=v"(ex[0]) : "v"(exo), "s"(exb));
  asm volatile("global_load_dwordx4 %0, %1, %2 sc1" : "=v"(ex[1]) : "v"(exo), "s"(exb + 16384));
  {
    const u32 rb = job >> 1, cb = job & 1;
    const u32 xa = L_XY + ch * XY_CH + (32 + 16 * rb + (lane & 15)) * XY_STR + g4 * 16;
    const u32 ya = L_XY + ch * XY_CH + (16 * cb + (lane & 15)) * XY_STR + g4 * 16;
    u32x4 fa[8], fb[8];
#pragma unroll
    for (int ks = 0; ks < 8; ks++) { fa[ks] = *lds<u32x4>(xa + ks * 64); fb[ks] = *lds<u32x4>(ya + ks * 64); }
    SCHED();
    u32 wr = L_W0 + w1_rd;
    PIN(wr);
#pragma unroll
    for (int ks = 0; ks < 10; ks++) { const u32 a = (ks < 8 ? wr : wr + 65536) + (ks & 7) * 8192; W[ks] = cat(tr_read(a), tr_read(a + 4096)); }
    SCHED();
    const u32 j = 16 * cb + (lane & 15);
    u32 i0, is, i2;
    if (rb == 0) {
      i0 = g4 < 2 ? 4 * g4 + 8 * j : (g4 == 2 ? 256 + 4 * j : 384 + 2 * j); is = 1; i2 = g4 == 3 ? 64 : 2;
    } else {
      i0 = g4 == 0 ? 512 + j : 640 + j; is = g4 == 0 ? 32 : 0; i2 = 2 * is;
    }
    f32x4 a0 = {0, 0, 0, 0}, a1 = {0, 0, 0, 0};
#pragma unroll
    for (int ks = 0; ks < 8; ks += 2) { a0 = mfma16h(fa[ks], fb[ks], a0); a1 = mfma16h(fa[ks + 1], fb[ks + 1], a1); }
    a0 += a1;
    const u32 vb = L_VT + ch * VT_CH;
    if (rb == 0 || g4 == 0) {
#pragma unroll
      for (int r = 0; r < 4; r++) *lds<_Float16>(vb + 2 * (i0 + (r & 1) * is + (r >> 1) * i2)) = (_Float16)a0[r];
    } else if (g4 == 1) {
      *lds<_Float16>(vb + 2 * i0) = (_Float16)a0[0];
    }
  }
  STAMP(4);
  asm volatile("s_waitcnt vmcnt(0)" : "+v"(ex[0]), "+v"(ex[1]));
#pragma unroll 1
  for (int spin = 0; spin < SPIN_CAP; spin++) {
    if (__all(ex[0][1] == tag && ex[0][3] == tag && ex[1][1] == tag && ex[1][3] == tag)) break;
    __builtin_amdgcn_s_sleep(1);
    asm volatile("global_load_dwordx4 %0, %2, %3 sc1\n\tglobal_load_dwordx4 %1, %2, %4 sc1\n\ts_waitcnt vmcnt(0)"
                 : "=&v"(ex[0]), "=&v"(ex[1]) : "v"(exo), "s"(exb), "s"(exb + 16384) : "memory");
  }
  BAR();
  STAMP(5);
#pragma unroll
  for (int i = 0; i < 4; i++) w1_one(10 + i, L_W0 + i * 8192);
  {
    const u32 ab = L_UR + 3 * UR_STR + (tid & 1) * UR_CH + (2044 - 1024 - 8 * (tid >> 2) - 2 * (tid & 2)) * 2;
#pragma unroll
    for (int i = 0; i < 2; i++) { u32x2 v; v[0] = ex[i][0]; v[1] = ex[i][2]; *lds<u32x2>(ab + (1 - i) * 2048) = v; }
  }
  {
    u32x4 kv;
    _Float16 t[8];
#pragma unroll
    for (int e = 0; e < 8; e++) t[e] = *lds<_Float16>(vidx[e]);
#pragma unroll
    for (int e = 0; e < 4; e++) { h16x2 t2; t2[0] = t[2 * e]; t2[1] = t[2 * e + 1]; kv[e] = __builtin_bit_cast(u32, t2); }
    const u32 kb = L_K + (tid >> 8) * K_CH + (tid & 255) * 16;
    u32x2 lo, hi; lo[0] = kv[0]; lo[1] = kv[1]; hi[0] = kv[2]; hi[1] = kv[3];
    *lds<u32x2>(kb + 120) = lo; *lds<u32x2>(kb + 128) = hi;
    *lds<u32x4>(kb + 4224 + 112) = kv;
    const u32x2 z2 = {0, 0};
    u32 za;
    if (tid < 64) {
      const u32 c = tid >> 5, i = tid & 31;
      za = L_K + c * K_CH + (i < 15 ? i * 8 : 4224 + (i < 29 ? i - 15 : 0) * 8);
    } else {
      const u32 i = tid < 448 ? tid - 64 : 0, c = i / 48;
      za = L_UR + c * UR_STR + 4096 + (i - c * 48) * 8;
    }
    *lds<u32x2>(za) = z2;
  }
#pragma unroll
  for (int i = 4; i < 8; i++) w1_one(10 + i, L_W0 + i * 8192);
  BAR();
  {
    const u32 ub = L_UR + (tid >> 8) * UR_CH + (tid & 255) * 16;
    const u32x4 a = *lds<u32x4>(ub + 3 * UR_STR);
    const u32x2 c = *lds<u32x2>(ub + 3 * UR_STR + 16);
    u32x4 o;
    o[0] = __builtin_amdgcn_alignbit(a[1], a[0], 16); o[1] = __builtin_amdgcn_alignbit(a[2], a[1], 16);
    o[2] = __builtin_amdgcn_alignbit(a[3], a[2], 16); o[3] = __builtin_amdgcn_alignbit(c[0], a[3], 16);
    *lds<u32x4>(ub + 2 * UR_STR) = o;
    u32x4 o1; o1[0] = a[1]; o1[1] = a[2]; o1[2] = a[3]; o1[3] = c[0];
    *lds<u32x4>(ub + UR_STR) = o1;
    u32x4 o0; o0[0] = o[1]; o0[1] = o[2]; o0[2] = o[3]; o0[3] = __builtin_amdgcn_alignbit(c[1], c[0], 16);
    *lds<u32x4>(ub) = o0;
  }
  WAIT_VM(0);
  BAR();
  STAMP(6);
  u32 f1;
  const u32 xp = 255 - 32 * w - (lane >> 1);
  {
    u32 wr = L_W0 + w1_rd;
    PIN(wr);
#pragma unroll
    for (int ks = 0; ks < 8; ks++) W[10 + ks] = cat(tr_read(wr + ks * 8192), tr_read(wr + ks * 8192 + 4096));
    SCHED();
  }
  const u32 pg = w & 1, cg = (w >> 1) & 1;
  f32x4 ya[4];
#pragma unroll
  for (int i = 0; i < 4; i++) ya[i] = f32x4{0, 0, 0, 0};
  {
    const u32 c16 = lane & 15;
    const u32 ka = L_K + ch * K_CH + (c16 & 1) * 4224 + (c16 >> 1) * 16 + g4 * 16 + cg * 4 * 64;
    const u32 ua = L_UR + ch * UR_CH + (c16 & 3) * UR_STR + (1984 - 1792 - 64 * (c16 >> 2) + 8 * g4) * 2 + cg * 4 * 64;
    u32x4 kf[2][8], uf[2][8];
    auto rd1 = [&](int s, int j) {
      if (j >= 1 && j <= 8) uf[s & 1][8 - j] = *lds<u32x4>(ua + (j - 1) * 512 + s * 64);
      else kf[s & 1][j ? j - 8 : 0] = *lds<u32x4>(ka + (j ? j - 8 : 0) * 512 + s * 64);
    };
    auto rd = [&](int s, int i) { rd1(s, 2 * i); rd1(s, 2 * i + 1); };
    auto side = [&](int s, int i) {
      const int n = s * 8 + i;
      if (n == 0) asm volatile("global_load_dword %0, %1, %2 sc1" : "=v"(f1) : "v"(xp * 4), "s"(ws + WS_FLAG1));
      if (n == 1 || n == 4) w1_one(26 + n / 3, L_W0 + (8 + n / 3) * 8192);
      if (n == 6) RAWBAR();
      if (n == XN) {
        asm volatile("s_waitcnt vmcnt(%1)" : "+v"(f1) : "n"(2 + (XN - 5) / 3));
        if (!__all(f1 == tag)) poll(WS_FLAG1 + xp * 4);
        glds_sc1(xp * 128 + (lane & 1) * 16, ws + WS_XT + (size_t)(cp >> 2) * 32768 + (cp & 3) * 32, L_XS + w * 1024);
      }
      if (n >= 7 && (n - 7) % 3 == 0 && n < 31) w1_one(18 + (n - 7) / 3, L_W0 + (n - 7) / 3 * 8192);
    };
#pragma unroll
    for (int i = 0; i < 8; i++) rd(0, i);
    if (pg == 0) {
#pragma unroll
      for (int s = 0; s < 4; s++) {
#pragma unroll
        for (int i = 0; i < 8; i++) {
          if (s < 3) rd(s + 1, i);
          side(s, i);
          ya[3] = mfma16h(uf[s & 1][7 - i], kf[s & 1][i], ya[3]);
          if (i <= 4) ya[2] = mfma16h(uf[s & 1][4 - i], kf[s & 1][i], ya[2]);
          if (i <= 3) ya[1] = mfma16h(uf[s & 1][3 - i], kf[s & 1][i], ya[1]);
          if (i <= 0) ya[0] = mfma16h(uf[s & 1][0 - i], kf[s & 1][i], ya[0]);
          SCHED();
        }
      }
    } else {
#pragma unroll
      for (int s = 0; s < 4; s++) {
#pragma unroll
        for (int i = 0; i < 8; i++) {
          if (s < 3) rd(s + 1, i);
          side(s, i);
          if (i <= 6) ya[3] = mfma16h(uf[s & 1][6 - i], kf[s & 1][i], ya[3]);
          if (i <= 5) ya[2] = mfma16h(uf[s & 1][5 - i], kf[s & 1][i], ya[2]);
          if (i <= 2) ya[1] = mfma16h(uf[s & 1][2 - i], kf[s & 1][i], ya[1]);
          if (i <= 1) ya[0] = mfma16h(uf[s & 1][1 - i], kf[s & 1][i], ya[0]);
          SCHED();
        }
      }
    }
  }
  wait_vm<8 - (XN - 5) / 3>();
  {
    const u32 yb = L_YP + w * 2048 + lane * 16;
    *lds<f32x4>(yb) = cg ? ya[0] : ya[2];
    *lds<f32x4>(yb + 1024) = cg ? ya[1] : ya[3];
  }
  BAR();
  STAMP(7);
  w1_one(28, L_W2); w1_one(29, L_W2 + 8192);
  {
    const u32 yb = L_YP + (w ^ 2) * 2048 + lane * 16;
    const float ksc = *lds<float>(L_MISC + ch * 4), dsk = ch ? bf_hi(dsk2) : bf_lo(dsk2);
    __amdgpu_buffer_rsrc_t rs = __builtin_amdgcn_make_buffer_rsrc(ws + WS_HT, 0, SEQ * DM * 2, 0x00020000);
    f32x4 o[2]; u32x2 uu[2], xx[2]; u32 t0[2];
#pragma unroll
    for (int i = 0; i < 2; i++) {
      o[i] = *lds<f32x4>(yb + i * 1024);
      t0[i] = 256 * (4 * cg + (i ? 3 - pg : pg)) + 4 * lane;
      uu[i] = *lds<u32x2>(L_UR + ch * UR_CH + 3 * UR_STR + (2044 - t0[i]) * 2);
      xx[i] = *lds<u32x2>(L_XS + ((2044 - t0[i]) >> 3) * 32 + ch * 16 + ((2044 - t0[i]) & 7) * 2);
    }
#pragma unroll
    for (int i = 0; i < 2; i++) {
      const f32x4 own = cg ? (i ? ya[3] : ya[2]) : (i ? ya[1] : ya[0]);
      const u32 uu0 = uu[i][0], uu1 = uu[i][1];
      const h16x2 ua = __builtin_bit_cast(h16x2, uu1), ub = __builtin_bit_cast(h16x2, uu0);
      const float uv[4] = {(float)ua[1], (float)ua[0], (float)ub[1], (float)ub[0]};
      const float xv[4] = {bf_hi(xx[i][1]), bf_lo(xx[i][1]), bf_hi(xx[i][0]), bf_lo(xx[i][0])};
      float hv[4];
#pragma unroll
      for (int r = 0; r < 4; r += 2) {
        const u32 y2 = pk_bf16((o[i][r] + own[r]) * ksc, (o[i][r + 1] + own[r + 1]) * ksc);
        const u32 d2 = pk_bf16(uv[r] * dsk, uv[r + 1] * dsk);
        const u32 z2 = pk_bf16(bf_lo(y2) + bf_lo(d2), bf_hi(y2) + bf_hi(d2));
        hv[r] = xv[r] + bf_lo(z2); hv[r + 1] = xv[r + 1] + bf_hi(z2);
      }
      u32x2 hv2; hv2[0] = pk_bf16(hv[0], hv[1]); hv2[1] = pk_bf16(hv[2], hv[3]);
      const u32 off = ((t0[i] >> 5) * DM + d0 + ch) * 64 + (t0[i] & 31) * 2;
      __builtin_amdgcn_raw_buffer_store_b64(hv2, rs, off, 0, 16);
    }
  }
  SCHED();
  if (b == 0 && tid == 0)
    __hip_atomic_store((GAS u32 *)(ws + WS_EPOCH), tag, __ATOMIC_RELAXED, __HIP_MEMORY_SCOPE_AGENT);
  BAR();
  f32x16 cs;
#pragma unroll
  for (int i = 0; i < 16; i++) cs[i] = 0;
  const u32x4 ones = {0x3f803f80u, 0x3f803f80u, 0x3f803f80u, 0x3f803f80u};
  w1_one(30, L_YP); w1_one(31, L_YP + 8192);
#pragma unroll
  for (int ks = 0; ks < 12; ks++) cs = mfma32(W[ks], ones, cs);
  SCHED();
  WAIT_VM(2);
  RAWBAR();
  if (tid == 0) __hip_atomic_store((GAS u32 *)(ws + WS_FLAG2) + b * 32, tag, __ATOMIC_RELAXED, __HIP_MEMORY_SCOPE_AGENT);
  STAMP(8);
  const u32 t32 = lane & 31;
  const u32 nt = jj & 7, tt = jj >> 3, pn2 = 2 * xcd + (tt >> 1), tgA = tt & 1, tgB = 3 - tgA;
  const u32 lv = lane * 16, rot2 = nt == 7 ? 0 : nt;
  auto ft_of = [&](int i) { const u32 k = (i % 7) + rot2; return 2 * (k >= 7 ? k - 7 : k) + (i < 7 ? tgA ^ 1 : tgA); };
  const u32 d_voff = (lane >> 3) * (DM * 2) + (((lane & 7) ^ (((lane >> 4) & 1) << 2)) * 16);
  const char *const wd_base = (const char *)p.wd + (size_t)(w * 16) * (DM * 2) + nt * 128;
  auto wd_one = [&](int u) {
    const u32 k = __builtin_amdgcn_readfirstlane((ft_of(u >> 1) << 17) + ((u & 1) << 13));
    glds(d_voff, wd_base + k, wd_slot(u) * 8192 + w * 1024);
  };
  u32x4 D[28];
  u32 d_rd[2][3];
#pragma unroll
  for (int h = 0; h < 2; h++) {
    d_rd[h][0] = w * 1024 + (((4 * (g4 >> 1) + q4) * 128 + h * 64 + (g4 & 1) * 32 + p4 * 8) ^ (((q4 >> 1) & 1) * 64));
    d_rd[h][1] = d_rd[h][0] + 65536; d_rd[h][2] = d_rd[h][0] + 131072;
  }
  auto wd_take = [&](int j, int h) {
    const u32 o = wd_slot(2 * j) * 8192;
    D[2 * j + h] = cat(tr_read(d_rd[h][o >> 16] + (o & 65535)), tr_read(d_rd[h][o >> 16] + (o & 65535) + 8192));
  };
  auto lane_again = []() {
    u32 l; asm volatile("v_mbcnt_lo_u32_b32 %0, -1, 0\n\tv_mbcnt_hi_u32_b32 %0, -1, %0" : "=v"(l)); return l;
  };
  auto foA_of = [&]() { return WS_FLAG3 + ((pn2 * 4 + tgA) * 16 + min(lane_again(), 13u)) * 128; };
  __amdgpu_buffer_rsrc_t prs = __builtin_amdgcn_make_buffer_rsrc(ws + WS_P, 0, SEQ * FF * 2, 0x00020000);
  __amdgpu_buffer_rsrc_t frs = __builtin_amdgcn_make_buffer_rsrc(ws + WS_FLAG3, 0, 131072, 0x00020000);
  const u32 OOB = 0x40000000u;
  const u32 p_off = g1 ? (panel * 4 * 112 + ft * 8 + w) * 1024 + lane * 16 : OOB;
  const u32 f_off = g1 && tid == 0 ? (panel * 64 + ft) * 128 : OOB;
  auto tg_of = [&](int g) { return g < 2 ? (u32)g : g ^ (ft & 1); };
  float e_m, e_ne, e_s2, e_g, e_u, e_e, e_o;
  u32 pko[4];
  u32x2 sg;
  const u32 sg_off = WS_ST + (panel * 128 + t32) * 8;
  auto st_take = [&](int tg) {
    PIN(sg);
#pragma unroll 1
    for (int spin = 0; spin < SPIN_CAP; spin++) {
      if (__all(sg[1] == tag)) break;
      __builtin_amdgcn_s_sleep(1);
      asm volatile("global_load_dwordx2 %0, %1, %2 sc1\n\ts_waitcnt vmcnt(0)" : "=&v"(sg) : "v"(sg_off), "s"(ws + tg_of(tg) * 256) : "memory");
    }
    e_m = bf_lo(sg[0]);
    const float sd = bf_hi(sg[0]);
    e_ne = -1.4426950408889634f * __builtin_amdgcn_rcpf(sd); e_s2 = sd * sd;
  };
  auto epi_step = [&](int ks, int tg, const f32x16 &prev) {
    if (ks == 0) st_take(tg);
    else if (ks >= 1 && ks < 25) {
      const int i = (ks - 1) / 3, ph = (ks - 1) % 3;
      if (ph == 0) {
        e_g = prev[i] - e_m * cs[i]; PIN(e_g);
        e_e = __builtin_amdgcn_exp2f(e_g * e_ne);
      } else if (ph == 1) {
        e_u = (prev[i + 8] - e_m * cs[i + 8]) * e_g;
        e_e = e_e * e_s2 + e_s2;
        PIN(e_u); PIN(e_e);
      } else {
        const float o = e_u * __builtin_amdgcn_rcpf(e_e);
        if (i & 1) { pko[i >> 1] = pk_bf16(e_o, o); PIN(pko[i >> 1]); } else { e_o = o; PIN(e_o); }
      }
    } else if (ks == 25) {
      u32x4 v; v[0] = pko[0]; v[1] = pko[1]; v[2] = pko[2]; v[3] = pko[3];
      __builtin_amdgcn_raw_buffer_store_b128(v, prs, p_off + tg_of(tg) * (112 * 1024), 0, 16);
    }
  };
  f32x16 acc[2];
  if (g1) {
    {
      u32 wr = L_W0 + w1_rd;
      PIN(wr);
#pragma unroll
      for (int ks = 0; ks < 8; ks++) W[18 + ks] = cat(tr_read(wr + ks * 8192), tr_read(wr + ks * 8192 + 4096));
      wr += 65536;
#pragma unroll
      for (int ks = 0; ks < 2; ks++) W[26 + ks] = cat(tr_read(wr + ks * 8192), tr_read(wr + ks * 8192 + 4096));
      wr = L_W2 + w1_rd;
      PIN(wr);
#pragma unroll
      for (int ks = 0; ks < 2; ks++) W[28 + ks] = cat(tr_read(wr + ks * 8192), tr_read(wr + ks * 8192 + 4096));
#pragma unroll
      for (int ks = 12; ks < 18; ks++) cs = mfma32(W[ks], ones, cs);
      SCHED();
      if (w == 0) WAIT_VM(1); else WAIT_VM(0);
      RAWBAR();
#pragma unroll
      for (int ks = 2; ks < 4; ks++) W[28 + ks] = cat(tr_read(wr + ks * 8192), tr_read(wr + ks * 8192 + 4096));
#pragma unroll
      for (int ks = 18; ks < 32; ks++) cs = mfma32(W[ks], ones, cs);
      SCHED();
    }
    STAMP(9);
    poll(WS_FLAG2 + (w * 32 + t32) * 128);
    BAR();
    STAMP(10);
    const char *const a_base = ws + WS_HT + (size_t)panel * 131072;
    auto a_one = [&](int i) { glds_sc1(lv, a_base + tg_of(i >> 2) * 32768 + (((i & 3) * 8 + w + rot1) & 31) * 1024, L_A + w * 1024 + i * 8192); };
#pragma unroll
    for (int i = 0; i < 8; i++) a_one(i);
    u32 ab = L_A + (4 * (g4 >> 1) + q4) * 64 + (g4 & 1) * 32 + p4 * 8;
    u32x4 F[LOOK + 1];
    auto frag = [&](u32 a) { return cat(tr_read(a), tr_read(a + 512)); };
    u32 fa;
    wait_vm<g1_younger(-8, 0)>();
    RAWBAR();
    u32 ab2 = ab + 65536;
    PIN(ab); PIN(ab2);
#pragma unroll
    for (int i = 0; i < LOOK; i++) F[i] = frag(ab + i * 1024);
    static_for<0, 128>([&](auto P_) {
      constexpr int P = decltype(P_)::v, tg = P >> 5, ks = P & 31;
      if constexpr (ks == 0) {
#pragma unroll
        for (int i = 0; i < 16; i++) acc[tg & 1][i] = 0;
      }
      if constexpr (P == 8 - LOOK) { wait_vm<g1_younger(-7, P)>(); RAWBAR(); }
      if constexpr (P == 16 - LOOK) { wait_vm<g1_younger(-6, P)>(); RAWBAR(); }
      if constexpr (P == 24 - LOOK) { wait_vm<g1_younger(-5, P)>(); RAWBAR(); }
      if constexpr (P == 32 - LOOK) { wait_vm<g1_younger(-1, P)>(); RAWBAR(); }
      if constexpr (P == 48) { wait_vm<g1_younger(14, P)>(); RAWBAR(); }
      if constexpr (P == 64) { wait_vm<g1_younger(30, P)>(); RAWBAR(); }
      if constexpr (P == 66) wait_vm<g1_younger(54, P)>();
      if constexpr (P == 96) { wait_vm<g1_younger(85, P)>(); RAWBAR(); }
      if constexpr (P == 116) wait_vm<g1_younger(102, P)>();
      if constexpr (P == 118) RAWBAR();
      if constexpr (P == 122) wait_vm<g1_younger(114, P)>();
      if constexpr (g1_find(23, 2, P) >= 0) __builtin_amdgcn_raw_buffer_store_b32(tag, frs, f_off, 2048 * g1_find(23, 2, P), 16);
      if constexpr (P == G1_OPS[25]) asm volatile("global_load_dword %0, %1, %2 sc1" : "=v"(fa) : "v"(foA_of()), "s"(ws));
      if constexpr (g1_find(8, 8, P) >= 0) a_one(8 + g1_find(8, 8, P));
      if constexpr (g1_find(16, 4, P) >= 0)
        asm volatile("global_load_dwordx2 %0, %1, %2 sc1" : "=v"(sg) : "v"(sg_off), "s"(ws + tg_of(g1_find(16, 4, P)) * 256));
      if constexpr (g1_find(26, 28, P) >= 0) wd_one(g1_find(26, 28, P));
      if constexpr (g1_take(P) >= 0) wd_take(g1_take(P) >> 1, g1_take(P) & 1);
      constexpr int n = P + LOOK;
      if constexpr (n < 128) F[n % (LOOK + 1)] = frag((n < 64 ? ab : ab2) + (n & 63) * 1024);
      acc[tg & 1] = mfma32(W[ks], F[P % (LOOK + 1)], acc[tg & 1]);
      if constexpr (tg > 0 && ks == EPI0) wait_vm<g1_younger(G1_OPS[16 + tg - 1], P + 1)>();
      if constexpr (tg > 0 && ks >= EPI0) epi_step(ks - EPI0, tg - 1, acc[(tg - 1) & 1]);
      SCHED();
      if constexpr (P == 31) STAMP(11);
      if constexpr (P == 63) STAMP(19);
      if constexpr (P == 95) STAMP(20);
      if constexpr (P == 127) STAMP(3);
    });
    wait_vm<g1_younger(121, 128)>();
    PIN(fa);
    st_take(3);
    if (!__all(fa == tag)) poll(foA_of());
    STAMP(12);
  } else {
    poll(WS_FLAG2 + (w * 32 + t32) * 128);
    RAWBAR();
    const u32 tgs = (2 * xcd + ((jj >> 1) & 1)) * 4 + (jj & 1);
    const char *base = ws + WS_HT + (size_t)tgs * 32768 + w * 4096;
#pragma unroll
    for (int i = 0; i < 8; i++) glds_sc1(lv, base + (i >> 2) * 65536 + (i & 3) * 1024, 65536 + (i >> 2) * 32768 + w * 4096 + (i & 3) * 1024);
    const u32 ab = 65536 + w * 4096 + (4 * (g4 >> 1) + q4) * 64 + (g4 & 1) * 32 + p4 * 8;
#pragma unroll
    for (int g = 0; g < 2; g++) {
      if (g == 0) WAIT_VM(4); else WAIT_VM(0);
      float s_s = 0, s_q = 0;
#pragma unroll
      for (int i = 0; i < 4; i++) {
        const u32x4 f = cat(tr_read(ab + g * 32768 + i * 1024), tr_read(ab + g * 32768 + i * 1024 + 512));
#pragma unroll
        for (int k = 0; k < 4; k++) { s_s = dot2bf(f[k], 0x3f803f80u, s_s); s_q = dot2bf(f[k], f[k], s_q); }
      }
      const auto sw = __builtin_amdgcn_permlane32_swap(__float_as_uint(s_s), __float_as_uint(s_q), false, false);
      *lds<float>(g * 2048 + w * 256 + lane * 4) = __uint_as_float(sw[0]) + __uint_as_float(sw[1]);
      BAR();
      float sp = 0;
#pragma unroll
      for (int i = 0; i < 8; i++) sp += *lds<float>(g * 2048 + i * 256 + lane * 4);
      const auto s2 = __builtin_amdgcn_permlane32_swap(__float_as_uint(sp), __float_as_uint(sp), false, false);
      const float mean = __uint_as_float(s2[0]) * (1.f / DM);
      const float var = rbf(fmaxf(__uint_as_float(s2[1]) * (1.f / DM) - mean * mean, 0.f));
      const u64 gr = ((u64)tag << 32) | pk_bf16(mean, __builtin_amdgcn_sqrtf(rbf(var + 1e-5f)));
      if (tid < 32) __hip_atomic_store((GAS u64 *)(ws + WS_ST) + (tgs + 2 * g) * 32 + lane, gr, __ATOMIC_RELAXED, __HIP_MEMORY_SCOPE_AGENT);
    }
    BAR();
#pragma unroll
    for (int u = 0; u < 12; u++) wd_one(u);
    WAIT_VM(0);
#pragma unroll
    for (int j = 0; j < 6; j++) { wd_take(j, 0); wd_take(j, 1); }
    WAIT_LGKM0();
#pragma unroll
    for (int u = 12; u < 28; u++) wd_one(u);
    poll(foA_of());
#pragma unroll
    for (int j = 6; j < 14; j++) { wd_take(j, 0); wd_take(j, 1); }
#pragma unroll
    for (int i = 0; i < 16; i++) acc[1][i] = 0;
    e_m = 0; e_ne = 0; e_s2 = 1.f;
  }

  {
    const char *const pbA = ws + WS_P + ((size_t)(pn2 * 4 + tgA) * 112 + w) * 1024;
    const char *const pbB = ws + WS_P + ((size_t)(pn2 * 4 + tgB) * 112 + w) * 1024;
    u32x4 PF[14], QF[14];
    const u32 ln = lane_again(), hh = ln >> 5, tl = ln & 31;
    const u32 foB = WS_FLAG3 + ((pn2 * 4 + tgB) * 16 + 2 * min(ln, 6u)) * 128;
    auto p_one = [&](const char *pb, int i, u32x4 &d) {
      const u32 j = __builtin_amdgcn_readfirstlane(ft_of(i) << 13);
      asm volatile("global_load_dwordx4 %0, %1, %2 sc1" : "=v"(d) : "v"(lv), "s"(pb + j));
    };
#pragma unroll
    for (int i = 0; i < 4; i++) p_one(pbA, i, PF[i]);
    SCHED();
    {
      const f32x2 m2 = {e_m, e_m}, ne2 = {e_ne, e_ne}, s22 = {e_s2, e_s2};
      u32x4 v;
#pragma unroll
      for (int i = 0; i < 8; i += 2) {
        const f32x2 g = f32x2{acc[1][i], acc[1][i + 1]} - m2 * f32x2{cs[i], cs[i + 1]};
        const f32x2 u = (f32x2{acc[1][i + 8], acc[1][i + 9]} - m2 * f32x2{cs[i + 8], cs[i + 9]}) * g;
        f32x2 e = g * ne2;
        e[0] = __builtin_amdgcn_exp2f(e[0]); e[1] = __builtin_amdgcn_exp2f(e[1]);
        e = e * s22 + s22;
        e[0] = __builtin_amdgcn_rcpf(e[0]); e[1] = __builtin_amdgcn_rcpf(e[1]);
        const f32x2 o = u * e;
        v[i >> 1] = pk_bf16(o[0], o[1]);
      }
      __builtin_amdgcn_raw_buffer_store_b128(v, prs, p_off + tg_of(3) * (112 * 1024), 0, 16);
    }
    SCHED();
    STAMP(13);
    wait_vm<5>();
    RAWBAR();
    __builtin_amdgcn_raw_buffer_store_b32(tag, frs, f_off, tg_of(2) * 2048, 16);
#pragma unroll
    for (int i = 4; i < 9; i++) p_one(pbA, i, PF[i]);
    const u32 own_n = nt * 64 + w * 8;
    u32 res[8];
    const u32 roA = WS_HT + ((pn2 * 4 + tgA) * DM + own_n + 4 * hh) * 64 + tl * 2;
    const u32 roB = WS_HT + ((pn2 * 4 + tgB) * DM + own_n + 4 * hh) * 64 + tl * 2;
    f32x16 ac[2];
    f32x4 r[2];
    auto red_put = [&](int t) {
#pragma unroll
      for (int o = 0; o < 8; o++) {
        f32x4 v; v[0] = ac[o >> 2][4 * (o & 3)]; v[1] = ac[o >> 2][4 * (o & 3) + 1]; v[2] = ac[o >> 2][4 * (o & 3) + 2]; v[3] = ac[o >> 2][4 * (o & 3) + 3];
        *lds<f32x4>(t * 65536 + o * 8192 + w * 1024 + lv) = v;
      }
    };
    auto red_get = [&](int t) {
      f32x4 v[8];
#pragma unroll
      for (int i = 0; i < 8; i++) v[i] = *lds<f32x4>(t * 65536 + w * 8192 + i * 1024 + lv);
      r[t] = ((v[0] + v[1]) + (v[2] + v[3])) + ((v[4] + v[5]) + (v[6] + v[7]));
    };
#pragma unroll
    for (int i = 0; i < 16; i++) { ac[0][i] = 0; ac[1][i] = 0; }
    u32 fb;
    static_for<0, 14>([&](auto I_) {
      constexpr int i = decltype(I_)::v;
      if constexpr (i == 4) {
        wait_vm<6>();
        RAWBAR();
        __builtin_amdgcn_raw_buffer_store_b32(tag, frs, f_off, tg_of(3) * 2048, 16);
        asm volatile("global_load_dword %0, %1, %2 sc1" : "=v"(fb) : "v"(foB + (tgA ^ 1) * 128), "s"(ws));
#pragma unroll
        for (int k = 9; k < 14; k++) p_one(pbA, k, PF[k]);
        STAMP(15);
      }
      if constexpr (i == 9) {
        wait_vm<5>();
        PIN(fb);
        if (!__all(fb == tag)) poll(foB + (tgA ^ 1) * 128);
#pragma unroll
        for (int k = 0; k < 7; k++) p_one(pbB, k, QF[k]);
      }
      wait_vm<i < 4 ? 10 - i : (i < 9 ? 15 - i : 20 - i)>();
      PIN(PF[i]);
      if constexpr (i == 0) STAMP(14);
      ac[0] = mfma32(D[2 * i], PF[i], ac[0]);
      ac[1] = mfma32(D[2 * i + 1], PF[i], ac[1]);
      SCHED();
    });
#define RES_LD(i, ro, o) asm volatile("global_load_ushort %0, %1, %2 offset:" #o " sc1" : "=v"(res[i]) : "v"(ro), "s"(ws));
    RES_LD(0, roA, 0) RES_LD(1, roA, 64) RES_LD(2, roA, 128) RES_LD(3, roA, 192)
    RES_LD(4, roB, 0) RES_LD(5, roB, 64) RES_LD(6, roB, 128) RES_LD(7, roB, 192)
#undef RES_LD
    BAR();
    red_put(0);
    STAMP(16);
    poll(foB + tgA * 128);
    asm volatile("" : "+v"(res[0]), "+v"(res[1]), "+v"(res[2]), "+v"(res[3]), "+v"(res[4]), "+v"(res[5]), "+v"(res[6]), "+v"(res[7]));
    STAMP(21);
#pragma unroll
    for (int i = 7; i < 14; i++) p_one(pbB, i, QF[i]);
    BAR();
#pragma unroll
    for (int i = 0; i < 16; i++) { ac[0][i] = 0; ac[1][i] = 0; }
    SCHED();
    static_for<0, 14>([&](auto I_) {
      constexpr int i = decltype(I_)::v;
      if constexpr (i >= 7) wait_vm<13 - i>();
      PIN(QF[i]);
      ac[0] = mfma32(D[2 * i], QF[i], ac[0]);
      ac[1] = mfma32(D[2 * i + 1], QF[i], ac[1]);
      if constexpr (i == 1) red_get(0);
      SCHED();
    });
    STAMP(17);
    red_put(1);
    BAR();
    red_get(1);
    u32 o4[4];
    {
      const u32 y0 = pk_bf16(r[0][0], r[0][1]), y1 = pk_bf16(r[0][2], r[0][3]), y2 = pk_bf16(r[1][0], r[1][1]), y3 = pk_bf16(r[1][2], r[1][3]);
      o4[0] = pk_bf16(bf_lo(res[0]) + bf_lo(y0), bf_lo(res[1]) + bf_hi(y0));
      o4[1] = pk_bf16(bf_lo(res[2]) + bf_lo(y1), bf_lo(res[3]) + bf_hi(y1));
      o4[2] = pk_bf16(bf_lo(res[4]) + bf_lo(y2), bf_lo(res[5]) + bf_hi(y2));
      o4[3] = pk_bf16(bf_lo(res[6]) + bf_lo(y3), bf_lo(res[7]) + bf_hi(y3));
    }
    const auto x0 = __builtin_amdgcn_permlane32_swap(o4[0], o4[2], false, false);
    const auto x1 = __builtin_amdgcn_permlane32_swap(o4[1], o4[3], false, false);
    u32x4 v; v[0] = x0[0]; v[1] = x1[0]; v[2] = x0[1]; v[3] = x1[1];
    *(u32x4 *)(p.out + (size_t)((pn2 * 4 + (hh ? tgB : tgA)) * 32 + tl) * DM + own_n) = v;
    STAMP(18);
  }
  asm volatile(".Lcode_end:");
}

extern "C" void kernel_launch(void *const *d_in, const int *in_sizes, int n_in, void *d_out, int out_size, void *d_ws,
                              size_t ws_size, hipStream_t stream) {
  Params p;
  p.x = (const u16 *)d_in[0]; p.logA = (const u16 *)d_in[1]; p.Aim = (const u16 *)d_in[2]; p.Cr = (const u16 *)d_in[3];
  p.Ci = (const u16 *)d_in[4]; p.logdt = (const u16 *)d_in[5]; p.Dsk = (const u16 *)d_in[6];
  p.wg = (const u16 *)d_in[8]; p.wu = (const u16 *)d_in[9]; p.wd = (const u16 *)d_in[10];
  p.out = (u16 *)d_out; p.ws = (char *)d_ws;
  (void)hipFuncSetAttribute((const void *)fused, hipFuncAttributeMaxDynamicSharedMemorySize, LDS_BYTES);
  fused<<<256, 512, LDS_BYTES, stream>>>(p);
}
```

```cpp
#include <hip/hip_runtime.h>
#include <hip/hip_bf16.h>
#include <stdint.h>

#define STAMPS 0

typedef unsigned short u16;
typedef unsigned int u32;
typedef unsigned long long u64;
typedef float f32x2 __attribute__((ext_vector_type(2)));
typedef float f32x4 __attribute__((ext_vector_type(4)));
typedef float f32x16 __attribute__((ext_vector_type(16)));
typedef _Float16 h16x8 __attribute__((ext_vector_type(8)));
typedef _Float16 h16x2 __attribute__((ext_vector_type(2)));
typedef __bf16 b16x8 __attribute__((ext_vector_type(8)));
typedef __bf16 b16x2 __attribute__((ext_vector_type(2)));
typedef short s16x4 __attribute__((ext_vector_type(4)));
typedef u32 u32x2 __attribute__((ext_vector_type(2)));
typedef u32 u32x4 __attribute__((ext_vector_type(4)));
#define LAS __attribute__((address_space(3)))
#define GAS __attribute__((address_space(1)))
#define CAS __attribute__((address_space(4)))

constexpr int SEQ = 2048, DM = 512, NMODE = 128, FF = 1792;

constexpr u32 WS_EPOCH = 0, WS_ST = 4096, WS_FLAG2 = 32768, WS_FLAG3 = 65536, WS_FLAG1 = 196608;
constexpr u32 WS_HT = 1u << 20, WS_P = 4u << 20, WS_DBG = 16u << 20, WS_EX = 12u << 20, WS_XT = 18u << 20;

constexpr u32 LDS_BYTES = 163840;
constexpr u32 XY_STR = 544, XY_CH = 53 * XY_STR;
constexpr u32 L_XY = 0;
constexpr u32 UR_STR = 4640, UR_CH = 4 * UR_STR;
constexpr u32 L_UR = 0;
constexpr u32 L_XS = 37120;
constexpr u32 K_CH = 8448;
constexpr u32 L_K = 45312;
constexpr u32 VT_CH = 1344;
constexpr u32 L_VT = 62464;
constexpr u32 L_MISC = 9216;
constexpr u32 L_YP = 65536;
constexpr u32 T_STR = 1056, L_T = 57664, L_TX = L_T + 8 * T_STR;
constexpr u32 L_W0 = 81920;
constexpr u32 L_W2 = 49152;
constexpr u32 L_A = 0;
constexpr u32 L_ST = 131072;
constexpr u32 L_RED = 0;
constexpr int SPIN_CAP = 1 << 17;

struct Params {
  const u16 *x, *logA, *Aim, *Cr, *Ci, *logdt, *Dsk, *wg, *wu, *wd;
  u16 *out;
  char *ws;
};

__device__ __forceinline__ float bf_lo(u32 v) { return __uint_as_float(v << 16); }
__device__ __forceinline__ float bf_hi(u32 v) { return __uint_as_float(v & 0xffff0000u); }
__device__ __forceinline__ u32 pk_bf16(float a, float b) {
  b16x2 r; r[0] = (__bf16)a; r[1] = (__bf16)b; return __builtin_bit_cast(u32, r);
}
__device__ __forceinline__ float rbf(float a) { return bf_lo(pk_bf16(a, a)); }
__device__ __forceinline__ u32 pk_f16(float a, float b) {
  h16x2 r; r[0] = (_Float16)a; r[1] = (_Float16)b; return __builtin_bit_cast(u32, r);
}
template <typename T> __device__ __forceinline__ LAS T *lds(u32 off) { return (LAS T *)(size_t)off; }
__device__ __forceinline__ u32x2 tr_read(u32 addr) {
  return __builtin_bit_cast(u32x2, __builtin_amdgcn_ds_read_tr16_b64_v4i16((LAS s16x4 *)(size_t)addr));
}
__device__ __forceinline__ u32x4 cat(u32x2 a, u32x2 b) { u32x4 r; r[0] = a[0]; r[1] = a[1]; r[2] = b[0]; r[3] = b[1]; return r; }
__device__ __forceinline__ f32x16 mfma32(u32x4 a, u32x4 b, f32x16 c) {
  return __builtin_amdgcn_mfma_f32_32x32x16_bf16(__builtin_bit_cast(b16x8, a), __builtin_bit_cast(b16x8, b), c, 0, 0, 0);
}
__device__ __forceinline__ f32x4 mfma16h(u32x4 a, u32x4 b, f32x4 c) {
  return __builtin_amdgcn_mfma_f32_16x16x32_f16(__builtin_bit_cast(h16x8, a), __builtin_bit_cast(h16x8, b), c, 0, 0, 0);
}
__device__ __forceinline__ float dot2bf(u32 a, u32 b, float c) {
  return __builtin_amdgcn_fdot2_f32_bf16(__builtin_bit_cast(b16x2, a), __builtin_bit_cast(b16x2, b), c, false);
}
__device__ __forceinline__ void glds(u32 voff, const void *sbase, u32 ldst) {
  asm volatile("s_mov_b32 m0, %2\n\ts_nop 0\n\tglobal_load_lds_dwordx4 %0, %1" ::"v"(voff), "s"(sbase), "s"(ldst) : "memory");
}
__device__ __forceinline__ void glds_sc1(u32 voff, const void *sbase, u32 ldst) {
  asm volatile("s_mov_b32 m0, %2\n\ts_nop 0\n\tglobal_load_lds_dwordx4 %0, %1 sc1" ::"v"(voff), "s"(sbase), "s"(ldst) : "memory");
}
#define WAIT_VM(n) asm volatile("s_waitcnt vmcnt(" #n ")" ::: "memory")
#define WAIT_LGKM0() asm volatile("s_waitcnt lgkmcnt(0)" ::: "memory")
#define BAR() do { asm volatile("s_waitcnt lgkmcnt(0)\n\ts_barrier" ::: "memory"); } while (0)
#define SCHED() __builtin_amdgcn_sched_barrier(0)
#define PIN(x) asm volatile("" : "+v"(x))
#define RAWBAR() asm volatile("s_barrier" ::: "memory")
template <int N> __device__ __forceinline__ void wait_vm() { asm volatile("s_waitcnt vmcnt(%0)" ::"n"(N) : "memory"); }
template <int I> struct IC { static constexpr int v = I; };
template <int B, int E, typename Fn> __device__ __forceinline__ void static_for(Fn &&f) {
  if constexpr (B < E) { f(IC<B>{}); static_for<B + 1, E>(f); }
}
constexpr int G1_OPS[] = {-8, -7, -6, -5, -4, -3, -2, -1, 2, 6, 10, 14, 18, 22, 26, 30,
                          27, 57, 89, 119,
                          62, 90, 122,
                          97, 101, 115,
                          34, 38, 42, 46, 50, 54, 58, 63, 65, 67, 69, 71, 73, 75, 78, 80, 82, 85,
                          96, 98, 100, 102, 104, 106, 108, 110, 112, 114,
                          121, 123, 124, 125, 126, 127};
constexpr int G1_TAKE[] = {66, 69, 72, 98, 101, 104, 110, 116, 122};
constexpr int g1_take(int p) { for (int i = 0; i < 9; i++) if (G1_TAKE[i] == p || G1_TAKE[i] + 1 == p) return 2 * i + p - G1_TAKE[i]; return -1; }
#define LOOK 3
constexpr int epi0(int tg) { return tg == 1 ? 5 : 1; }
constexpr int g1_younger(int x, int p) { int n = 0; for (int q : G1_OPS) n += (q > x && q < p); return n; }
constexpr int g1_find(int first, int cnt, int p) { for (int i = 0; i < cnt; i++) if (G1_OPS[first + i] == p) return i; return -1; }
constexpr int wd_slot(int u) { return u < 4 ? 16 + u : u < 12 ? u - 4 : u < 16 ? u + 4 : u < 18 ? u - 16 : u < 22 ? u - 10 : u - 20; }

template <int CTRL> __device__ __forceinline__ float dpp(float v) {
  return __uint_as_float(__builtin_amdgcn_update_dpp(0u, __float_as_uint(v), CTRL, 0xf, 0xf, true));
}
__device__ __forceinline__ float wave_sum(float v) {
  v += dpp<0xB1>(v); v += dpp<0x4E>(v); v += dpp<0x141>(v); v += dpp<0x140>(v);
  auto a = __builtin_amdgcn_permlane16_swap(__float_as_uint(v), __float_as_uint(v), false, false);
  v = __uint_as_float(a[0]) + __uint_as_float(a[1]);
  auto c = __builtin_amdgcn_permlane32_swap(__float_as_uint(v), __float_as_uint(v), false, false);
  return __uint_as_float(c[0]) + __uint_as_float(c[1]);
}
__device__ __forceinline__ float wave_max(float v) {
  v = fmaxf(v, dpp<0xB1>(v)); v = fmaxf(v, dpp<0x4E>(v)); v = fmaxf(v, dpp<0x141>(v)); v = fmaxf(v, dpp<0x140>(v));
  auto a = __builtin_amdgcn_permlane16_swap(__float_as_uint(v), __float_as_uint(v), false, false);
  v = fmaxf(__uint_as_float(a[0]), __uint_as_float(a[1]));
  auto c = __builtin_amdgcn_permlane32_swap(__float_as_uint(v), __float_as_uint(v), false, false);
  return fmaxf(__uint_as_float(c[0]), __uint_as_float(c[1]));
}
struct cf { float r, i; };
__device__ __forceinline__ cf cmul(cf a, cf b) { return {a.r * b.r - a.i * b.i, a.r * b.i + a.i * b.r}; }
__device__ __forceinline__ cf cexp_at(float lr, float rev, float pos) {
  float e = __builtin_amdgcn_exp2f(lr * pos), f = __builtin_amdgcn_fractf(rev * pos);
  return {e * __builtin_amdgcn_cosf(f), e * __builtin_amdgcn_sinf(f)};
}

#define STAMP(i)

__global__ void __launch_bounds__(512, 2) fused(Params p) {
  extern __shared__ __attribute__((aligned(1024))) char smem_[];
  const u32 tid = threadIdx.x, lane = tid & 63;
  const u32 w = __builtin_amdgcn_readfirstlane(tid >> 6);
  const u32 b = blockIdx.x, xcd = b & 7, jj = b >> 3;
  char *const ws = p.ws;
  STAMP(0);
  const bool g1 = jj < 28;
  const u32 panel = (xcd & 4) * 2 + (jj < 24 ? jj & 7 : (xcd & 1) * 4 + (jj & 3));
  const u32 ft = 7 * ((xcd >> 1) & 1) + (jj < 24 ? (xcd & 1) * 4 + (jj >> 3) : 3);
  const u32 rot1 = (jj >> 3) * 8;
  u32 tag = 0;

  u32x4 W[32];
  const u32 w1_voff = ((lane >> 4) * FF + ft * 128) * 2 + ((((lane & 15) >> 1) ^ ((lane >> 4) | ((w & 1) << 2))) * 32) + (lane & 1) * 16;
  const char *const w1_base = (const char *)((w & 1) ? p.wu : p.wg) + (size_t)((w >> 1) * 4) * (FF * 2);
  auto w1_one = [&](u32 ks, u32 dst) {
    glds(w1_voff, w1_base + (size_t)((ks + rot1) & 31) * 16 * (FF * 2), dst + w * 1024);
  };
  const u32 g4 = lane >> 4, q4 = (lane >> 2) & 3, p4 = lane & 3;
  const u32 w1_rd = (g4 >> 1) * 2048 + (g4 & 1) * 1024 + q4 * 256 + ((w ^ (q4 | ((g4 & 1) << 2))) * 32) + p4 * 8;

  const u32 cp = xcd * 32 + jj, d0 = 2 * cp;
  const u32 ch = w >> 2, job = w & 3;
  u32x4 xr; u32 pl, pa, pr, pi;
  {
    const u32 o1 = (8 * b + w) * (DM * 2) + lane * 16;
    asm volatile("global_load_dwordx4 %0, %1, %2" : "=v"(xr) : "v"(o1), "s"(p.x));
    const u32 o2 = ((d0 + ch) * NMODE + 2 * lane) * 2;
    asm volatile("global_load_dword %0, %1, %2" : "=v"(pl) : "v"(o2), "s"(p.logA));
    asm volatile("global_load_dword %0, %1, %2" : "=v"(pa) : "v"(o2), "s"(p.Aim));
    asm volatile("global_load_dword %0, %1, %2" : "=v"(pr) : "v"(o2), "s"(p.Cr));
    asm volatile("global_load_dword %0, %1, %2" : "=v"(pi) : "v"(o2), "s"(p.Ci));
  }
  u32 junk;
  {
    u64 pc; u32 sz;
    asm volatile(".Lcode_begin:\n\ts_getpc_b64 %0\n\ts_mov_b32 %1, .Lcode_end-.Lcode_begin" : "=s"(pc), "=s"(sz));
    const u32 co = min((jj * 8 + w) * 256 + lane * 4, sz);
    asm volatile("global_load_dword %0, %1, %2" : "=v"(junk) : "v"(co), "s"(pc));
  }
  const u32 ldt2 = *(const CAS u32 *)(p.logdt + d0), dsk2 = *(const CAS u32 *)(p.Dsk + d0);
  asm volatile("s_load_dword %0, %1, 0x0\n\ts_waitcnt lgkmcnt(0)" : "=s"(tag) : "s"(ws) : "memory");
  tag += 1u;
  auto poll = [&](u32 fo) {
#pragma unroll 1
    for (int spin = 0; spin < SPIN_CAP; spin++) {
      u32 f;
      asm volatile("global_load_dword %0, %1, %2 sc1\n\ts_waitcnt vmcnt(0)" : "=&v"(f) : "v"(fo), "s"(ws) : "memory");
      if (__all(f == tag)) break;
      __builtin_amdgcn_s_sleep(1);
    }
  };
  u32 vidx[8];
  {
    const u32 l0 = 8 * (tid & 255);
#pragma unroll
    for (int e = 0; e < 8; e++) {
      const u32 l = l0 + e, bits = pk_bf16((float)l, 0.f) & 0xffffu;
      vidx[e] = L_VT + (tid >> 8) * VT_CH + 2 * (l < 256 ? l : bits - (0x4380u - 256u));
    }
#pragma unroll
    for (int e = 0; e < 8; e++) asm volatile("" : "+v"(vidx[e]));
  }
  {
#pragma unroll
    for (int i = 0; i < 4; i++) w1_one(i, L_W0 + i * 8192);
    const u32 gw = b * 8 + w, po = (gw < 1792 ? gw : gw - 1024) * 1024 + lane * 16;
    glds(po, p.wg, L_W0 + 65536 + w * 1024); glds(po, p.wu, L_W0 + 65536 + w * 1024); glds(po, p.wd, L_W0 + 65536 + w * 1024);
  }
  asm volatile("s_waitcnt vmcnt(12)" : "+v"(xr));
  {
    *lds<u32x4>(L_TX + w * T_STR + lane * 16) = xr;
    float v[8];
#pragma unroll
    for (int i = 0; i < 4; i++) { v[2 * i] = bf_lo(xr[i]); v[2 * i + 1] = bf_hi(xr[i]); }
    const float m = wave_sum(((v[0] + v[1]) + (v[2] + v[3])) + ((v[4] + v[5]) + (v[6] + v[7]))) * (1.f / DM);
    float q0 = 0, q1 = 0;
#pragma unroll
    for (int i = 0; i < 4; i++) { const float t0 = v[i] - m, t1 = v[i + 4] - m; q0 += t0 * t0; q1 += t1 * t1; }
    const float var = rbf(wave_sum(q0 + q1) * (1.f / DM));
    const float rs = __builtin_amdgcn_rcpf(rbf(__builtin_amdgcn_sqrtf(rbf(var + 1e-5f)))), mb = rbf(m);
    u32 uu[4];
#pragma unroll
    for (int i = 0; i < 4; i++) {
      const u32 a = pk_bf16(v[2 * i] - mb, v[2 * i + 1] - mb);
      const u32 u = pk_bf16(bf_lo(a) * rs, bf_hi(a) * rs);
      uu[i] = pk_f16(bf_lo(u), bf_hi(u));
    }
    u32x4 o; o[0] = uu[0]; o[1] = uu[1]; o[2] = uu[2]; o[3] = uu[3];
    *lds<u32x4>(L_T + w * T_STR + lane * 16) = o;
  }
  BAR();
  {
    __amdgpu_buffer_rsrc_t rs = __builtin_amdgcn_make_buffer_rsrc(ws + WS_EX, 0, 8u << 20, 0x00020000);
    const u32 ra = L_T + (4 * (p4 & 1) + 3 - q4) * T_STR + (64 * w + 8 * g4 + 4 * (p4 >> 1)) * 2;
    const u32 rx = L_TX + (3 - q4) * T_STR + (64 * w + 16 * g4 + 4 * p4) * 2;
    const u32 so = ((lane >> 3) * 256 + b) * 128 + (lane & 7) * 16;
    u32x2 r[2];
#pragma unroll
    for (int i = 0; i < 2; i++) r[i] = tr_read(ra + i * 64);
    const u32x4 xt = cat(tr_read(rx + 4 * T_STR), tr_read(rx));
#pragma unroll
    for (int i = 0; i < 2; i++) {
      u32x4 o; o[0] = r[i][0]; o[1] = tag; o[2] = r[i][1]; o[3] = tag;
      __builtin_amdgcn_raw_buffer_store_b128(o, rs, so + (16 * w + 8 * i) * 32768, 0, 16);
    }
    __builtin_amdgcn_raw_buffer_store_b128(xt, rs, so + (WS_XT - WS_EX) + 8 * w * 32768, 0, 16);
  }
  STAMP(1);
  asm volatile("s_waitcnt vmcnt(11)" : "+v"(pl), "+v"(pa), "+v"(pr), "+v"(pi));
  const float dt = rbf(__expf(ch ? bf_hi(ldt2) : bf_lo(ldt2)));
  {
    const float LOG2E = 1.4426950408889634f, INV2PI = 0.15915494309189535f;
    float lr[2], rv[2];
    lr[0] = -rbf(__expf(bf_lo(pl))) * dt * LOG2E; lr[1] = -rbf(__expf(bf_hi(pl))) * dt * LOG2E;
    rv[0] = bf_lo(pa) * dt * INV2PI; rv[1] = bf_hi(pa) * dt * INV2PI;
    const u32 wb = L_XY + ch * XY_CH + lane * 8;
    auto put = [&](int row, cf a, cf c) {
      u32x2 v; v[0] = pk_f16(a.r, a.i); v[1] = pk_f16(c.r, c.i);
      *lds<u32x2>(wb + row * XY_STR) = v;
    };
    auto G = [&](int i) { w1_one(4 + i, L_W0 + (4 + i) * 8192); if (i >= 2) w1_one(6 + i, L_W0 + (6 + i) * 8192); };
    if (job < 2) {
      G(0);
      cf s0 = cexp_at(lr[0], -rv[0], 8.f), s1 = cexp_at(lr[1], -rv[1], 8.f);
      const float p0 = job ? 128.f : 0.f;
      cf z0 = cexp_at(lr[0], -rv[0], p0), z1 = cexp_at(lr[1], -rv[1], p0);
      const u32 wb2 = wb + job * 16 * XY_STR;
#pragma unroll
      for (int i = 0; i < 16; i++) {
        u32x2 v; v[0] = pk_f16(z0.r, z0.i); v[1] = pk_f16(z1.r, z1.i);
        *lds<u32x2>(wb2 + i * XY_STR) = v;
        if (i < 15) { z0 = cmul(z0, s0); z1 = cmul(z1, s1); }
        if (i == 2 || i == 7 || i == 12) G(1 + i / 5);
        SCHED();
      }
    } else {
      G(0);
      cf c0 = {bf_lo(pr), bf_lo(pi)}, c1 = {bf_hi(pr), bf_hi(pi)};
      const float mx = wave_max(fmaxf(fmaxf(fabsf(c0.r), fabsf(c0.i)), fmaxf(fabsf(c1.r), fabsf(c1.i))));
      const int ex = __builtin_amdgcn_frexp_expf(mx);
      const float sc = __builtin_amdgcn_ldexpf(1.f, -ex);
      c0.r *= sc; c0.i *= sc; c1.r *= sc; c1.i *= sc;
      G(1);
      if (job == 2) {
        if (lane == 0) *lds<float>(L_MISC + ch * 4) = __builtin_amdgcn_ldexpf(2.f * dt, ex);
        cf s0 = cexp_at(lr[0], rv[0], 1.f), s1 = cexp_at(lr[1], rv[1], 1.f);
        cf z0 = c0, z1 = c1;
#pragma unroll
        for (int i = 0; i < 8; i++) { put(32 + i, z0, z1); if (i < 7) { z0 = cmul(z0, s0); z1 = cmul(z1, s1); } if (i == 2 || i == 7) G(2 + i / 5); SCHED(); }
        s0 = cmul(s0, s0); s1 = cmul(s1, s1);
        z0 = cmul(c0, cexp_at(lr[0], rv[0], 256.f)); z1 = cmul(c1, cexp_at(lr[1], rv[1], 256.f));
#pragma unroll
        for (int i = 0; i < 4; i++) { put(40 + i, z0, z1); if (i < 3) { z0 = cmul(z0, s0); z1 = cmul(z1, s1); } }
      } else {
        cf f0 = cexp_at(lr[0], rv[0], 4.f), f1 = cexp_at(lr[1], rv[1], 4.f);
        cf s0 = cexp_at(lr[0], rv[0], 256.f), s1 = cexp_at(lr[1], rv[1], 256.f);
        G(2); SCHED();
        cf z0 = cmul(c0, cexp_at(lr[0], rv[0], 512.f)), z1 = cmul(c1, cexp_at(lr[1], rv[1], 512.f));
        put(44, z0, z1); put(45, cmul(z0, f0), cmul(z1, f1));
        z0 = cmul(z0, s0); z1 = cmul(z1, s1);
        put(46, z0, z1); put(47, cmul(z0, f0), cmul(z1, f1));
#pragma unroll
        for (int i = 0; i < 5; i++) { z0 = cmul(z0, s0); z1 = cmul(z1, s1); put(48 + i, z0, z1); if (i == 1) G(3); SCHED(); }
      }
    }
  }
  asm volatile("s_waitcnt vmcnt(0)" : "+v"(junk));
  BAR();
  STAMP(2);
  if (tid == 0) __hip_atomic_store((GAS u32 *)(ws + WS_FLAG1) + b, tag, __ATOMIC_RELAXED, __HIP_MEMORY_SCOPE_AGENT);
  u32x4 ex[2];
  const char *const exb = ws + WS_EX + (size_t)(cp >> 1) * 32768 + (cp & 1) * 32;
  const u32 exo = (tid >> 2) * 128 + (tid & 2) * 32 + (tid & 1) * 16;
  asm volatile("global_load_dwordx4 %0, %1, %2 sc1" : "=v"(ex[0]) : "v"(exo), "s"(exb));
  asm volatile("global_load_dwordx4 %0, %1, %2 sc1" : "=v"(ex[1]) : "v"(exo), "s"(exb + 16384));
  {
    const u32 rb = job >> 1, cb = job & 1;
    const u32 xa = L_XY + ch * XY_CH + (32 + 16 * rb + (lane & 15)) * XY_STR + g4 * 16;
    const u32 ya = L_XY + ch * XY_CH + (16 * cb + (lane & 15)) * XY_STR + g4 * 16;
    u32x4 fa[8], fb[8];
#pragma unroll
    for (int ks = 0; ks < 8; ks++) { fa[ks] = *lds<u32x4>(xa + ks * 64); fb[ks] = *lds<u32x4>(ya + ks * 64); }
    SCHED();
    u32 wr = L_W0 + w1_rd;
    PIN(wr);
#pragma unroll
    for (int ks = 0; ks < 10; ks++) { const u32 a = (ks < 8 ? wr : wr + 65536) + (ks & 7) * 8192; W[ks] = cat(tr_read(a), tr_read(a + 4096)); }
    SCHED();
    const u32 j = 16 * cb + (lane & 15);
    u32 i0, is, i2;
    if (rb == 0) {
      i0 = g4 < 2 ? 4 * g4 + 8 * j : (g4 == 2 ? 256 + 4 * j : 384 + 2 * j); is = 1; i2 = g4 == 3 ? 64 : 2;
    } else {
      i0 = g4 == 0 ? 512 + j : 640 + j; is = g4 == 0 ? 32 : 0; i2 = 2 * is;
    }
    f32x4 a0 = {0, 0, 0, 0}, a1 = {0, 0, 0, 0};
#pragma unroll
    for (int ks = 0; ks < 8; ks += 2) { a0 = mfma16h(fa[ks], fb[ks], a0); a1 = mfma16h(fa[ks + 1], fb[ks + 1], a1); }
    a0 += a1;
    const u32 vb = L_VT + ch * VT_CH;
    if (rb == 0 || g4 == 0) {
#pragma unroll
      for (int r = 0; r < 4; r++) *lds<_Float16>(vb + 2 * (i0 + (r & 1) * is + (r >> 1) * i2)) = (_Float16)a0[r];
    } else if (g4 == 1) {
      *lds<_Float16>(vb + 2 * i0) = (_Float16)a0[0];
    }
  }
  STAMP(4);
  asm volatile("s_waitcnt vmcnt(0)" : "+v"(ex[0]), "+v"(ex[1]));
#pragma unroll 1
  for (int spin = 0; spin < SPIN_CAP; spin++) {
    if (__all(ex[0][1] == tag && ex[0][3] == tag && ex[1][1] == tag && ex[1][3] == tag)) break;
    __builtin_amdgcn_s_sleep(1);
    asm volatile("global_load_dwordx4 %0, %2, %3 sc1\n\tglobal_load_dwordx4 %1, %2, %4 sc1\n\ts_waitcnt vmcnt(0)"
                 : "=&v"(ex[0]), "=&v"(ex[1]) : "v"(exo), "s"(exb), "s"(exb + 16384) : "memory");
  }
  BAR();
  STAMP(5);
#pragma unroll
  for (int i = 0; i < 4; i++) w1_one(10 + i, L_W0 + i * 8192);
  {
    const u32 ab = L_UR + 3 * UR_STR + (tid & 1) * UR_CH + (2044 - 1024 - 8 * (tid >> 2) - 2 * (tid & 2)) * 2;
#pragma unroll
    for (int i = 0; i < 2; i++) { u32x2 v; v[0] = ex[i][0]; v[1] = ex[i][2]; *lds<u32x2>(ab + (1 - i) * 2048) = v; }
  }
  {
    u32x4 kv;
    _Float16 t[8];
#pragma unroll
    for (int e = 0; e < 8; e++) t[e] = *lds<_Float16>(vidx[e]);
#pragma unroll
    for (int e = 0; e < 4; e++) { h16x2 t2; t2[0] = t[2 * e]; t2[1] = t[2 * e + 1]; kv[e] = __builtin_bit_cast(u32, t2); }
    const u32 kb = L_K + (tid >> 8) * K_CH + (tid & 255) * 16;
    u32x2 lo, hi; lo[0] = kv[0]; lo[1] = kv[1]; hi[0] = kv[2]; hi[1] = kv[3];
    *lds<u32x2>(kb + 120) = lo; *lds<u32x2>(kb + 128) = hi;
    *lds<u32x4>(kb + 4224 + 112) = kv;
    const u32x2 z2 = {0, 0};
    u32 za;
    if (tid < 64) {
      const u32 c = tid >> 5, i = tid & 31;
      za = L_K + c * K_CH + (i < 15 ? i * 8 : 4224 + (i < 29 ? i - 15 : 0) * 8);
    } else {
      const u32 i = tid < 448 ? tid - 64 : 0, c = i / 48;
      za = L_UR + c * UR_STR + 4096 + (i - c * 48) * 8;
    }
    *lds<u32x2>(za) = z2;
  }
#pragma unroll
  for (int i = 4; i < 8; i++) w1_one(10 + i, L_W0 + i * 8192);
  BAR();
  {
    const u32 ub = L_UR + (tid >> 8) * UR_CH + (tid & 255) * 16;
    const u32x4 a = *lds<u32x4>(ub + 3 * UR_STR);
    const u32x2 c = *lds<u32x2>(ub + 3 * UR_STR + 16);
    u32x4 o;
    o[0] = __builtin_amdgcn_alignbit(a[1], a[0], 16); o[1] = __builtin_amdgcn_alignbit(a[2], a[1], 16);
    o[2] = __builtin_amdgcn_alignbit(a[3], a[2], 16); o[3] = __builtin_amdgcn_alignbit(c[0], a[3], 16);
    *lds<u32x4>(ub + 2 * UR_STR) = o;
    u32x4 o1; o1[0] = a[1]; o1[1] = a[2]; o1[2] = a[3]; o1[3] = c[0];
    *lds<u32x4>(ub + UR_STR) = o1;
    u32x4 o0; o0[0] = o[1]; o0[1] = o[2]; o0[2] = o[3]; o0[3] = __builtin_amdgcn_alignbit(c[1], c[0], 16);
    *lds<u32x4>(ub) = o0;
  }
  BAR();
  STAMP(6);
  u32 f1;
  const u32 xp = 255 - 32 * w - (lane >> 1);
  const u32 pg = w & 1, cg = (w >> 1) & 1;
  f32x4 ya[4];
#pragma unroll
  for (int i = 0; i < 4; i++) ya[i] = f32x4{0, 0, 0, 0};
  {
    const u32 c16 = lane & 15;
    const u32 ka = L_K + ch * K_CH + (c16 & 1) * 4224 + (c16 >> 1) * 16 + g4 * 16 + cg * 4 * 64;
    const u32 ua = L_UR + ch * UR_CH + (c16 & 3) * UR_STR + (1984 - 1792 - 64 * (c16 >> 2) + 8 * g4) * 2 + cg * 4 * 64;
    u32x4 kf[2][8], uf[2][8];
    auto rd1 = [&](int s, int j) {
      if (j >= 1 && j <= 8) uf[s & 1][8 - j] = *lds<u32x4>(ua + (j - 1) * 512 + s * 64);
      else kf[s & 1][j ? j - 8 : 0] = *lds<u32x4>(ka + (j ? j - 8 : 0) * 512 + s * 64);
    };
    auto rd = [&](int s, int i) { rd1(s, 2 * i); rd1(s, 2 * i + 1); };
    u32 wr = L_W0 + w1_rd;
    PIN(wr);
    auto side = [&](int s, int i) {
      const int n = s * 8 + i;
      if (n == 0) asm volatile("global_load_dword %0, %1, %2 sc1" : "=v"(f1) : "v"(xp * 4), "s"(ws + WS_FLAG1));
      if (n == 1 || n == 3) w1_one(26 + n / 3, L_W0 + (8 + n / 3) * 8192);
      if (n == 4) { WAIT_VM(3); RAWBAR(); }
      if (n >= 4 && n < 8) {
#pragma unroll
        for (int k = 2 * n - 8; k < 2 * n - 6; k++) W[10 + k] = cat(tr_read(wr + k * 8192), tr_read(wr + k * 8192 + 4096));
      }
      if (n == 9) {
        asm volatile("s_waitcnt vmcnt(2)" : "+v"(f1));
        if (!__all(f1 == tag)) poll(WS_FLAG1 + xp * 4);
        glds_sc1(xp * 128 + (lane & 1) * 16, ws + WS_XT + (size_t)(cp >> 2) * 32768 + (cp & 3) * 32, L_XS + w * 1024);
      }
      if (n == 12) BAR();
      if (n >= 12 && n < 28 && !(n & 1)) w1_one(18 + (n - 12) / 2, L_W0 + (n - 12) / 2 * 8192);
    };
#pragma unroll
    for (int i = 0; i < 8; i++) rd(0, i);
    if (pg == 0) {
#pragma unroll
      for (int s = 0; s < 4; s++) {
#pragma unroll
        for (int i = 0; i < 8; i++) {
          if (s < 3) rd(s + 1, i);
          side(s, i);
          ya[3] = mfma16h(uf[s & 1][7 - i], kf[s & 1][i], ya[3]);
          if (i <= 4) ya[2] = mfma16h(uf[s & 1][4 - i], kf[s & 1][i], ya[2]);
          if (i <= 3) ya[1] = mfma16h(uf[s & 1][3 - i], kf[s & 1][i], ya[1]);
          if (i <= 0) ya[0] = mfma16h(uf[s & 1][0 - i], kf[s & 1][i], ya[0]);
          SCHED();
        }
      }
    } else {
#pragma unroll
      for (int s = 0; s < 4; s++) {
#pragma unroll
        for (int i = 0; i < 8; i++) {
          if (s < 3) rd(s + 1, i);
          side(s, i);
          if (i <= 6) ya[3] = mfma16h(uf[s & 1][6 - i], kf[s & 1][i], ya[3]);
          if (i <= 5) ya[2] = mfma16h(uf[s & 1][5 - i], kf[s & 1][i], ya[2]);
          if (i <= 2) ya[1] = mfma16h(uf[s & 1][2 - i], kf[s & 1][i], ya[1]);
          if (i <= 1) ya[0] = mfma16h(uf[s & 1][1 - i], kf[s & 1][i], ya[0]);
          SCHED();
        }
      }
    }
  }
  WAIT_VM(8);
  {
    const u32 yb = L_YP + w * 2048 + lane * 16;
    *lds<f32x4>(yb) = cg ? ya[0] : ya[2];
    *lds<f32x4>(yb + 1024) = cg ? ya[1] : ya[3];
  }
  BAR();
  STAMP(7);
  w1_one(28, L_W2); w1_one(29, L_W2 + 8192);
  {
    const u32 yb = L_YP + (w ^ 2) * 2048 + lane * 16;
    const float ksc = *lds<float>(L_MISC + ch * 4), dsk = ch ? bf_hi(dsk2) : bf_lo(dsk2);
    __amdgpu_buffer_rsrc_t rs = __builtin_amdgcn_make_buffer_rsrc(ws + WS_HT, 0, SEQ * DM * 2, 0x00020000);
    f32x4 o[2]; u32x2 uu[2], xx[2]; u32 t0[2];
#pragma unroll
    for (int i = 0; i < 2; i++) {
      o[i] = *lds<f32x4>(yb + i * 1024);
      t0[i] = 256 * (4 * cg + (i ? 3 - pg : pg)) + 4 * lane;
      uu[i] = *lds<u32x2>(L_UR + ch * UR_CH + 3 * UR_STR + (2044 - t0[i]) * 2);
      xx[i] = *lds<u32x2>(L_XS + ((2044 - t0[i]) >> 3) * 32 + ch * 16 + ((2044 - t0[i]) & 7) * 2);
    }
#pragma unroll
    for (int i = 0; i < 2; i++) {
      const f32x4 own = cg ? (i ? ya[3] : ya[2]) : (i ? ya[1] : ya[0]);
      const u32 uu0 = uu[i][0], uu1 = uu[i][1];
      const h16x2 ua = __builtin_bit_cast(h16x2, uu1), ub = __builtin_bit_cast(h16x2, uu0);
      const float uv[4] = {(float)ua[1], (float)ua[0], (float)ub[1], (float)ub[0]};
      const float xv[4] = {bf_hi(xx[i][1]), bf_lo(xx[i][1]), bf_hi(xx[i][0]), bf_lo(xx[i][0])};
      float hv[4];
#pragma unroll
      for (int r = 0; r < 4; r += 2) {
        const u32 y2 = pk_bf16((o[i][r] + own[r]) * ksc, (o[i][r + 1] + own[r + 1]) * ksc);
        const u32 d2 = pk_bf16(uv[r] * dsk, uv[r + 1] * dsk);
        const u32 z2 = pk_bf16(bf_lo(y2) + bf_lo(d2), bf_hi(y2) + bf_hi(d2));
        hv[r] = xv[r] + bf_lo(z2); hv[r + 1] = xv[r + 1] + bf_hi(z2);
      }
      u32x2 hv2; hv2[0] = pk_bf16(hv[0], hv[1]); hv2[1] = pk_bf16(hv[2], hv[3]);
      const u32 off = ((t0[i] >> 5) * DM + d0 + ch) * 64 + (t0[i] & 31) * 2;
      __builtin_amdgcn_raw_buffer_store_b64(hv2, rs, off, 0, 16);
    }
  }
  SCHED();
  if (b == 0 && tid == 0)
    __hip_atomic_store((GAS u32 *)(ws + WS_EPOCH), tag, __ATOMIC_RELAXED, __HIP_MEMORY_SCOPE_AGENT);
  BAR();
  f32x16 cs;
#pragma unroll
  for (int i = 0; i < 16; i++) cs[i] = 0;
  const u32x4 ones = {0x3f803f80u, 0x3f803f80u, 0x3f803f80u, 0x3f803f80u};
  w1_one(30, L_YP); w1_one(31, L_YP + 8192);
#pragma unroll
  for (int ks = 0; ks < 10; ks++) cs = mfma32(W[ks], ones, cs);
  SCHED();
  WAIT_VM(2);
  RAWBAR();
  if (tid == 0) __hip_atomic_store((GAS u32 *)(ws + WS_FLAG2) + b * 32, tag, __ATOMIC_RELAXED, __HIP_MEMORY_SCOPE_AGENT);
  STAMP(8);
  const u32 t32 = lane & 31;
  const u32 nt = jj & 7, tt = jj >> 3, pn2 = 2 * xcd + (tt >> 1), tgA = tt & 1, tgB = 3 - tgA;
  const u32 lv = lane * 16, rot2 = nt == 7 ? 0 : nt;
  auto ft_of = [&](int i) { const u32 k = (i % 7) + rot2; return 2 * (k >= 7 ? k - 7 : k) + (i < 7 ? tgA ^ 1 : tgA); };
  const u32 d_voff = (lane >> 3) * (DM * 2) + (((lane & 7) ^ (((lane >> 4) & 1) << 2)) * 16);
  const char *const wd_base = (const char *)p.wd + (size_t)(w * 16) * (DM * 2) + nt * 128;
  auto wd_one = [&](int u) {
    const u32 k = __builtin_amdgcn_readfirstlane((ft_of(u >> 1) << 17) + ((u & 1) << 13));
    glds(d_voff, wd_base + k, wd_slot(u) * 8192 + w * 1024);
  };
  u32x4 D[28];
  u32 d_rd[2][3];
#pragma unroll
  for (int h = 0; h < 2; h++) {
    d_rd[h][0] = w * 1024 + (((4 * (g4 >> 1) + q4) * 128 + h * 64 + (g4 & 1) * 32 + p4 * 8) ^ (((q4 >> 1) & 1) * 64));
    d_rd[h][1] = d_rd[h][0] + 65536; d_rd[h][2] = d_rd[h][0] + 131072;
  }
  auto wd_take = [&](int j, int h) {
    const u32 o = wd_slot(2 * j) * 8192;
    D[2 * j + h] = cat(tr_read(d_rd[h][o >> 16] + (o & 65535)), tr_read(d_rd[h][o >> 16] + (o & 65535) + 8192));
  };
  auto lane_again = []() {
    u32 l; asm volatile("v_mbcnt_lo_u32_b32 %0, -1, 0\n\tv_mbcnt_hi_u32_b32 %0, -1, %0" : "=v"(l)); return l;
  };
  auto foA_of = [&]() { return WS_FLAG3 + ((pn2 * 4 + tgA) * 16 + min(lane_again(), 13u)) * 128; };
  __amdgpu_buffer_rsrc_t prs = __builtin_amdgcn_make_buffer_rsrc(ws + WS_P, 0, SEQ * FF * 2, 0x00020000);
  __amdgpu_buffer_rsrc_t frs = __builtin_amdgcn_make_buffer_rsrc(ws + WS_FLAG3, 0, 131072, 0x00020000);
  const u32 OOB = 0x40000000u;
  const u32 p_off = g1 ? (panel * 4 * 112 + ft * 8 + w) * 1024 + lane * 16 : OOB;
  const u32 f_off = g1 && tid == 0 ? (panel * 64 + ft) * 128 : OOB;
  auto tg_of = [&](int g) { return g < 2 ? (u32)g : g ^ (ft & 1); };
  float e_m, e_ne, e_s2, e_g, e_u, e_e, e_o;
  u32 pko[4];
  u32x2 sg;
  const u32 sg_off = WS_ST + (panel * 128 + t32) * 8;
  auto st_take = [&](int tg) {
    PIN(sg);
#pragma unroll 1
    for (int spin = 0; spin < SPIN_CAP; spin++) {
      if (__all(sg[1] == tag)) break;
      __builtin_amdgcn_s_sleep(1);
      asm volatile("global_load_dwordx2 %0, %1, %2 sc1\n\ts_waitcnt vmcnt(0)" : "=&v"(sg) : "v"(sg_off), "s"(ws + tg_of(tg) * 256) : "memory");
    }
    e_m = bf_lo(sg[0]);
    const float sd = bf_hi(sg[0]);
    e_ne = -1.4426950408889634f * __builtin_amdgcn_rcpf(sd); e_s2 = sd * sd;
  };
  auto epi_step = [&](int ks, int tg, const f32x16 &prev) {
    if (ks == 0) st_take(tg);
    else if (ks >= 1 && ks < 25) {
      const int i = (ks - 1) / 3, ph = (ks - 1) % 3;
      if (ph == 0) {
        e_g = prev[i] - e_m * cs[i]; PIN(e_g);
        e_e = __builtin_amdgcn_exp2f(e_g * e_ne);
      } else if (ph == 1) {
        e_u = (prev[i + 8] - e_m * cs[i + 8]) * e_g;
        e_e = e_e * e_s2 + e_s2;
        PIN(e_u); PIN(e_e);
      } else {
        const float o = e_u * __builtin_amdgcn_rcpf(e_e);
        if (i & 1) { pko[i >> 1] = pk_bf16(e_o, o); PIN(pko[i >> 1]); } else { e_o = o; PIN(e_o); }
      }
    } else if (ks == 25) {
      u32x4 v; v[0] = pko[0]; v[1] = pko[1]; v[2] = pko[2]; v[3] = pko[3];
      __builtin_amdgcn_raw_buffer_store_b128(v, prs, p_off + tg_of(tg) * (112 * 1024), 0, 16);
    }
  };
  const char *const pbA = ws + WS_P + ((size_t)(pn2 * 4 + tgA) * 112 + w) * 1024;
  const char *const pbB = ws + WS_P + ((size_t)(pn2 * 4 + tgB) * 112 + w) * 1024;
  u32x4 PF[14], QF[14];
  auto p_one = [&](const char *pb, int i, u32x4 &d) {
    const u32 j = __builtin_amdgcn_readfirstlane(ft_of(i) << 13);
    asm volatile("global_load_dwordx4 %0, %1, %2 sc1" : "=v"(d) : "v"(lv), "s"(pb + j));
  };
  f32x16 acc[2];
  if (g1) {
    {
      u32 wr = L_W0 + w1_rd;
      PIN(wr);
#pragma unroll
      for (int ks = 0; ks < 8; ks++) W[18 + ks] = cat(tr_read(wr + ks * 8192), tr_read(wr + ks * 8192 + 4096));
      wr += 65536;
#pragma unroll
      for (int ks = 0; ks < 2; ks++) W[26 + ks] = cat(tr_read(wr + ks * 8192), tr_read(wr + ks * 8192 + 4096));
      wr = L_W2 + w1_rd;
      PIN(wr);
#pragma unroll
      for (int ks = 0; ks < 2; ks++) W[28 + ks] = cat(tr_read(wr + ks * 8192), tr_read(wr + ks * 8192 + 4096));
#pragma unroll
      for (int ks = 10; ks < 18; ks++) cs = mfma32(W[ks], ones, cs);
      SCHED();
      if (w == 0) WAIT_VM(1); else WAIT_VM(0);
      RAWBAR();
#pragma unroll
      for (int ks = 2; ks < 4; ks++) W[28 + ks] = cat(tr_read(wr + ks * 8192), tr_read(wr + ks * 8192 + 4096));
    }
    STAMP(9);
    poll(WS_FLAG2 + (w * 32 + t32) * 128);
    BAR();
    STAMP(10);
    const char *const a_base = ws + WS_HT + (size_t)panel * 131072;
    auto a_one = [&](int i) { glds_sc1(lv, a_base + tg_of(i >> 2) * 32768 + (((i & 3) * 8 + w + rot1) & 31) * 1024, L_A + w * 1024 + i * 8192); };
#pragma unroll
    for (int i = 0; i < 4; i++) a_one(i);
#pragma unroll
    for (int ks = 18; ks < 32; ks++) { cs = mfma32(W[ks], ones, cs); if (ks % 3 == 0 && ks < 30) { a_one(ks / 3 - 2); SCHED(); } }
    SCHED();
    u32 ab = L_A + (4 * (g4 >> 1) + q4) * 64 + (g4 & 1) * 32 + p4 * 8;
    u32x4 F[LOOK + 1];
    auto frag = [&](u32 a) { return cat(tr_read(a), tr_read(a + 512)); };
    u32 fa;
    wait_vm<g1_younger(-8, 0)>();
    RAWBAR();
    u32 ab2 = ab + 65536;
    PIN(ab); PIN(ab2);
#pragma unroll
    for (int i = 0; i < LOOK; i++) F[i] = frag(ab + i * 1024);
    static_for<0, 128>([&](auto P_) {
      constexpr int P = decltype(P_)::v, tg = P >> 5, ks = P & 31;
      if constexpr (ks == 0) {
#pragma unroll
        for (int i = 0; i < 16; i++) acc[tg & 1][i] = 0;
      }
      if constexpr (P == 8 - LOOK) { wait_vm<g1_younger(-7, P)>(); RAWBAR(); }
      if constexpr (P == 16 - LOOK) { wait_vm<g1_younger(-6, P)>(); RAWBAR(); }
      if constexpr (P == 24 - LOOK) { wait_vm<g1_younger(-5, P)>(); RAWBAR(); }
      if constexpr (P == 32 - LOOK) { wait_vm<g1_younger(-1, P)>(); RAWBAR(); }
      if constexpr (P == 48) { wait_vm<g1_younger(14, P)>(); RAWBAR(); }
      if constexpr (P == 64) { wait_vm<g1_younger(30, P)>(); RAWBAR(); }
      if constexpr (P == 66) wait_vm<g1_younger(54, P)>();
      if constexpr (P == 96) { wait_vm<g1_younger(85, P)>(); RAWBAR(); }
      if constexpr (P == 101) { wait_vm<g1_younger(90, P)>(); RAWBAR(); }
      if constexpr (g1_find(23, 2, P) >= 0) __builtin_amdgcn_raw_buffer_store_b32(tag, frs, f_off, 2048 * g1_find(23, 2, P), 16);
      if constexpr (P == G1_OPS[25]) asm volatile("global_load_dword %0, %1, %2 sc1" : "=v"(fa) : "v"(foA_of()), "s"(ws));
      if constexpr (P == 121) {
        wait_vm<g1_younger(115, P)>();
        PIN(fa);
        if (!__all(fa == tag)) poll(foA_of());
      }
      if constexpr (g1_find(54, 6, P) >= 0) p_one(pbA, g1_find(54, 6, P), PF[g1_find(54, 6, P)]);
      if constexpr (g1_find(8, 8, P) >= 0) a_one(8 + g1_find(8, 8, P));
      if constexpr (g1_find(16, 4, P) >= 0)
        asm volatile("global_load_dwordx2 %0, %1, %2 sc1" : "=v"(sg) : "v"(sg_off), "s"(ws + tg_of(g1_find(16, 4, P)) * 256));
      if constexpr (g1_find(26, 28, P) >= 0) wd_one(g1_find(26, 28, P));
      if constexpr (g1_take(P) >= 0) wd_take(g1_take(P) >> 1, g1_take(P) & 1);
      constexpr int n = P + LOOK;
      if constexpr (n < 128) F[n % (LOOK + 1)] = frag((n < 64 ? ab : ab2) + (n & 63) * 1024);
      acc[tg & 1] = mfma32(W[ks], F[P % (LOOK + 1)], acc[tg & 1]);
      if constexpr (tg > 0 && ks == epi0(tg)) wait_vm<g1_younger(G1_OPS[16 + tg - 1], P + 1)>();
      if constexpr (tg > 0 && ks >= epi0(tg)) epi_step(ks - epi0(tg), tg - 1, acc[(tg - 1) & 1]);
      SCHED();
      if constexpr (P == 31) STAMP(11);
      if constexpr (P == 63) STAMP(19);
      if constexpr (P == 95) STAMP(20);
      if constexpr (P == 127) STAMP(3);
    });
    wait_vm<g1_younger(119, 128)>();
    st_take(3);
    STAMP(12);
  } else {
    poll(WS_FLAG2 + (w * 32 + t32) * 128);
    RAWBAR();
    const u32 tgs = (2 * xcd + ((jj >> 1) & 1)) * 4 + (jj & 1);
    const char *base = ws + WS_HT + (size_t)tgs * 32768 + w * 4096;
#pragma unroll
    for (int i = 0; i < 8; i++) glds_sc1(lv, base + (i >> 2) * 65536 + (i & 3) * 1024, 65536 + (i >> 2) * 32768 + w * 4096 + (i & 3) * 1024);
    const u32 ab = 65536 + w * 4096 + (4 * (g4 >> 1) + q4) * 64 + (g4 & 1) * 32 + p4 * 8;
#pragma unroll
    for (int g = 0; g < 2; g++) {
      if (g == 0) WAIT_VM(4); else WAIT_VM(0);
      float s_s = 0, s_q = 0;
#pragma unroll
      for (int i = 0; i < 4; i++) {
        const u32x4 f = cat(tr_read(ab + g * 32768 + i * 1024), tr_read(ab + g * 32768 + i * 1024 + 512));
#pragma unroll
        for (int k = 0; k < 4; k++) { s_s = dot2bf(f[k], 0x3f803f80u, s_s); s_q = dot2bf(f[k], f[k], s_q); }
      }
      const auto sw = __builtin_amdgcn_permlane32_swap(__float_as_uint(s_s), __float_as_uint(s_q), false, false);
      *lds<float>(g * 2048 + w * 256 + lane * 4) = __uint_as_float(sw[0]) + __uint_as_float(sw[1]);
      BAR();
      float sp = 0;
#pragma unroll
      for (int i = 0; i < 8; i++) sp += *lds<float>(g * 2048 + i * 256 + lane * 4);
      const auto s2 = __builtin_amdgcn_permlane32_swap(__float_as_uint(sp), __float_as_uint(sp), false, false);
      const float mean = __uint_as_float(s2[0]) * (1.f / DM);
      const float var = rbf(fmaxf(__uint_as_float(s2[1]) * (1.f / DM) - mean * mean, 0.f));
      const u64 gr = ((u64)tag << 32) | pk_bf16(mean, __builtin_amdgcn_sqrtf(rbf(var + 1e-5f)));
      if (tid < 32) __hip_atomic_store((GAS u64 *)(ws + WS_ST) + (tgs + 2 * g) * 32 + lane, gr, __ATOMIC_RELAXED, __HIP_MEMORY_SCOPE_AGENT);
    }
    BAR();
#pragma unroll
    for (int u = 0; u < 12; u++) wd_one(u);
    WAIT_VM(0);
#pragma unroll
    for (int j = 0; j < 6; j++) { wd_take(j, 0); wd_take(j, 1); }
    WAIT_LGKM0();
#pragma unroll
    for (int u = 12; u < 28; u++) wd_one(u);
    poll(foA_of());
#pragma unroll
    for (int j = 6; j < 9; j++) { wd_take(j, 0); wd_take(j, 1); }
#pragma unroll
    for (int i = 0; i < 6; i++) p_one(pbA, i, PF[i]);
    asm volatile("s_waitcnt vmcnt(0)" : "+v"(PF[0]), "+v"(PF[1]), "+v"(PF[2]), "+v"(PF[3]), "+v"(PF[4]), "+v"(PF[5]));
#pragma unroll
    for (int i = 0; i < 16; i++) acc[1][i] = 0;
    e_m = 0; e_ne = 0; e_s2 = 1.f;
  }

  {
    const u32 ln = lane_again(), hh = ln >> 5, tl = ln & 31;
    const u32 foB = WS_FLAG3 + ((pn2 * 4 + tgB) * 16 + 2 * min(ln, 6u)) * 128;
    p_one(pbA, 6, PF[6]); p_one(pbA, 7, PF[7]);
    SCHED();
    {
      const f32x2 m2 = {e_m, e_m}, ne2 = {e_ne, e_ne}, s22 = {e_s2, e_s2};
      u32x4 v;
#pragma unroll
      for (int i = 0; i < 8; i += 2) {
        const f32x2 g = f32x2{acc[1][i], acc[1][i + 1]} - m2 * f32x2{cs[i], cs[i + 1]};
        const f32x2 u = (f32x2{acc[1][i + 8], acc[1][i + 9]} - m2 * f32x2{cs[i + 8], cs[i + 9]}) * g;
        f32x2 e = g * ne2;
        e[0] = __builtin_amdgcn_exp2f(e[0]); e[1] = __builtin_amdgcn_exp2f(e[1]);
        e = e * s22 + s22;
        e[0] = __builtin_amdgcn_rcpf(e[0]); e[1] = __builtin_amdgcn_rcpf(e[1]);
        const f32x2 o = u * e;
        v[i >> 1] = pk_bf16(o[0], o[1]);
      }
      __builtin_amdgcn_raw_buffer_store_b128(v, prs, p_off + tg_of(3) * (112 * 1024), 0, 16);
    }
    SCHED();
    STAMP(13);
    wait_vm<8>();
    RAWBAR();
    __builtin_amdgcn_raw_buffer_store_b32(tag, frs, f_off, tg_of(2) * 2048, 16);
    const u32 own_n = nt * 64 + w * 8;
    u32 res[8];
    const u32 roA = WS_HT + ((pn2 * 4 + tgA) * DM + own_n + 4 * hh) * 64 + tl * 2;
    const u32 roB = WS_HT + ((pn2 * 4 + tgB) * DM + own_n + 4 * hh) * 64 + tl * 2;
    f32x16 ac[2];
    f32x4 r[2];
    f32x16 bc[2];
    u32 rw = w * 1024 + lv;
    PIN(rw);
    auto red_put = [&](int t, const f32x16 &a, int o) {
      f32x4 v; v[0] = a[4 * (o & 3)]; v[1] = a[4 * (o & 3) + 1]; v[2] = a[4 * (o & 3) + 2]; v[3] = a[4 * (o & 3) + 3];
      *lds<f32x4>((t ? rw + 65536 : rw) + o * 8192) = v;
    };
    auto red_get = [&](int t) {
      f32x4 v[8];
#pragma unroll
      for (int i = 0; i < 8; i++) v[i] = *lds<f32x4>(t * 65536 + w * 8192 + i * 1024 + lv);
      r[t] = ((v[0] + v[1]) + (v[2] + v[3])) + ((v[4] + v[5]) + (v[6] + v[7]));
    };
#pragma unroll
    for (int i = 0; i < 16; i++) { ac[0][i] = 0; ac[1][i] = 0; }
    u32 fb;
    static_for<0, 14>([&](auto I_) {
      constexpr int i = decltype(I_)::v;
      constexpr int YOUNGER[10] = {0, 12, 13, 12, 11, 10, 10, 8, 7, 6};
      if constexpr (i < 3) { p_one(pbA, 8 + 2 * i, PF[8 + 2 * i]); p_one(pbA, 9 + 2 * i, PF[9 + 2 * i]); }
      if constexpr (i == 6) asm volatile("global_load_dword %0, %1, %2 sc1" : "=v"(fb) : "v"(foB + (tgA ^ 1) * 128), "s"(ws));
      if constexpr (i >= 1 && i < 10) wait_vm<YOUNGER[i < 10 ? i : 0]>();
      if constexpr (i == 7) {
        RAWBAR();
        __builtin_amdgcn_raw_buffer_store_b32(tag, frs, f_off, tg_of(3) * 2048, 16);
        STAMP(14);
      }
      if constexpr (i == 10) {
        if (w == 0) wait_vm<2>();
        else {
          wait_vm<0>();
          u32 fc;
          asm volatile("v_mov_b32 %0, %1" : "=v"(fc) : "v"(fb));
          if (!__all(fc == tag)) poll(foB + (tgA ^ 1) * 128);
        }
        BAR();
#pragma unroll
        for (int k = 0; k < 7; k++) p_one(pbB, k, QF[k]);
        STAMP(15);
      }
      if constexpr (i < 5) { wd_take(9 + i, 0); wd_take(9 + i, 1); }
      PIN(PF[i]);
      ac[0] = mfma32(D[2 * i], PF[i], ac[0]);
      ac[1] = mfma32(D[2 * i + 1], PF[i], ac[1]);
      SCHED();
    });
#define RES_LD(i, ro, o) asm volatile("global_load_ushort %0, %1, %2 offset:" #o " sc1" : "=v"(res[i]) : "v"(ro), "s"(ws));
    RES_LD(0, roA, 0) RES_LD(1, roA, 64) RES_LD(2, roA, 128) RES_LD(3, roA, 192)
    RES_LD(4, roB, 0) RES_LD(5, roB, 64) RES_LD(6, roB, 128) RES_LD(7, roB, 192)
#undef RES_LD
    STAMP(16);
#pragma unroll
    for (int i = 0; i < 16; i++) { bc[0][i] = 0; bc[1][i] = 0; }
    SCHED();
    static_for<0, 14>([&](auto I_) {
      constexpr int i = decltype(I_)::v;
      if constexpr (i == 7) {
        poll(foB + tgA * 128);
        asm volatile("" : "+v"(res[0]), "+v"(res[1]), "+v"(res[2]), "+v"(res[3]), "+v"(res[4]), "+v"(res[5]), "+v"(res[6]), "+v"(res[7]), "+v"(fb));
        STAMP(21);
#pragma unroll
        for (int k = 7; k < 14; k++) p_one(pbB, k, QF[k]);
        BAR();
        red_get(0);
      }
      wait_vm<i < 7 ? 14 - i : 13 - i>();
      PIN(QF[i]);
      bc[0] = mfma32(D[2 * i], QF[i], bc[0]);
      if constexpr (i < 4) red_put(0, ac[i >> 1], 2 * i);
      SCHED();
      if constexpr (i < 8) bc[1] = mfma32(D[2 * i + 1], QF[i], bc[1]);
      if constexpr (i < 4) red_put(0, ac[i >> 1], 2 * i + 1);
      SCHED();
    });
#pragma unroll
    for (int i = 8; i < 14; i++) {
      bc[1] = mfma32(D[2 * i + 1], QF[i], bc[1]);
      if (i >= 9 && i < 13) red_put(1, bc[0], i - 9);
      SCHED();
    }
    STAMP(17);
#pragma unroll
    for (int o = 4; o < 8; o++) red_put(1, bc[1], o);
    BAR();
    red_get(1);
    u32 o4[4];
    {
      const u32 y0 = pk_bf16(r[0][0], r[0][1]), y1 = pk_bf16(r[0][2], r[0][3]), y2 = pk_bf16(r[1][0], r[1][1]), y3 = pk_bf16(r[1][2], r[1][3]);
      o4[0] = pk_bf16(bf_lo(res[0]) + bf_lo(y0), bf_lo(res[1]) + bf_hi(y0));
      o4[1] = pk_bf16(bf_lo(res[2]) + bf_lo(y1), bf_lo(res[3]) + bf_hi(y1));
      o4[2] = pk_bf16(bf_lo(res[4]) + bf_lo(y2), bf_lo(res[5]) + bf_hi(y2));
      o4[3] = pk_bf16(bf_lo(res[6]) + bf_lo(y3), bf_lo(res[7]) + bf_hi(y3));
    }
    const auto x0 = __builtin_amdgcn_permlane32_swap(o4[0], o4[2], false, false);
    const auto x1 = __builtin_amdgcn_permlane32_swap(o4[1], o4[3], false, false);
    u32x4 v; v[0] = x0[0]; v[1] = x1[0]; v[2] = x0[1]; v[3] = x1[1];
    *(u32x4 *)(p.out + (size_t)((pn2 * 4 + (hh ? tgB : tgA)) * 32 + tl) * DM + own_n) = v;
    STAMP(18);
  }
  asm volatile(".Lcode_end:");
}

extern "C" void kernel_launch(void *const *d_in, const int *in_sizes, int n_in, void *d_out, int out_size, void *d_ws,
                              size_t ws_size, hipStream_t stream) {
  Params p;
  p.x = (const u16 *)d_in[0]; p.logA = (const u16 *)d_in[1]; p.Aim = (const u16 *)d_in[2]; p.Cr = (const u16 *)d_in[3];
  p.Ci = (const u16 *)d_in[4]; p.logdt = (const u16 *)d_in[5]; p.Dsk = (const u16 *)d_in[6];
  p.wg = (const u16 *)d_in[8]; p.wu = (const u16 *)d_in[9]; p.wd = (const u16 *)d_in[10];
  p.out = (u16 *)d_out; p.ws = (char *)d_ws;
  (void)hipFuncSetAttribute((const void *)fused, hipFuncAttributeMaxDynamicSharedMemorySize, LDS_BYTES);
  fused<<<256, 512, LDS_BYTES, stream>>>(p);
}
```

```cpp
#include <hip/hip_runtime.h>
#include <hip/hip_bf16.h>
#include <stdint.h>

#define STAMPS 0

typedef unsigned short u16;
typedef unsigned int u32;
typedef unsigned long long u64;
typedef float f32x2 __attribute__((ext_vector_type(2)));
typedef float f32x4 __attribute__((ext_vector_type(4)));
typedef float f32x16 __attribute__((ext_vector_type(16)));
typedef _Float16 h16x8 __attribute__((ext_vector_type(8)));
typedef _Float16 h16x2 __attribute__((ext_vector_type(2)));
typedef __bf16 b16x8 __attribute__((ext_vector_type(8)));
typedef __bf16 b16x2 __attribute__((ext_vector_type(2)));
typedef short s16x4 __attribute__((ext_vector_type(4)));
typedef u32 u32x2 __attribute__((ext_vector_type(2)));
typedef u32 u32x4 __attribute__((ext_vector_type(4)));
#define LAS __attribute__((address_space(3)))
#define GAS __attribute__((address_space(1)))
#define CAS __attribute__((address_space(4)))

constexpr int SEQ = 2048, DM = 512, NMODE = 128, FF = 1792;

constexpr u32 WS_EPOCH = 0, WS_ST = 4096, WS_FLAG2 = 32768, WS_FLAG3 = 65536, WS_FLAG1 = 196608;
constexpr u32 WS_HT = 1u << 20, WS_P = 4u << 20, WS_DBG = 16u << 20, WS_EX = 12u << 20, WS_XT = 18u << 20;

constexpr u32 LDS_BYTES = 163840;
constexpr u32 XY_STR = 544, XY_CH = 53 * XY_STR;
constexpr u32 L_XY = 0;
constexpr u32 UR_STR = 4640, UR_CH = 4 * UR_STR;
constexpr u32 L_UR = 0;
constexpr u32 L_XS = 37120;
constexpr u32 K_CH = 8448;
constexpr u32 L_K = 45312;
constexpr u32 VT_CH = 1344;
constexpr u32 L_VT = 62464;
constexpr u32 L_MISC = 9216;
constexpr u32 L_YP = 65536;
constexpr u32 T_STR = 1056, L_T = 57664, L_TX = L_T + 8 * T_STR;
constexpr u32 L_W0 = 81920;
constexpr u32 L_W2 = 49152;
constexpr u32 L_A = 0;
constexpr u32 L_ST = 131072;
constexpr u32 L_RED = 0;
constexpr int SPIN_CAP = 1 << 17;

struct Params {
  const u16 *x, *logA, *Aim, *Cr, *Ci, *logdt, *Dsk, *wg, *wu, *wd;
  u16 *out;
  char *ws;
};

__device__ __forceinline__ float bf_lo(u32 v) { return __uint_as_float(v << 16); }
__device__ __forceinline__ float bf_hi(u32 v) { return __uint_as_float(v & 0xffff0000u); }
__device__ __forceinline__ u32 pk_bf16(float a, float b) {
  b16x2 r; r[0] = (__bf16)a; r[1] = (__bf16)b; return __builtin_bit_cast(u32, r);
}
__device__ __forceinline__ float rbf(float a) { return bf_lo(pk_bf16(a, a)); }
__device__ __forceinline__ u32 pk_f16(float a, float b) {
  h16x2 r; r[0] = (_Float16)a; r[1] = (_Float16)b; return __builtin_bit_cast(u32, r);
}
template <typename T> __device__ __forceinline__ LAS T *lds(u32 off) { return (LAS T *)(size_t)off; }
__device__ __forceinline__ u32x2 tr_read(u32 addr) {
  return __builtin_bit_cast(u32x2, __builtin_amdgcn_ds_read_tr16_b64_v4i16((LAS s16x4 *)(size_t)addr));
}
__device__ __forceinline__ u32x4 cat(u32x2 a, u32x2 b) { u32x4 r; r[0] = a[0]; r[1] = a[1]; r[2] = b[0]; r[3] = b[1]; return r; }
__device__ __forceinline__ f32x16 mfma32(u32x4 a, u32x4 b, f32x16 c) {
  return __builtin_amdgcn_mfma_f32_32x32x16_bf16(__builtin_bit_cast(b16x8, a), __builtin_bit_cast(b16x8, b), c, 0, 0, 0);
}
__device__ __forceinline__ f32x4 mfma16h(u32x4 a, u32x4 b, f32x4 c) {
  return __builtin_amdgcn_mfma_f32_16x16x32_f16(__builtin_bit_cast(h16x8, a), __builtin_bit_cast(h16x8, b), c, 0, 0, 0);
}
__device__ __forceinline__ float dot2bf(u32 a, u32 b, float c) {
  return __builtin_amdgcn_fdot2_f32_bf16(__builtin_bit_cast(b16x2, a), __builtin_bit_cast(b16x2, b), c, false);
}
__device__ __forceinline__ void glds(u32 voff, const void *sbase, u32 ldst) {
  asm volatile("s_mov_b32 m0, %2\n\ts_nop 0\n\tglobal_load_lds_dwordx4 %0, %1" ::"v"(voff), "s"(sbase), "s"(ldst) : "memory");
}
__device__ __forceinline__ void glds_sc1(u32 voff, const void *sbase, u32 ldst) {
  asm volatile("s_mov_b32 m0, %2\n\ts_nop 0\n\tglobal_load_lds_dwordx4 %0, %1 sc1" ::"v"(voff), "s"(sbase), "s"(ldst) : "memory");
}
#define WAIT_VM(n) asm volatile("s_waitcnt vmcnt(" #n ")" ::: "memory")
#define WAIT_LGKM0() asm volatile("s_waitcnt lgkmcnt(0)" ::: "memory")
#define BAR() do { asm volatile("s_waitcnt lgkmcnt(0)\n\ts_barrier" ::: "memory"); } while (0)
#define SCHED() __builtin_amdgcn_sched_barrier(0)
#define PIN(x) asm volatile("" : "+v"(x))
#define RAWBAR() asm volatile("s_barrier" ::: "memory")
template <int N> __device__ __forceinline__ void wait_vm() { asm volatile("s_waitcnt vmcnt(%0)" ::"n"(N) : "memory"); }
template <int I> struct IC { static constexpr int v = I; };
template <int B, int E, typename Fn> __device__ __forceinline__ void static_for(Fn &&f) {
  if constexpr (B < E) { f(IC<B>{}); static_for<B + 1, E>(f); }
}
constexpr int G1_OPS[] = {-8, -7, -6, -5, -4, -3, -2, -1, 2, 6, 10, 14, 18, 22, 26, 30,
                          27, 57, 89, 119,
                          62, 90, 122,
                          97, 101, 115,
                          34, 38, 42, 46, 50, 54, 58, 63, 65, 67, 69, 71, 73, 75, 78, 80, 82, 85,
                          96, 98, 100, 102, 104, 106, 108, 110, 112, 114,
                          121, 123, 124, 125, 126, 127};
constexpr int G1_TAKE[] = {66, 69, 72, 98, 101, 104, 110, 116, 122};
constexpr int g1_take(int p) { for (int i = 0; i < 9; i++) if (G1_TAKE[i] == p || G1_TAKE[i] + 1 == p) return 2 * i + p - G1_TAKE[i]; return -1; }
#define BP 1
#define LOOK 3
constexpr int epi0(int tg) { return tg == 1 ? 5 : 1; }
constexpr int g1_younger(int x, int p) { int n = 0; for (int q : G1_OPS) n += (q > x && q < p); return n; }
constexpr int g1_find(int first, int cnt, int p) { for (int i = 0; i < cnt; i++) if (G1_OPS[first + i] == p) return i; return -1; }
constexpr int wd_slot(int u) { return u < 4 ? 16 + u : u < 12 ? u - 4 : u < 16 ? u + 4 : u < 18 ? u - 16 : u < 22 ? u - 10 : u - 20; }

template <int CTRL> __device__ __forceinline__ float dpp(float v) {
  return __uint_as_float(__builtin_amdgcn_update_dpp(0u, __float_as_uint(v), CTRL, 0xf, 0xf, true));
}
__device__ __forceinline__ float wave_sum(float v) {
  v += dpp<0xB1>(v); v += dpp<0x4E>(v); v += dpp<0x141>(v); v += dpp<0x140>(v);
  auto a = __builtin_amdgcn_permlane16_swap(__float_as_uint(v), __float_as_uint(v), false, false);
  v = __uint_as_float(a[0]) + __uint_as_float(a[1]);
  auto c = __builtin_amdgcn_permlane32_swap(__float_as_uint(v), __float_as_uint(v), false, false);
  return __uint_as_float(c[0]) + __uint_as_float(c[1]);
}
__device__ __forceinline__ float wave_max(float v) {
  v = fmaxf(v, dpp<0xB1>(v)); v = fmaxf(v, dpp<0x4E>(v)); v = fmaxf(v, dpp<0x141>(v)); v = fmaxf(v, dpp<0x140>(v));
  auto a = __builtin_amdgcn_permlane16_swap(__float_as_uint(v), __float_as_uint(v), false, false);
  v = fmaxf(__uint_as_float(a[0]), __uint_as_float(a[1]));
  auto c = __builtin_amdgcn_permlane32_swap(__float_as_uint(v), __float_as_uint(v), false, false);
  return fmaxf(__uint_as_float(c[0]), __uint_as_float(c[1]));
}
struct cf { float r, i; };
__device__ __forceinline__ cf cmul(cf a, cf b) { return {a.r * b.r - a.i * b.i, a.r * b.i + a.i * b.r}; }
__device__ __forceinline__ cf cexp_at(float lr, float rev, float pos) {
  float e = __builtin_amdgcn_exp2f(lr * pos), f = __builtin_amdgcn_fractf(rev * pos);
  return {e * __builtin_amdgcn_cosf(f), e * __builtin_amdgcn_sinf(f)};
}

#define STAMP(i)

__global__ void __launch_bounds__(512, 2) fused(Params p) {
  extern __shared__ __attribute__((aligned(1024))) char smem_[];
  const u32 tid = threadIdx.x, lane = tid & 63;
  const u32 w = __builtin_amdgcn_readfirstlane(tid >> 6);
  const u32 b = blockIdx.x, xcd = b & 7, jj = b >> 3;
  u32 junk;
  {
    u64 pc; u32 sz;
    asm volatile(".Lcode_begin:\n\ts_getpc_b64 %0\n\ts_mov_b32 %1, .Lcode_end-.Lcode_begin" : "=s"(pc), "=s"(sz));
    const u32 co = min((jj * 8 + w) * 256 + lane * 4, sz);
    asm volatile("global_load_dword %0, %1, %2" : "=v"(junk) : "v"(co), "s"(pc));
  }
  char *const ws = p.ws;
  STAMP(0);
  const bool g1 = jj < 28;
  const u32 panel = (xcd & 4) * 2 + (jj < 24 ? jj & 7 : (xcd & 1) * 4 + (jj & 3));
  const u32 ft = 7 * ((xcd >> 1) & 1) + (jj < 24 ? (xcd & 1) * 4 + (jj >> 3) : 3);
  const u32 rot1 = (jj >> 3) * 8;
  u32 tag = 0;

  u32x4 W[32];
  const u32 w1_voff = ((lane >> 4) * FF + ft * 128) * 2 + ((((lane & 15) >> 1) ^ ((lane >> 4) | ((w & 1) << 2))) * 32) + (lane & 1) * 16;
  const char *const w1_base = (const char *)((w & 1) ? p.wu : p.wg) + (size_t)((w >> 1) * 4) * (FF * 2);
  auto w1_one = [&](u32 ks, u32 dst) {
    glds(w1_voff, w1_base + (size_t)((ks + rot1) & 31) * 16 * (FF * 2), dst + w * 1024);
  };
  const u32 g4 = lane >> 4, q4 = (lane >> 2) & 3, p4 = lane & 3;
  const u32 w1_rd = (g4 >> 1) * 2048 + (g4 & 1) * 1024 + q4 * 256 + ((w ^ (q4 | ((g4 & 1) << 2))) * 32) + p4 * 8;

  const u32 cp = xcd * 32 + jj, d0 = 2 * cp;
  const u32 ch = w >> 2, job = w & 3;
  u32x4 xr; u32 pl, pa, pr, pi;
  {
    const u32 o1 = (8 * b + w) * (DM * 2) + lane * 16;
    asm volatile("global_load_dwordx4 %0, %1, %2" : "=v"(xr) : "v"(o1), "s"(p.x));
    const u32 o2 = ((d0 + ch) * NMODE + 2 * lane) * 2;
    asm volatile("global_load_dword %0, %1, %2" : "=v"(pl) : "v"(o2), "s"(p.logA));
    asm volatile("global_load_dword %0, %1, %2" : "=v"(pa) : "v"(o2), "s"(p.Aim));
    asm volatile("global_load_dword %0, %1, %2" : "=v"(pr) : "v"(o2), "s"(p.Cr));
    asm volatile("global_load_dword %0, %1, %2" : "=v"(pi) : "v"(o2), "s"(p.Ci));
  }
  u32 ldt2, dsk2;
  asm volatile("s_load_dword %0, %1, 0x0" : "=s"(tag) : "s"(ws) : "memory");
  asm volatile("s_load_dword %0, %1, 0x0" : "=s"(ldt2) : "s"(p.logdt + d0));
  asm volatile("s_load_dword %0, %1, 0x0" : "=s"(dsk2) : "s"(p.Dsk + d0));
  {
#pragma unroll
    for (int i = 0; i < 4; i++) w1_one(i, L_W0 + i * 8192);
    const u32 gw = b * 8 + w, po = (gw < 1792 ? gw : gw - 1024) * 1024 + lane * 16;
    glds(po, p.wg, L_W0 + 65536 + w * 1024); glds(po, p.wu, L_W0 + 65536 + w * 1024); glds(po, p.wd, L_W0 + 65536 + w * 1024);
  }
  asm volatile("s_waitcnt lgkmcnt(0)" : "+s"(tag), "+s"(ldt2), "+s"(dsk2));
  tag += 1u;
  auto poll = [&](u32 fo) {
#pragma unroll 1
    for (int spin = 0; spin < SPIN_CAP; spin++) {
      u32 f;
      asm volatile("global_load_dword %0, %1, %2 sc1\n\ts_waitcnt vmcnt(0)" : "=&v"(f) : "v"(fo), "s"(ws) : "memory");
      if (__all(f == tag)) break;
      __builtin_amdgcn_s_sleep(1);
    }
  };
  asm volatile("s_waitcnt vmcnt(11)" : "+v"(xr));
  {
    *lds<u32x4>(L_TX + w * T_STR + lane * 16) = xr;
    float v[8];
#pragma unroll
    for (int i = 0; i < 4; i++) { v[2 * i] = bf_lo(xr[i]); v[2 * i + 1] = bf_hi(xr[i]); }
    const float m = wave_sum(((v[0] + v[1]) + (v[2] + v[3])) + ((v[4] + v[5]) + (v[6] + v[7]))) * (1.f / DM);
    float q0 = 0, q1 = 0;
#pragma unroll
    for (int i = 0; i < 4; i++) { const float t0 = v[i] - m, t1 = v[i + 4] - m; q0 += t0 * t0; q1 += t1 * t1; }
    const float var = rbf(wave_sum(q0 + q1) * (1.f / DM));
    const float rs = __builtin_amdgcn_rcpf(rbf(__builtin_amdgcn_sqrtf(rbf(var + 1e-5f)))), mb = rbf(m);
    u32 uu[4];
#pragma unroll
    for (int i = 0; i < 4; i++) {
      const u32 a = pk_bf16(v[2 * i] - mb, v[2 * i + 1] - mb);
      const u32 u = pk_bf16(bf_lo(a) * rs, bf_hi(a) * rs);
      uu[i] = pk_f16(bf_lo(u), bf_hi(u));
    }
    u32x4 o; o[0] = uu[0]; o[1] = uu[1]; o[2] = uu[2]; o[3] = uu[3];
    *lds<u32x4>(L_T + w * T_STR + lane * 16) = o;
  }
  BAR();
  {
    __amdgpu_buffer_rsrc_t rs = __builtin_amdgcn_make_buffer_rsrc(ws + WS_EX, 0, 8u << 20, 0x00020000);
    const u32 ra = L_T + (4 * (p4 & 1) + 3 - q4) * T_STR + (64 * w + 8 * g4 + 4 * (p4 >> 1)) * 2;
    const u32 rx = L_TX + (3 - q4) * T_STR + (64 * w + 16 * g4 + 4 * p4) * 2;
    const u32 so = ((lane >> 3) * 256 + b) * 128 + (lane & 7) * 16;
    u32x2 r[2];
#pragma unroll
    for (int i = 0; i < 2; i++) r[i] = tr_read(ra + i * 64);
    const u32x4 xt = cat(tr_read(rx + 4 * T_STR), tr_read(rx));
#pragma unroll
    for (int i = 0; i < 2; i++) {
      u32x4 o; o[0] = r[i][0]; o[1] = tag; o[2] = r[i][1]; o[3] = tag;
      __builtin_amdgcn_raw_buffer_store_b128(o, rs, so + (16 * w + 8 * i) * 32768, 0, 16);
    }
    __builtin_amdgcn_raw_buffer_store_b128(xt, rs, so + (WS_XT - WS_EX) + 8 * w * 32768, 0, 16);
  }
  STAMP(1);
  u32 vidx[8];
  {
    const u32 l0 = 8 * (tid & 255);
#pragma unroll
    for (int e = 0; e < 8; e++) {
      const u32 l = l0 + e, bits = pk_bf16((float)l, 0.f) & 0xffffu;
      vidx[e] = L_VT + (tid >> 8) * VT_CH + 2 * (l < 256 ? l : bits - (0x4380u - 256u));
    }
#pragma unroll
    for (int e = 0; e < 8; e++) asm volatile("" : "+v"(vidx[e]));
  }
  asm volatile("s_waitcnt vmcnt(10)" : "+v"(pl), "+v"(pa), "+v"(pr), "+v"(pi));
  const float dt = rbf(__expf(ch ? bf_hi(ldt2) : bf_lo(ldt2)));
  {
    const float LOG2E = 1.4426950408889634f, INV2PI = 0.15915494309189535f;
    float lr[2], rv[2];
    lr[0] = -rbf(__expf(bf_lo(pl))) * dt * LOG2E; lr[1] = -rbf(__expf(bf_hi(pl))) * dt * LOG2E;
    rv[0] = bf_lo(pa) * dt * INV2PI; rv[1] = bf_hi(pa) * dt * INV2PI;
    const u32 wb = L_XY + ch * XY_CH + lane * 8;
    auto put = [&](int row, cf a, cf c) {
      u32x2 v; v[0] = pk_f16(a.r, a.i); v[1] = pk_f16(c.r, c.i);
      *lds<u32x2>(wb + row * XY_STR) = v;
    };
    auto G = [&](int i) { w1_one(4 + i, L_W0 + (4 + i) * 8192); if (i >= 2) w1_one(6 + i, L_W0 + (6 + i) * 8192); };
    if (job < 2) {
      G(0);
      cf s0 = cexp_at(lr[0], -rv[0], 8.f), s1 = cexp_at(lr[1], -rv[1], 8.f);
      const float p0 = job ? 128.f : 0.f;
      cf z0 = cexp_at(lr[0], -rv[0], p0), z1 = cexp_at(lr[1], -rv[1], p0);
      const u32 wb2 = wb + job * 16 * XY_STR;
#pragma unroll
      for (int i = 0; i < 16; i++) {
        u32x2 v; v[0] = pk_f16(z0.r, z0.i); v[1] = pk_f16(z1.r, z1.i);
        *lds<u32x2>(wb2 + i * XY_STR) = v;
        if (i < 15) { z0 = cmul(z0, s0); z1 = cmul(z1, s1); }
        if (i == 2 || i == 7 || i == 12) G(1 + i / 5);
        SCHED();
      }
    } else {
      G(0);
      cf c0 = {bf_lo(pr), bf_lo(pi)}, c1 = {bf_hi(pr), bf_hi(pi)};
      const float mx = wave_max(fmaxf(fmaxf(fabsf(c0.r), fabsf(c0.i)), fmaxf(fabsf(c1.r), fabsf(c1.i))));
      const int ex = __builtin_amdgcn_frexp_expf(mx);
      const float sc = __builtin_amdgcn_ldexpf(1.f, -ex);
      c0.r *= sc; c0.i *= sc; c1.r *= sc; c1.i *= sc;
      G(1);
      if (job == 2) {
        if (lane == 0) *lds<float>(L_MISC + ch * 4) = __builtin_amdgcn_ldexpf(2.f * dt, ex);
        cf s0 = cexp_at(lr[0], rv[0], 1.f), s1 = cexp_at(lr[1], rv[1], 1.f);
        cf z0 = c0, z1 = c1;
#pragma unroll
        for (int i = 0; i < 8; i++) { put(32 + i, z0, z1); if (i < 7) { z0 = cmul(z0, s0); z1 = cmul(z1, s1); } if (i == 2 || i == 7) G(2 + i / 5); SCHED(); }
        s0 = cmul(s0, s0); s1 = cmul(s1, s1);
        z0 = cmul(c0, cexp_at(lr[0], rv[0], 256.f)); z1 = cmul(c1, cexp_at(lr[1], rv[1], 256.f));
#pragma unroll
        for (int i = 0; i < 4; i++) { put(40 + i, z0, z1); if (i < 3) { z0 = cmul(z0, s0); z1 = cmul(z1, s1); } }
      } else {
        cf f0 = cexp_at(lr[0], rv[0], 4.f), f1 = cexp_at(lr[1], rv[1], 4.f);
        cf s0 = cexp_at(lr[0], rv[0], 256.f), s1 = cexp_at(lr[1], rv[1], 256.f);
        G(2); SCHED();
        cf z0 = cmul(c0, cexp_at(lr[0], rv[0], 512.f)), z1 = cmul(c1, cexp_at(lr[1], rv[1], 512.f));
        put(44, z0, z1); put(45, cmul(z0, f0), cmul(z1, f1));
        z0 = cmul(z0, s0); z1 = cmul(z1, s1);
        put(46, z0, z1); put(47, cmul(z0, f0), cmul(z1, f1));
#pragma unroll
        for (int i = 0; i < 5; i++) { z0 = cmul(z0, s0); z1 = cmul(z1, s1); put(48 + i, z0, z1); if (i == 1) G(3); SCHED(); }
      }
    }
  }
  asm volatile("s_waitcnt vmcnt(0)" : "+v"(junk));
  BAR();
  STAMP(2);
  if (tid == 0) __hip_atomic_store((GAS u32 *)(ws + WS_FLAG1) + b, tag, __ATOMIC_RELAXED, __HIP_MEMORY_SCOPE_AGENT);
  u32x4 ex[2];
  const char *const exb = ws + WS_EX + (size_t)(cp >> 1) * 32768 + (cp & 1) * 32;
  const u32 exo = (tid >> 2) * 128 + (tid & 2) * 32 + (tid & 1) * 16;
  asm volatile("global_load_dwordx4 %0, %1, %2 sc1" : "=v"(ex[0]) : "v"(exo), "s"(exb));
  asm volatile("global_load_dwordx4 %0, %1, %2 sc1" : "=v"(ex[1]) : "v"(exo), "s"(exb + 16384));
  {
    const u32 rb = job >> 1, cb = job & 1;
    const u32 xa = L_XY + ch * XY_CH + (32 + 16 * rb + (lane & 15)) * XY_STR + g4 * 16;
    const u32 ya = L_XY + ch * XY_CH + (16 * cb + (lane & 15)) * XY_STR + g4 * 16;
    u32x4 fa[8], fb[8];
#pragma unroll
    for (int ks = 0; ks < 8; ks++) { fa[ks] = *lds<u32x4>(xa + ks * 64); fb[ks] = *lds<u32x4>(ya + ks * 64); }
    SCHED();
    u32 wr = L_W0 + w1_rd;
    PIN(wr);
#pragma unroll
    for (int ks = 0; ks < 10; ks++) { const u32 a = (ks < 8 ? wr : wr + 65536) + (ks & 7) * 8192; W[ks] = cat(tr_read(a), tr_read(a + 4096)); }
    SCHED();
    const u32 j = 16 * cb + (lane & 15);
    u32 i0, is, i2;
    if (rb == 0) {
      i0 = g4 < 2 ? 4 * g4 + 8 * j : (g4 == 2 ? 256 + 4 * j : 384 + 2 * j); is = 1; i2 = g4 == 3 ? 64 : 2;
    } else {
      i0 = g4 == 0 ? 512 + j : 640 + j; is = g4 == 0 ? 32 : 0; i2 = 2 * is;
    }
    f32x4 a0 = {0, 0, 0, 0}, a1 = {0, 0, 0, 0};
#pragma unroll
    for (int ks = 0; ks < 8; ks += 2) { a0 = mfma16h(fa[ks], fb[ks], a0); a1 = mfma16h(fa[ks + 1], fb[ks + 1], a1); }
    a0 += a1;
    const u32 vb = L_VT + ch * VT_CH;
    if (rb == 0 || g4 == 0) {
#pragma unroll
      for (int r = 0; r < 4; r++) *lds<_Float16>(vb + 2 * (i0 + (r & 1) * is + (r >> 1) * i2)) = (_Float16)a0[r];
    } else if (g4 == 1) {
      *lds<_Float16>(vb + 2 * i0) = (_Float16)a0[0];
    }
  }
  STAMP(4);
  asm volatile("s_waitcnt vmcnt(0)" : "+v"(ex[0]), "+v"(ex[1]));
#pragma unroll 1
  for (int spin = 0; spin < SPIN_CAP; spin++) {
    if (__all(ex[0][1] == tag && ex[0][3] == tag && ex[1][1] == tag && ex[1][3] == tag)) break;
    __builtin_amdgcn_s_sleep(1);
    asm volatile("global_load_dwordx4 %0, %2, %3 sc1\n\tglobal_load_dwordx4 %1, %2, %4 sc1\n\ts_waitcnt vmcnt(0)"
                 : "=&v"(ex[0]), "=&v"(ex[1]) : "v"(exo), "s"(exb), "s"(exb + 16384) : "memory");
  }
  BAR();
  STAMP(5);
#pragma unroll
  for (int i = 0; i < 4; i++) w1_one(10 + i, L_W0 + i * 8192);
  {
    const u32 ab = L_UR + 3 * UR_STR + (tid & 1) * UR_CH + (2044 - 1024 - 8 * (tid >> 2) - 2 * (tid & 2)) * 2;
#pragma unroll
    for (int i = 0; i < 2; i++) { u32x2 v; v[0] = ex[i][0]; v[1] = ex[i][2]; *lds<u32x2>(ab + (1 - i) * 2048) = v; }
  }
  {
    u32x4 kv;
    _Float16 t[8];
#pragma unroll
    for (int e = 0; e < 8; e++) t[e] = *lds<_Float16>(vidx[e]);
#pragma unroll
    for (int e = 0; e < 4; e++) { h16x2 t2; t2[0] = t[2 * e]; t2[1] = t[2 * e + 1]; kv[e] = __builtin_bit_cast(u32, t2); }
    const u32 kb = L_K + (tid >> 8) * K_CH + (tid & 255) * 16;
    u32x2 lo, hi; lo[0] = kv[0]; lo[1] = kv[1]; hi[0] = kv[2]; hi[1] = kv[3];
    *lds<u32x2>(kb + 120) = lo; *lds<u32x2>(kb + 128) = hi;
    *lds<u32x4>(kb + 4224 + 112) = kv;
    const u32x2 z2 = {0, 0};
    u32 za;
    if (tid < 64) {
      const u32 c = tid >> 5, i = tid & 31;
      za = L_K + c * K_CH + (i < 15 ? i * 8 : 4224 + (i < 29 ? i - 15 : 0) * 8);
    } else {
      const u32 i = tid < 448 ? tid - 64 : 0, c = i / 48;
      za = L_UR + c * UR_STR + 4096 + (i - c * 48) * 8;
    }
    *lds<u32x2>(za) = z2;
  }
#pragma unroll
  for (int i = 4; i < 8; i++) w1_one(10 + i, L_W0 + i * 8192);
  BAR();
  {
    const u32 ub = L_UR + (tid >> 8) * UR_CH + (tid & 255) * 16;
    const u32x4 a = *lds<u32x4>(ub + 3 * UR_STR);
    const u32x2 c = *lds<u32x2>(ub + 3 * UR_STR + 16);
    u32x4 o;
    o[0] = __builtin_amdgcn_alignbit(a[1], a[0], 16); o[1] = __builtin_amdgcn_alignbit(a[2], a[1], 16);
    o[2] = __builtin_amdgcn_alignbit(a[3], a[2], 16); o[3] = __builtin_amdgcn_alignbit(c[0], a[3], 16);
    *lds<u32x4>(ub + 2 * UR_STR) = o;
    u32x4 o1; o1[0] = a[1]; o1[1] = a[2]; o1[2] = a[3]; o1[3] = c[0];
    *lds<u32x4>(ub + UR_STR) = o1;
    u32x4 o0; o0[0] = o[1]; o0[1] = o[2]; o0[2] = o[3]; o0[3] = __builtin_amdgcn_alignbit(c[1], c[0], 16);
    *lds<u32x4>(ub) = o0;
  }
  BAR();
  STAMP(6);
  u32 f1;
  const u32 xp = 255 - 32 * w - (lane >> 1);
  const u32 pg = w & 1, cg = (w >> 1) & 1;
  f32x4 ya[4];
#pragma unroll
  for (int i = 0; i < 4; i++) ya[i] = f32x4{0, 0, 0, 0};
  {
    const u32 c16 = lane & 15;
    const u32 ka = L_K + ch * K_CH + (c16 & 1) * 4224 + (c16 >> 1) * 16 + g4 * 16 + cg * 4 * 64;
    const u32 ua = L_UR + ch * UR_CH + (c16 & 3) * UR_STR + (1984 - 1792 - 64 * (c16 >> 2) + 8 * g4) * 2 + cg * 4 * 64;
    u32x4 kf[2][8], uf[2][8];
    auto rd1 = [&](int s, int j) {
      if (j >= 1 && j <= 8) uf[s & 1][8 - j] = *lds<u32x4>(ua + (j - 1) * 512 + s * 64);
      else kf[s & 1][j ? j - 8 : 0] = *lds<u32x4>(ka + (j ? j - 8 : 0) * 512 + s * 64);
    };
    auto rd = [&](int s, int i) { rd1(s, 2 * i); rd1(s, 2 * i + 1); };
    u32 wr = L_W0 + w1_rd;
    PIN(wr);
    auto side = [&](int s, int i) {
      const int n = s * 8 + i;
      if (n == 0) asm volatile("global_load_dword %0, %1, %2 sc1" : "=v"(f1) : "v"(xp * 4), "s"(ws + WS_FLAG1));
      if (n == 1 || n == 3) w1_one(26 + n / 3, L_W0 + (8 + n / 3) * 8192);
      if (n == 4) { WAIT_VM(3); RAWBAR(); }
      if (n >= 4 && n < 8) {
#pragma unroll
        for (int k = 2 * n - 8; k < 2 * n - 6; k++) W[10 + k] = cat(tr_read(wr + k * 8192), tr_read(wr + k * 8192 + 4096));
      }
      if (n == 9) {
        asm volatile("s_waitcnt vmcnt(2)" : "+v"(f1));
        if (!__all(f1 == tag)) poll(WS_FLAG1 + xp * 4);
        glds_sc1(xp * 128 + (lane & 1) * 16, ws + WS_XT + (size_t)(cp >> 2) * 32768 + (cp & 3) * 32, L_XS + w * 1024);
      }
      if (n == 12) BAR();
      if (n >= 12 && n < 28 && !(n & 1)) w1_one(18 + (n - 12) / 2, L_W0 + (n - 12) / 2 * 8192);
    };
#pragma unroll
    for (int i = 0; i < 8; i++) rd(0, i);
    if (pg == 0) {
#pragma unroll
      for (int s = 0; s < 4; s++) {
#pragma unroll
        for (int i = 0; i < 8; i++) {
          if (s < 3) rd(s + 1, i);
          side(s, i);
          ya[3] = mfma16h(uf[s & 1][7 - i], kf[s & 1][i], ya[3]);
          if (i <= 4) ya[2] = mfma16h(uf[s & 1][4 - i], kf[s & 1][i], ya[2]);
          if (i <= 3) ya[1] = mfma16h(uf[s & 1][3 - i], kf[s & 1][i], ya[1]);
          if (i <= 0) ya[0] = mfma16h(uf[s & 1][0 - i], kf[s & 1][i], ya[0]);
          SCHED();
        }
      }
    } else {
#pragma unroll
      for (int s = 0; s < 4; s++) {
#pragma unroll
        for (int i = 0; i < 8; i++) {
          if (s < 3) rd(s + 1, i);
          side(s, i);
          if (i <= 6) ya[3] = mfma16h(uf[s & 1][6 - i], kf[s & 1][i], ya[3]);
          if (i <= 5) ya[2] = mfma16h(uf[s & 1][5 - i], kf[s & 1][i], ya[2]);
          if (i <= 2) ya[1] = mfma16h(uf[s & 1][2 - i], kf[s & 1][i], ya[1]);
          if (i <= 1) ya[0] = mfma16h(uf[s & 1][1 - i], kf[s & 1][i], ya[0]);
          SCHED();
        }
      }
    }
  }
  WAIT_VM(8);
  {
    const u32 yb = L_YP + w * 2048 + lane * 16;
    *lds<f32x4>(yb) = cg ? ya[0] : ya[2];
    *lds<f32x4>(yb + 1024) = cg ? ya[1] : ya[3];
  }
  BAR();
  STAMP(7);
  w1_one(28, L_W2); w1_one(29, L_W2 + 8192);
  {
    const u32 yb = L_YP + (w ^ 2) * 2048 + lane * 16;
    const float ksc = *lds<float>(L_MISC + ch * 4), dsk = ch ? bf_hi(dsk2) : bf_lo(dsk2);
    __amdgpu_buffer_rsrc_t rs = __builtin_amdgcn_make_buffer_rsrc(ws + WS_HT, 0, SEQ * DM * 2, 0x00020000);
    f32x4 o[2]; u32x2 uu[2], xx[2]; u32 t0[2];
#pragma unroll
    for (int i = 0; i < 2; i++) {
      o[i] = *lds<f32x4>(yb + i * 1024);
      t0[i] = 256 * (4 * cg + (i ? 3 - pg : pg)) + 4 * lane;
      uu[i] = *lds<u32x2>(L_UR + ch * UR_CH + 3 * UR_STR + (2044 - t0[i]) * 2);
      xx[i] = *lds<u32x2>(L_XS + ((2044 - t0[i]) >> 3) * 32 + ch * 16 + ((2044 - t0[i]) & 7) * 2);
    }
#pragma unroll
    for (int i = 0; i < 2; i++) {
      const f32x4 own = cg ? (i ? ya[3] : ya[2]) : (i ? ya[1] : ya[0]);
      const u32 uu0 = uu[i][0], uu1 = uu[i][1];
      const h16x2 ua = __builtin_bit_cast(h16x2, uu1), ub = __builtin_bit_cast(h16x2, uu0);
      const float uv[4] = {(float)ua[1], (float)ua[0], (float)ub[1], (float)ub[0]};
      const float xv[4] = {bf_hi(xx[i][1]), bf_lo(xx[i][1]), bf_hi(xx[i][0]), bf_lo(xx[i][0])};
      float hv[4];
#pragma unroll
      for (int r = 0; r < 4; r += 2) {
        const u32 y2 = pk_bf16((o[i][r] + own[r]) * ksc, (o[i][r + 1] + own[r + 1]) * ksc);
        const u32 d2 = pk_bf16(uv[r] * dsk, uv[r + 1] * dsk);
        const u32 z2 = pk_bf16(bf_lo(y2) + bf_lo(d2), bf_hi(y2) + bf_hi(d2));
        hv[r] = xv[r] + bf_lo(z2); hv[r + 1] = xv[r + 1] + bf_hi(z2);
      }
      u32x2 hv2; hv2[0] = pk_bf16(hv[0], hv[1]); hv2[1] = pk_bf16(hv[2], hv[3]);
      const u32 off = ((t0[i] >> 5) * DM + d0 + ch) * 64 + (t0[i] & 31) * 2;
      __builtin_amdgcn_raw_buffer_store_b64(hv2, rs, off, 0, 16);
    }
  }
  SCHED();
  if (b == 0 && tid == 0)
    __hip_atomic_store((GAS u32 *)(ws + WS_EPOCH), tag, __ATOMIC_RELAXED, __HIP_MEMORY_SCOPE_AGENT);
  BAR();
  f32x16 cs;
#pragma unroll
  for (int i = 0; i < 16; i++) cs[i] = 0;
  const u32x4 ones = {0x3f803f80u, 0x3f803f80u, 0x3f803f80u, 0x3f803f80u};
  w1_one(30, L_YP); w1_one(31, L_YP + 8192);
#pragma unroll
  for (int ks = 0; ks < 10; ks++) cs = mfma32(W[ks], ones, cs);
  SCHED();
  WAIT_VM(2);
  RAWBAR();
  if (tid == 0) __hip_atomic_store((GAS u32 *)(ws + WS_FLAG2) + b * 32, tag, __ATOMIC_RELAXED, __HIP_MEMORY_SCOPE_AGENT);
  STAMP(8);
  const u32 t32 = lane & 31;
  const u32 nt = jj & 7, tt = jj >> 3, pn2 = 2 * xcd + (tt >> 1), tgA = tt & 1, tgB = 3 - tgA;
  const u32 lv = lane * 16, rot2 = nt == 7 ? 0 : nt;
  auto ft_of = [&](int i) { const u32 k = (i % 7) + rot2; return 2 * (k >= 7 ? k - 7 : k) + (i < 7 ? tgA ^ 1 : tgA); };
  const u32 d_voff = (lane >> 3) * (DM * 2) + (((lane & 7) ^ (((lane >> 4) & 1) << 2)) * 16);
  const char *const wd_base = (const char *)p.wd + (size_t)(w * 16) * (DM * 2) + nt * 128;
  auto wd_one = [&](int u) {
    const u32 k = __builtin_amdgcn_readfirstlane((ft_of(u >> 1) << 17) + ((u & 1) << 13));
    glds(d_voff, wd_base + k, wd_slot(u) * 8192 + w * 1024);
  };
  u32x4 D[28];
  u32 d_rd[2][3];
#pragma unroll
  for (int h = 0; h < 2; h++) {
    d_rd[h][0] = w * 1024 + (((4 * (g4 >> 1) + q4) * 128 + h * 64 + (g4 & 1) * 32 + p4 * 8) ^ (((q4 >> 1) & 1) * 64));
    d_rd[h][1] = d_rd[h][0] + 65536; d_rd[h][2] = d_rd[h][0] + 131072;
  }
  auto wd_take = [&](int j, int h) {
    const u32 o = wd_slot(2 * j) * 8192;
    D[2 * j + h] = cat(tr_read(d_rd[h][o >> 16] + (o & 65535)), tr_read(d_rd[h][o >> 16] + (o & 65535) + 8192));
  };
  auto lane_again = []() {
    u32 l; asm volatile("v_mbcnt_lo_u32_b32 %0, -1, 0\n\tv_mbcnt_hi_u32_b32 %0, -1, %0" : "=v"(l)); return l;
  };
  auto foA_of = [&]() { return WS_FLAG3 + ((pn2 * 4 + tgA) * 16 + min(lane_again(), 13u)) * 128; };
  __amdgpu_buffer_rsrc_t prs = __builtin_amdgcn_make_buffer_rsrc(ws + WS_P, 0, SEQ * FF * 2, 0x00020000);
  __amdgpu_buffer_rsrc_t frs = __builtin_amdgcn_make_buffer_rsrc(ws + WS_FLAG3, 0, 131072, 0x00020000);
  const u32 OOB = 0x40000000u;
  const u32 p_off = g1 ? (panel * 4 * 112 + ft * 8 + w) * 1024 + lane * 16 : OOB;
  const u32 f_off = g1 && tid == 0 ? (panel * 64 + ft) * 128 : OOB;
  auto tg_of = [&](int g) { return g < 2 ? (u32)g : g ^ (ft & 1); };
  float e_m, e_ne, e_s2, e_g, e_u, e_e, e_o;
  u32 pko[4];
  u32x2 sg;
  const u32 sg_off = WS_ST + (panel * 128 + t32) * 8;
  auto st_take = [&](int tg) {
    PIN(sg);
#pragma unroll 1
    for (int spin = 0; spin < SPIN_CAP; spin++) {
      if (__all(sg[1] == tag)) break;
      __builtin_amdgcn_s_sleep(1);
      asm volatile("global_load_dwordx2 %0, %1, %2 sc1\n\ts_waitcnt vmcnt(0)" : "=&v"(sg) : "v"(sg_off), "s"(ws + tg_of(tg) * 256) : "memory");
    }
    e_m = bf_lo(sg[0]);
    const float sd = bf_hi(sg[0]);
    e_ne = -1.4426950408889634f * __builtin_amdgcn_rcpf(sd); e_s2 = sd * sd;
  };
  auto epi_step = [&](int ks, int tg, const f32x16 &prev) {
    if (ks == 0) st_take(tg);
    else if (ks >= 1 && ks < 25) {
      const int i = (ks - 1) / 3, ph = (ks - 1) % 3;
      if (ph == 0) {
        e_g = prev[i] - e_m * cs[i]; PIN(e_g);
        e_e = __builtin_amdgcn_exp2f(e_g * e_ne);
      } else if (ph == 1) {
        e_u = (prev[i + 8] - e_m * cs[i + 8]) * e_g;
        e_e = e_e * e_s2 + e_s2;
        PIN(e_u); PIN(e_e);
      } else {
        const float o = e_u * __builtin_amdgcn_rcpf(e_e);
        if (i & 1) { pko[i >> 1] = pk_bf16(e_o, o); PIN(pko[i >> 1]); } else { e_o = o; PIN(e_o); }
      }
    } else if (ks == 25) {
      u32x4 v; v[0] = pko[0]; v[1] = pko[1]; v[2] = pko[2]; v[3] = pko[3];
      __builtin_amdgcn_raw_buffer_store_b128(v, prs, p_off + tg_of(tg) * (112 * 1024), 0, 16);
    }
  };
  const char *const pbA = ws + WS_P + ((size_t)(pn2 * 4 + tgA) * 112 + w) * 1024;
  const char *const pbB = ws + WS_P + ((size_t)(pn2 * 4 + tgB) * 112 + w) * 1024;
  u32x4 PF[14], QF[14];
  auto p_one = [&](const char *pb, int i, u32x4 &d) {
    const u32 j = __builtin_amdgcn_readfirstlane(ft_of(i) << 13);
    asm volatile("global_load_dwordx4 %0, %1, %2 sc1" : "=v"(d) : "v"(lv), "s"(pb + j));
  };
  f32x16 acc[2];
  if (g1) {
    {
      u32 wr = L_W0 + w1_rd;
      PIN(wr);
#pragma unroll
      for (int ks = 0; ks < 8; ks++) W[18 + ks] = cat(tr_read(wr + ks * 8192), tr_read(wr + ks * 8192 + 4096));
      wr += 65536;
#pragma unroll
      for (int ks = 0; ks < 2; ks++) W[26 + ks] = cat(tr_read(wr + ks * 8192), tr_read(wr + ks * 8192 + 4096));
      wr = L_W2 + w1_rd;
      PIN(wr);
#pragma unroll
      for (int ks = 0; ks < 2; ks++) W[28 + ks] = cat(tr_read(wr + ks * 8192), tr_read(wr + ks * 8192 + 4096));
#pragma unroll
      for (int ks = 10; ks < 18; ks++) cs = mfma32(W[ks], ones, cs);
      SCHED();
      if (w == 0) WAIT_VM(1); else WAIT_VM(0);
      RAWBAR();
#pragma unroll
      for (int ks = 2; ks < 4; ks++) W[28 + ks] = cat(tr_read(wr + ks * 8192), tr_read(wr + ks * 8192 + 4096));
    }
    STAMP(9);
    poll(WS_FLAG2 + (w * 32 + t32) * 128);
    BAR();
    STAMP(10);
    const char *const a_base = ws + WS_HT + (size_t)panel * 131072;
    auto a_one = [&](int i) { glds_sc1(lv, a_base + tg_of(i >> 2) * 32768 + (((i & 3) * 8 + w + rot1) & 31) * 1024, L_A + w * 1024 + i * 8192); };
#pragma unroll
    for (int i = 0; i < 4; i++) a_one(i);
#pragma unroll
    for (int ks = 18; ks < 32; ks++) { cs = mfma32(W[ks], ones, cs); if (ks % 3 == 0 && ks < 30) { a_one(ks / 3 - 2); SCHED(); } }
    SCHED();
    u32 ab = L_A + (4 * (g4 >> 1) + q4) * 64 + (g4 & 1) * 32 + p4 * 8;
    u32x4 F[LOOK + 1];
    auto frag = [&](u32 a) { return cat(tr_read(a), tr_read(a + 512)); };
    u32 fa;
    wait_vm<g1_younger(-8, 0)>();
    RAWBAR();
    u32 ab2 = ab + 65536;
    PIN(ab); PIN(ab2);
#pragma unroll
    for (int i = 0; i < LOOK; i++) F[i] = frag(ab + i * 1024);
    static_for<0, 128>([&](auto P_) {
      constexpr int P = decltype(P_)::v, tg = P >> 5, ks = P & 31;
      if constexpr (ks == 0) {
#pragma unroll
        for (int i = 0; i < 16; i++) acc[tg & 1][i] = 0;
      }
      if constexpr (P == 8 - LOOK) { wait_vm<g1_younger(-7, P)>(); RAWBAR(); }
      if constexpr (P == 16 - LOOK) { wait_vm<g1_younger(-6, P)>(); RAWBAR(); }
      if constexpr (P == 24 - LOOK) { wait_vm<g1_younger(-5, P)>(); RAWBAR(); }
      if constexpr (P == 32 - LOOK) { wait_vm<g1_younger(-1, P)>(); RAWBAR(); }
      if constexpr (P == 48) { wait_vm<g1_younger(14, P)>(); RAWBAR(); }
      if constexpr (P == 64) { wait_vm<g1_younger(30, P)>(); RAWBAR(); }
      if constexpr (P == 66) wait_vm<g1_younger(54, P)>();
      if constexpr (P == 96) { wait_vm<g1_younger(85, P)>(); RAWBAR(); }
      if constexpr (P == 101) { wait_vm<g1_younger(90, P)>(); RAWBAR(); }
      if constexpr (g1_find(23, 2, P) >= 0) __builtin_amdgcn_raw_buffer_store_b32(tag, frs, f_off, 2048 * g1_find(23, 2, P), 16);
      if constexpr (P == G1_OPS[25]) asm volatile("global_load_dword %0, %1, %2 sc1" : "=v"(fa) : "v"(foA_of()), "s"(ws));
      if constexpr (P == 121) {
        wait_vm<g1_younger(115, P)>();
        PIN(fa);
        if (!__all(fa == tag)) poll(foA_of());
      }
      if constexpr (g1_find(54, 6, P) >= 0) p_one(pbA, g1_find(54, 6, P), PF[g1_find(54, 6, P)]);
      if constexpr (g1_find(8, 8, P) >= 0) a_one(8 + g1_find(8, 8, P));
      if constexpr (g1_find(16, 4, P) >= 0)
        asm volatile("global_load_dwordx2 %0, %1, %2 sc1" : "=v"(sg) : "v"(sg_off), "s"(ws + tg_of(g1_find(16, 4, P)) * 256));
      if constexpr (g1_find(26, 28, P) >= 0) wd_one(g1_find(26, 28, P));
      if constexpr (g1_take(P) >= 0) wd_take(g1_take(P) >> 1, g1_take(P) & 1);
      constexpr int n = P + LOOK;
      if constexpr (n < 128) F[n % (LOOK + 1)] = frag((n < 64 ? ab : ab2) + (n & 63) * 1024);
      acc[tg & 1] = mfma32(W[ks], F[P % (LOOK + 1)], acc[tg & 1]);
      if constexpr (tg > 0 && ks == epi0(tg)) wait_vm<g1_younger(G1_OPS[16 + tg - 1], P + 1)>();
      if constexpr (tg > 0 && ks >= epi0(tg)) epi_step(ks - epi0(tg), tg - 1, acc[(tg - 1) & 1]);
      SCHED();
      if constexpr (P == 31) STAMP(11);
      if constexpr (P == 63) STAMP(19);
      if constexpr (P == 95) STAMP(20);
      if constexpr (P == 127) STAMP(3);
    });
    wait_vm<g1_younger(119, 128)>();
    st_take(3);
    STAMP(12);
  } else {
    poll(WS_FLAG2 + (w * 32 + t32) * 128);
    RAWBAR();
    const u32 tgs = (2 * xcd + ((jj >> 1) & 1)) * 4 + (jj & 1);
    const char *base = ws + WS_HT + (size_t)tgs * 32768 + w * 4096;
#pragma unroll
    for (int i = 0; i < 8; i++) glds_sc1(lv, base + (i >> 2) * 65536 + (i & 3) * 1024, 65536 + (i >> 2) * 32768 + w * 4096 + (i & 3) * 1024);
    const u32 ab = 65536 + w * 4096 + (4 * (g4 >> 1) + q4) * 64 + (g4 & 1) * 32 + p4 * 8;
#pragma unroll
    for (int g = 0; g < 2; g++) {
      if (g == 0) WAIT_VM(4); else WAIT_VM(0);
      float s_s = 0, s_q = 0;
#pragma unroll
      for (int i = 0; i < 4; i++) {
        const u32x4 f = cat(tr_read(ab + g * 32768 + i * 1024), tr_read(ab + g * 32768 + i * 1024 + 512));
#pragma unroll
        for (int k = 0; k < 4; k++) { s_s = dot2bf(f[k], 0x3f803f80u, s_s); s_q = dot2bf(f[k], f[k], s_q); }
      }
      const auto sw = __builtin_amdgcn_permlane32_swap(__float_as_uint(s_s), __float_as_uint(s_q), false, false);
      *lds<float>(g * 2048 + w * 256 + lane * 4) = __uint_as_float(sw[0]) + __uint_as_float(sw[1]);
      BAR();
      float sp = 0;
#pragma unroll
      for (int i = 0; i < 8; i++) sp += *lds<float>(g * 2048 + i * 256 + lane * 4);
      const auto s2 = __builtin_amdgcn_permlane32_swap(__float_as_uint(sp), __float_as_uint(sp), false, false);
      const float mean = __uint_as_float(s2[0]) * (1.f / DM);
      const float var = rbf(fmaxf(__uint_as_float(s2[1]) * (1.f / DM) - mean * mean, 0.f));
      const u64 gr = ((u64)tag << 32) | pk_bf16(mean, __builtin_amdgcn_sqrtf(rbf(var + 1e-5f)));
      if (tid < 32) __hip_atomic_store((GAS u64 *)(ws + WS_ST) + (tgs + 2 * g) * 32 + lane, gr, __ATOMIC_RELAXED, __HIP_MEMORY_SCOPE_AGENT);
    }
    BAR();
#pragma unroll
    for (int u = 0; u < 12; u++) wd_one(u);
    WAIT_VM(0);
#pragma unroll
    for (int j = 0; j < 6; j++) { wd_take(j, 0); wd_take(j, 1); }
    WAIT_LGKM0();
#pragma unroll
    for (int u = 12; u < 28; u++) wd_one(u);
    poll(foA_of());
#pragma unroll
    for (int j = 6; j < 9; j++) { wd_take(j, 0); wd_take(j, 1); }
#pragma unroll
    for (int i = 0; i < 6; i++) p_one(pbA, i, PF[i]);
    asm volatile("s_waitcnt vmcnt(0)" : "+v"(PF[0]), "+v"(PF[1]), "+v"(PF[2]), "+v"(PF[3]), "+v"(PF[4]), "+v"(PF[5]));
#pragma unroll
    for (int i = 0; i < 16; i++) acc[1][i] = 0;
    e_m = 0; e_ne = 0; e_s2 = 1.f;
  }

  {
    const u32 ln = lane_again(), hh = ln >> 5, tl = ln & 31;
    const u32 foB = WS_FLAG3 + ((pn2 * 4 + tgB) * 16 + 2 * min(ln, 6u)) * 128;
    p_one(pbA, 6, PF[6]); p_one(pbA, 7, PF[7]);
    SCHED();
    {
      const f32x2 m2 = {e_m, e_m}, ne2 = {e_ne, e_ne}, s22 = {e_s2, e_s2};
      u32x4 v;
#pragma unroll
      for (int i = 0; i < 8; i += 2) {
        const f32x2 g = f32x2{acc[1][i], acc[1][i + 1]} - m2 * f32x2{cs[i], cs[i + 1]};
        const f32x2 u = (f32x2{acc[1][i + 8], acc[1][i + 9]} - m2 * f32x2{cs[i + 8], cs[i + 9]}) * g;
        f32x2 e = g * ne2;
        e[0] = __builtin_amdgcn_exp2f(e[0]); e[1] = __builtin_amdgcn_exp2f(e[1]);
        e = e * s22 + s22;
        e[0] = __builtin_amdgcn_rcpf(e[0]); e[1] = __builtin_amdgcn_rcpf(e[1]);
        const f32x2 o = u * e;
        v[i >> 1] = pk_bf16(o[0], o[1]);
      }
      __builtin_amdgcn_raw_buffer_store_b128(v, prs, p_off + tg_of(3) * (112 * 1024), 0, 16);
    }
    SCHED();
    STAMP(13);
    wait_vm<8>();
    RAWBAR();
    __builtin_amdgcn_raw_buffer_store_b32(tag, frs, f_off, tg_of(2) * 2048, 16);
    const u32 own_n = nt * 64 + w * 8;
    u32 res[8];
    const u32 roA = WS_HT + ((pn2 * 4 + tgA) * DM + own_n + 4 * hh) * 64 + tl * 2;
    const u32 roB = WS_HT + ((pn2 * 4 + tgB) * DM + own_n + 4 * hh) * 64 + tl * 2;
    f32x16 ac[2];
    f32x4 r[2];
    f32x16 bc[2];
    u32 rw = w * 1024 + lv;
    PIN(rw);
    auto red_put = [&](int t, const f32x16 &a, int o) {
      f32x4 v; v[0] = a[4 * (o & 3)]; v[1] = a[4 * (o & 3) + 1]; v[2] = a[4 * (o & 3) + 2]; v[3] = a[4 * (o & 3) + 3];
      *lds<f32x4>((t ? rw + 65536 : rw) + o * 8192) = v;
    };
    auto red_get = [&](int t) {
      f32x4 v[8];
#pragma unroll
      for (int i = 0; i < 8; i++) v[i] = *lds<f32x4>(t * 65536 + w * 8192 + i * 1024 + lv);
      r[t] = ((v[0] + v[1]) + (v[2] + v[3])) + ((v[4] + v[5]) + (v[6] + v[7]));
    };
#pragma unroll
    for (int i = 0; i < 16; i++) { ac[0][i] = 0; ac[1][i] = 0; }
    u32 fb;
    static_for<0, 14>([&](auto I_) {
      constexpr int i = decltype(I_)::v;
      constexpr int YOUNGER[7] = {0, 12, 13, 12, 12, 11, 10};
      if constexpr (i < 3) { p_one(pbA, 8 + 2 * i, PF[8 + 2 * i]); p_one(pbA, 9 + 2 * i, PF[9 + 2 * i]); }
      if constexpr (i == 4) asm volatile("global_load_dword %0, %1, %2 sc1" : "=v"(fb) : "v"(foB + (tgA ^ 1) * 128), "s"(ws));
      if constexpr (i >= 1 && i < 7) wait_vm<YOUNGER[i < 7 ? i : 0]>();
      if constexpr (i == 7) {
        if (w == 0) wait_vm<8>();
        else {
          wait_vm<0>();
          u32 fc;
          asm volatile("v_mov_b32 %0, %1" : "=v"(fc) : "v"(fb));
          if (!__all(fc == tag)) poll(foB + (tgA ^ 1) * 128);
        }
        BAR();
        __builtin_amdgcn_raw_buffer_store_b32(tag, frs, f_off, tg_of(3) * 2048, 16);
        STAMP(14);
#pragma unroll
        for (int k = 0; k < 7; k++) p_one(pbB, k, QF[k]);
        STAMP(15);
      }
      if constexpr (i >= 8) wait_vm<22 - i>();
      if constexpr (i < 5) { wd_take(9 + i, 0); wd_take(9 + i, 1); }
      PIN(PF[i]);
      ac[0] = mfma32(D[2 * i], PF[i], ac[0]);
      ac[1] = mfma32(D[2 * i + 1], PF[i], ac[1]);
      SCHED();
    });
#define RES_LD(i, ro, o) asm volatile("global_load_ushort %0, %1, %2 offset:" #o " sc1" : "=v"(res[i]) : "v"(ro), "s"(ws));
    RES_LD(0, roA, 0) RES_LD(1, roA, 64) RES_LD(2, roA, 128) RES_LD(3, roA, 192)
    RES_LD(4, roB, 0) RES_LD(5, roB, 64) RES_LD(6, roB, 128) RES_LD(7, roB, 192)
#undef RES_LD
    STAMP(16);
#pragma unroll
    for (int i = 0; i < 16; i++) { bc[0][i] = 0; bc[1][i] = 0; }
    SCHED();
    static_for<0, 14>([&](auto I_) {
      constexpr int i = decltype(I_)::v;
      if constexpr (i == BP) {
        poll(foB + tgA * 128);
        asm volatile("" : "+v"(res[0]), "+v"(res[1]), "+v"(res[2]), "+v"(res[3]), "+v"(res[4]), "+v"(res[5]), "+v"(res[6]), "+v"(res[7]), "+v"(fb));
        STAMP(21);
#pragma unroll
        for (int k = 7; k < 14; k++) p_one(pbB, k, QF[k]);
      }
      if constexpr (i == 7) { BAR(); red_get(0); }
      if constexpr (i < BP || i >= 7) wait_vm<i < 7 ? 14 - i : 13 - i>();
      PIN(QF[i]);
      bc[0] = mfma32(D[2 * i], QF[i], bc[0]);
      if constexpr (i < 4) red_put(0, ac[i >> 1], 2 * i);
      SCHED();
      if constexpr (i < 8) bc[1] = mfma32(D[2 * i + 1], QF[i], bc[1]);
      if constexpr (i < 4) red_put(0, ac[i >> 1], 2 * i + 1);
      SCHED();
    });
#pragma unroll
    for (int i = 8; i < 14; i++) {
      bc[1] = mfma32(D[2 * i + 1], QF[i], bc[1]);
      if (i >= 9 && i < 13) red_put(1, bc[0], i - 9);
      SCHED();
    }
    STAMP(17);
#pragma unroll
    for (int o = 4; o < 8; o++) red_put(1, bc[1], o);
    BAR();
    red_get(1);
    u32 o4[4];
    {
      const u32 y0 = pk_bf16(r[0][0], r[0][1]), y1 = pk_bf16(r[0][2], r[0][3]), y2 = pk_bf16(r[1][0], r[1][1]), y3 = pk_bf16(r[1][2], r[1][3]);
      o4[0] = pk_bf16(bf_lo(res[0]) + bf_lo(y0), bf_lo(res[1]) + bf_hi(y0));
      o4[1] = pk_bf16(bf_lo(res[2]) + bf_lo(y1), bf_lo(res[3]) + bf_hi(y1));
      o4[2] = pk_bf16(bf_lo(res[4]) + bf_lo(y2), bf_lo(res[5]) + bf_hi(y2));
      o4[3] = pk_bf16(bf_lo(res[6]) + bf_lo(y3), bf_lo(res[7]) + bf_hi(y3));
    }
    const auto x0 = __builtin_amdgcn_permlane32_swap(o4[0], o4[2], false, false);
    const auto x1 = __builtin_amdgcn_permlane32_swap(o4[1], o4[3], false, false);
    u32x4 v; v[0] = x0[0]; v[1] = x1[0]; v[2] = x0[1]; v[3] = x1[1];
    *(u32x4 *)(p.out + (size_t)((pn2 * 4 + (hh ? tgB : tgA)) * 32 + tl) * DM + own_n) = v;
    STAMP(18);
  }
  asm volatile(".Lcode_end:");
}

extern "C" void kernel_launch(void *const *d_in, const int *in_sizes, int n_in, void *d_out, int out_size, void *d_ws,
                              size_t ws_size, hipStream_t stream) {
  Params p;
  p.x = (const u16 *)d_in[0]; p.logA = (const u16 *)d_in[1]; p.Aim = (const u16 *)d_in[2]; p.Cr = (const u16 *)d_in[3];
  p.Ci = (const u16 *)d_in[4]; p.logdt = (const u16 *)d_in[5]; p.Dsk = (const u16 *)d_in[6];
  p.wg = (const u16 *)d_in[8]; p.wu = (const u16 *)d_in[9]; p.wd = (const u16 *)d_in[10];
  p.out = (u16 *)d_out; p.ws = (char *)d_ws;
  (void)hipFuncSetAttribute((const void *)fused, hipFuncAttributeMaxDynamicSharedMemorySize, LDS_BYTES);
  fused<<<256, 512, LDS_BYTES, stream>>>(p);
}
```

```cpp
#include <hip/hip_runtime.h>
#include <hip/hip_bf16.h>
#include <stdint.h>

#define STAMPS 0

typedef unsigned short u16;
typedef unsigned int u32;
typedef unsigned long long u64;
typedef float f32x2 __attribute__((ext_vector_type(2)));
typedef float f32x4 __attribute__((ext_vector_type(4)));
typedef float f32x16 __attribute__((ext_vector_type(16)));
typedef _Float16 h16x8 __attribute__((ext_vector_type(8)));
typedef _Float16 h16x2 __attribute__((ext_vector_type(2)));
typedef __bf16 b16x8 __attribute__((ext_vector_type(8)));
typedef __bf16 b16x2 __attribute__((ext_vector_type(2)));
typedef short s16x4 __attribute__((ext_vector_type(4)));
typedef u32 u32x2 __attribute__((ext_vector_type(2)));
typedef u32 u32x4 __attribute__((ext_vector_type(4)));
#define LAS __attribute__((address_space(3)))
#define GAS __attribute__((address_space(1)))
#define CAS __attribute__((address_space(4)))

constexpr int SEQ = 2048, DM = 512, NMODE = 128, FF = 1792;

constexpr u32 WS_EPOCH = 0, WS_ST = 4096, WS_FLAG2 = 32768, WS_FLAG3 = 65536, WS_FLAG1 = 196608;
constexpr u32 WS_HT = 1u << 20, WS_P = 4u << 20, WS_DBG = 16u << 20, WS_EX = 12u << 20, WS_XT = 18u << 20;

constexpr u32 LDS_BYTES = 163840;
constexpr u32 XY_STR = 544, XY_CH = 53 * XY_STR;
constexpr u32 L_XY = 0;
constexpr u32 UR_STR = 4640, UR_CH = 4 * UR_STR;
constexpr u32 L_UR = 0;
constexpr u32 L_XS = 37120;
constexpr u32 K_CH = 8448;
constexpr u32 L_K = 45312;
constexpr u32 VT_CH = 1344;
constexpr u32 L_VT = 62464;
constexpr u32 L_MISC = 9216;
constexpr u32 L_YP = 65536;
constexpr u32 T_STR = 1056, L_T = 57664, L_TX = L_T + 8 * T_STR;
constexpr u32 L_W0 = 81920;
constexpr u32 L_W2 = 49152;
constexpr u32 L_A = 0;
constexpr u32 L_ST = 131072;
constexpr u32 L_RED = 0;
constexpr int SPIN_CAP = 1 << 17;

struct Params {
  const u16 *x, *logA, *Aim, *Cr, *Ci, *logdt, *Dsk, *wg, *wu, *wd;
  u16 *out;
  char *ws;
};

__device__ __forceinline__ float bf_lo(u32 v) { return __uint_as_float(v << 16); }
__device__ __forceinline__ float bf_hi(u32 v) { return __uint_as_float(v & 0xffff0000u); }
__device__ __forceinline__ u32 pk_bf16(float a, float b) {
  b16x2 r; r[0] = (__bf16)a; r[1] = (__bf16)b; return __builtin_bit_cast(u32, r);
}
__device__ __forceinline__ float rbf(float a) { return bf_lo(pk_bf16(a, a)); }
__device__ __forceinline__ u32 pk_f16(float a, float b) {
  h16x2 r; r[0] = (_Float16)a; r[1] = (_Float16)b; return __builtin_bit_cast(u32, r);
}
template <typename T> __device__ __forceinline__ LAS T *lds(u32 off) { return (LAS T *)(size_t)off; }
__device__ __forceinline__ u32x2 tr_read(u32 addr) {
  return __builtin_bit_cast(u32x2, __builtin_amdgcn_ds_read_tr16_b64_v4i16((LAS s16x4 *)(size_t)addr));
}
__device__ __forceinline__ u32x4 cat(u32x2 a, u32x2 b) { u32x4 r; r[0] = a[0]; r[1] = a[1]; r[2] = b[0]; r[3] = b[1]; return r; }
__device__ __forceinline__ f32x16 mfma32(u32x4 a, u32x4 b, f32x16 c) {
  return __builtin_amdgcn_mfma_f32_32x32x16_bf16(__builtin_bit_cast(b16x8, a), __builtin_bit_cast(b16x8, b), c, 0, 0, 0);
}
__device__ __forceinline__ f32x4 mfma16h(u32x4 a, u32x4 b, f32x4 c) {
  return __builtin_amdgcn_mfma_f32_16x16x32_f16(__builtin_bit_cast(h16x8, a), __builtin_bit_cast(h16x8, b), c, 0, 0, 0);
}
__device__ __forceinline__ float dot2bf(u32 a, u32 b, float c) {
  return __builtin_amdgcn_fdot2_f32_bf16(__builtin_bit_cast(b16x2, a), __builtin_bit_cast(b16x2, b), c, false);
}
__device__ __forceinline__ void glds(u32 voff, const void *sbase, u32 ldst) {
  asm volatile("s_mov_b32 m0, %2\n\ts_nop 0\n\tglobal_load_lds_dwordx4 %0, %1" ::"v"(voff), "s"(sbase), "s"(ldst) : "memory");
}
__device__ __forceinline__ void glds_sc1(u32 voff, const void *sbase, u32 ldst) {
  asm volatile("s_mov_b32 m0, %2\n\ts_nop 0\n\tglobal_load_lds_dwordx4 %0, %1 sc1" ::"v"(voff), "s"(sbase), "s"(ldst) : "memory");
}
#define WAIT_VM(n) asm volatile("s_waitcnt vmcnt(" #n ")" ::: "memory")
#define WAIT_LGKM0() asm volatile("s_waitcnt lgkmcnt(0)" ::: "memory")
#define BAR() do { asm volatile("s_waitcnt lgkmcnt(0)\n\ts_barrier" ::: "memory"); } while (0)
#define SCHED() __builtin_amdgcn_sched_barrier(0)
#define PIN(x) asm volatile("" : "+v"(x))
#define RAWBAR() asm volatile("s_barrier" ::: "memory")
template <int N> __device__ __forceinline__ void wait_vm() { asm volatile("s_waitcnt vmcnt(%0)" ::"n"(N) : "memory"); }
template <int I> struct IC { static constexpr int v = I; };
template <int B, int E, typename Fn> __device__ __forceinline__ void static_for(Fn &&f) {
  if constexpr (B < E) { f(IC<B>{}); static_for<B + 1, E>(f); }
}
constexpr int G1_OPS[] = {-8, -7, -6, -5, -4, -3, -2, -1, 2, 6, 10, 14, 18, 22, 26, 30,
                          27, 57, 89, 119,
                          62, 90, 122,
                          97, 101, 115,
                          34, 38, 42, 46, 50, 54, 58, 63, 65, 67, 69, 71, 73, 75, 78, 80, 82, 85,
                          96, 98, 100, 102, 104, 106, 108, 110, 112, 114,
                          121, 123, 124, 125, 126, 127};
constexpr int G1_TAKE[] = {66, 69, 72, 98, 101, 104, 110, 116, 122};
constexpr int g1_take(int p) { for (int i = 0; i < 9; i++) if (G1_TAKE[i] == p || G1_TAKE[i] + 1 == p) return 2 * i + p - G1_TAKE[i]; return -1; }
#define BP 1
#define LOOK 3
constexpr int epi0(int tg) { return tg == 1 ? 5 : 1; }
constexpr int g1_younger(int x, int p) { int n = 0; for (int q : G1_OPS) n += (q > x && q < p); return n; }
constexpr int g1_find(int first, int cnt, int p) { for (int i = 0; i < cnt; i++) if (G1_OPS[first + i] == p) return i; return -1; }
constexpr int wd_slot(int u) { return u < 4 ? 16 + u : u < 12 ? u - 4 : u < 16 ? u + 4 : u < 18 ? u - 16 : u < 22 ? u - 10 : u - 20; }

template <int CTRL> __device__ __forceinline__ float dpp(float v) {
  return __uint_as_float(__builtin_amdgcn_update_dpp(0u, __float_as_uint(v), CTRL, 0xf, 0xf, true));
}
__device__ __forceinline__ float wave_sum(float v) {
  v += dpp<0xB1>(v); v += dpp<0x4E>(v); v += dpp<0x141>(v); v += dpp<0x140>(v);
  auto a = __builtin_amdgcn_permlane16_swap(__float_as_uint(v), __float_as_uint(v), false, false);
  v = __uint_as_float(a[0]) + __uint_as_float(a[1]);
  auto c = __builtin_amdgcn_permlane32_swap(__float_as_uint(v), __float_as_uint(v), false, false);
  return __uint_as_float(c[0]) + __uint_as_float(c[1]);
}
__device__ __forceinline__ float wave_max(float v) {
  v = fmaxf(v, dpp<0xB1>(v)); v = fmaxf(v, dpp<0x4E>(v)); v = fmaxf(v, dpp<0x141>(v)); v = fmaxf(v, dpp<0x140>(v));
  auto a = __builtin_amdgcn_permlane16_swap(__float_as_uint(v), __float_as_uint(v), false, false);
  v = fmaxf(__uint_as_float(a[0]), __uint_as_float(a[1]));
  auto c = __builtin_amdgcn_permlane32_swap(__float_as_uint(v), __float_as_uint(v), false, false);
  return fmaxf(__uint_as_float(c[0]), __uint_as_float(c[1]));
}
struct cf { float r, i; };
__device__ __forceinline__ cf cmul(cf a, cf b) { return {a.r * b.r - a.i * b.i, a.r * b.i + a.i * b.r}; }
__device__ __forceinline__ cf cexp_at(float lr, float rev, float pos) {
  float e = __builtin_amdgcn_exp2f(lr * pos), f = __builtin_amdgcn_fractf(rev * pos);
  return {e * __builtin_amdgcn_cosf(f), e * __builtin_amdgcn_sinf(f)};
}

#define STAMP(i)

__global__ void __launch_bounds__(512, 2) fused(Params p) {
  extern __shared__ __attribute__((aligned(1024))) char smem_[];
  const u32 tid = threadIdx.x, lane = tid & 63;
  const u32 w = __builtin_amdgcn_readfirstlane(tid >> 6);
  const u32 b = blockIdx.x, xcd = b & 7, jj = b >> 3;
  u32 junk;
  {
    u64 pc; u32 sz;
    asm volatile(".Lcode_begin:\n\ts_getpc_b64 %0\n\ts_mov_b32 %1, .Lcode_end-.Lcode_begin" : "=s"(pc), "=s"(sz));
    const u32 co = min((jj * 8 + w) * 256 + lane * 4, sz);
    asm volatile("global_load_dword %0, %1, %2" : "=v"(junk) : "v"(co), "s"(pc));
  }
  char *const ws = p.ws;
  STAMP(0);
  const bool g1 = jj < 28;
  const u32 panel = (xcd & 4) * 2 + (jj < 24 ? jj & 7 : (xcd & 1) * 4 + (jj & 3));
  const u32 ft = 7 * ((xcd >> 1) & 1) + (jj < 24 ? (xcd & 1) * 4 + (jj >> 3) : 3);
  const u32 rot1 = (jj >> 3) * 8;
  u32 tag = 0;

  u32x4 W[32];
  const u32 w1_voff = ((lane >> 4) * FF + ft * 128) * 2 + ((((lane & 15) >> 1) ^ ((lane >> 4) | ((w & 1) << 2))) * 32) + (lane & 1) * 16;
  const char *const w1_base = (const char *)((w & 1) ? p.wu : p.wg) + (size_t)((w >> 1) * 4) * (FF * 2);
  auto w1_one = [&](u32 ks, u32 dst) {
    glds(w1_voff, w1_base + (size_t)((ks + rot1) & 31) * 16 * (FF * 2), dst + w * 1024);
  };
  const u32 g4 = lane >> 4, q4 = (lane >> 2) & 3, p4 = lane & 3;
  const u32 w1_rd = (g4 >> 1) * 2048 + (g4 & 1) * 1024 + q4 * 256 + ((w ^ (q4 | ((g4 & 1) << 2))) * 32) + p4 * 8;

  const u32 cp = xcd * 32 + jj, d0 = 2 * cp;
  const u32 ch = w >> 2, job = w & 3;
  u32x4 xr; u32 pl, pa, pr, pi;
  {
    const u32 o1 = (8 * b + w) * (DM * 2) + lane * 16;
    asm volatile("global_load_dwordx4 %0, %1, %2" : "=v"(xr) : "v"(o1), "s"(p.x));
    const u32 o2 = ((d0 + ch) * NMODE + 2 * lane) * 2;
    asm volatile("global_load_dword %0, %1, %2" : "=v"(pl) : "v"(o2), "s"(p.logA));
    asm volatile("global_load_dword %0, %1, %2" : "=v"(pa) : "v"(o2), "s"(p.Aim));
    asm volatile("global_load_dword %0, %1, %2" : "=v"(pr) : "v"(o2), "s"(p.Cr));
    asm volatile("global_load_dword %0, %1, %2" : "=v"(pi) : "v"(o2), "s"(p.Ci));
  }
  u32 ldt2, dsk2;
  asm volatile("s_load_dword %0, %1, 0x0" : "=s"(tag) : "s"(ws) : "memory");
  asm volatile("s_load_dword %0, %1, 0x0" : "=s"(ldt2) : "s"(p.logdt + d0));
  asm volatile("s_load_dword %0, %1, 0x0" : "=s"(dsk2) : "s"(p.Dsk + d0));
  {
#pragma unroll
    for (int i = 0; i < 4; i++) w1_one(i, L_W0 + i * 8192);
    const u32 gw = b * 8 + w, po = (gw < 1792 ? gw : gw - 1024) * 1024 + lane * 16;
    glds(po, p.wg, L_W0 + 65536 + w * 1024); glds(po, p.wu, L_W0 + 65536 + w * 1024); glds(po, p.wd, L_W0 + 65536 + w * 1024);
  }
  asm volatile("s_waitcnt lgkmcnt(0)" : "+s"(tag), "+s"(ldt2), "+s"(dsk2));
  tag += 1u;
  auto poll = [&](u32 fo) {
#pragma unroll 1
    for (int spin = 0; spin < SPIN_CAP; spin++) {
      u32 f;
      asm volatile("global_load_dword %0, %1, %2 sc1\n\ts_waitcnt vmcnt(0)" : "=&v"(f) : "v"(fo), "s"(ws) : "memory");
      if (__all(f == tag)) break;
      __builtin_amdgcn_s_sleep(4);
    }
  };
  asm volatile("s_waitcnt vmcnt(11)" : "+v"(xr));
  {
    *lds<u32x4>(L_TX + w * T_STR + lane * 16) = xr;
    float v[8];
#pragma unroll
    for (int i = 0; i < 4; i++) { v[2 * i] = bf_lo(xr[i]); v[2 * i + 1] = bf_hi(xr[i]); }
    const float m = wave_sum(((v[0] + v[1]) + (v[2] + v[3])) + ((v[4] + v[5]) + (v[6] + v[7]))) * (1.f / DM);
    float q0 = 0, q1 = 0;
#pragma unroll
    for (int i = 0; i < 4; i++) { const float t0 = v[i] - m, t1 = v[i + 4] - m; q0 += t0 * t0; q1 += t1 * t1; }
    const float var = rbf(wave_sum(q0 + q1) * (1.f / DM));
    const float rs = __builtin_amdgcn_rcpf(rbf(__builtin_amdgcn_sqrtf(rbf(var + 1e-5f)))), mb = rbf(m);
    u32 uu[4];
#pragma unroll
    for (int i = 0; i < 4; i++) {
      const u32 a = pk_bf16(v[2 * i] - mb, v[2 * i + 1] - mb);
      const u32 u = pk_bf16(bf_lo(a) * rs, bf_hi(a) * rs);
      uu[i] = pk_f16(bf_lo(u), bf_hi(u));
    }
    u32x4 o; o[0] = uu[0]; o[1] = uu[1]; o[2] = uu[2]; o[3] = uu[3];
    *lds<u32x4>(L_T + w * T_STR + lane * 16) = o;
  }
  BAR();
  {
    __amdgpu_buffer_rsrc_t rs = __builtin_amdgcn_make_buffer_rsrc(ws + WS_EX, 0, 8u << 20, 0x00020000);
    const u32 ra = L_T + (4 * (p4 & 1) + 3 - q4) * T_STR + (64 * w + 8 * g4 + 4 * (p4 >> 1)) * 2;
    const u32 rx = L_TX + (3 - q4) * T_STR + (64 * w + 16 * g4 + 4 * p4) * 2;
    const u32 so = ((lane >> 3) * 256 + b) * 128 + (lane & 7) * 16;
    u32x2 r[2];
#pragma unroll
    for (int i = 0; i < 2; i++) r[i] = tr_read(ra + i * 64);
    const u32x4 xt = cat(tr_read(rx + 4 * T_STR), tr_read(rx));
#pragma unroll
    for (int i = 0; i < 2; i++) {
      u32x4 o; o[0] = r[i][0]; o[1] = tag; o[2] = r[i][1]; o[3] = tag;
      __builtin_amdgcn_raw_buffer_store_b128(o, rs, so + (16 * w + 8 * i) * 32768, 0, 16);
    }
    __builtin_amdgcn_raw_buffer_store_b128(xt, rs, so + (WS_XT - WS_EX) + 8 * w * 32768, 0, 16);
  }
  STAMP(1);
  u32 vidx[8];
  {
    const u32 l0 = 8 * (tid & 255);
#pragma unroll
    for (int e = 0; e < 8; e++) {
      const u32 l = l0 + e, bits = pk_bf16((float)l, 0.f) & 0xffffu;
      vidx[e] = L_VT + (tid >> 8) * VT_CH + 2 * (l < 256 ? l : bits - (0x4380u - 256u));
    }
#pragma unroll
    for (int e = 0; e < 8; e++) asm volatile("" : "+v"(vidx[e]));
  }
  asm volatile("s_waitcnt vmcnt(10)" : "+v"(pl), "+v"(pa), "+v"(pr), "+v"(pi));
  const float dt = rbf(__expf(ch ? bf_hi(ldt2) : bf_lo(ldt2)));
  {
    const float LOG2E = 1.4426950408889634f, INV2PI = 0.15915494309189535f;
    float lr[2], rv[2];
    lr[0] = -rbf(__expf(bf_lo(pl))) * dt * LOG2E; lr[1] = -rbf(__expf(bf_hi(pl))) * dt * LOG2E;
    rv[0] = bf_lo(pa) * dt * INV2PI; rv[1] = bf_hi(pa) * dt * INV2PI;
    const u32 wb = L_XY + ch * XY_CH + lane * 8;
    auto put = [&](int row, cf a, cf c) {
      u32x2 v; v[0] = pk_f16(a.r, a.i); v[1] = pk_f16(c.r, c.i);
      *lds<u32x2>(wb + row * XY_STR) = v;
    };
    auto G = [&](int i) { w1_one(4 + i, L_W0 + (4 + i) * 8192); if (i >= 2) w1_one(6 + i, L_W0 + (6 + i) * 8192); };
    if (job < 2) {
      G(0);
      cf s0 = cexp_at(lr[0], -rv[0], 8.f), s1 = cexp_at(lr[1], -rv[1], 8.f);
      const float p0 = job ? 128.f : 0.f;
      cf z0 = cexp_at(lr[0], -rv[0], p0), z1 = cexp_at(lr[1], -rv[1], p0);
      const u32 wb2 = wb + job * 16 * XY_STR;
#pragma unroll
      for (int i = 0; i < 16; i++) {
        u32x2 v; v[0] = pk_f16(z0.r, z0.i); v[1] = pk_f16(z1.r, z1.i);
        *lds<u32x2>(wb2 + i * XY_STR) = v;
        if (i < 15) { z0 = cmul(z0, s0); z1 = cmul(z1, s1); }
        if (i == 2 || i == 7 || i == 12) G(1 + i / 5);
        SCHED();
      }
    } else {
      G(0);
      cf c0 = {bf_lo(pr), bf_lo(pi)}, c1 = {bf_hi(pr), bf_hi(pi)};
      const float mx = wave_max(fmaxf(fmaxf(fabsf(c0.r), fabsf(c0.i)), fmaxf(fabsf(c1.r), fabsf(c1.i))));
      const int ex = __builtin_amdgcn_frexp_expf(mx);
      const float sc = __builtin_amdgcn_ldexpf(1.f, -ex);
      c0.r *= sc; c0.i *= sc; c1.r *= sc; c1.i *= sc;
      G(1);
      if (job == 2) {
        if (lane == 0) *lds<float>(L_MISC + ch * 4) = __builtin_amdgcn_ldexpf(2.f * dt, ex);
        cf s0 = cexp_at(lr[0], rv[0], 1.f), s1 = cexp_at(lr[1], rv[1], 1.f);
        cf z0 = c0, z1 = c1;
#pragma unroll
        for (int i = 0; i < 8; i++) { put(32 + i, z0, z1); if (i < 7) { z0 = cmul(z0, s0); z1 = cmul(z1, s1); } if (i == 2 || i == 7) G(2 + i / 5); SCHED(); }
        s0 = cmul(s0, s0); s1 = cmul(s1, s1);
        z0 = cmul(c0, cexp_at(lr[0], rv[0], 256.f)); z1 = cmul(c1, cexp_at(lr[1], rv[1], 256.f));
#pragma unroll
        for (int i = 0; i < 4; i++) { put(40 + i, z0, z1); if (i < 3) { z0 = cmul(z0, s0); z1 = cmul(z1, s1); } }
      } else {
        cf f0 = cexp_at(lr[0], rv[0], 4.f), f1 = cexp_at(lr[1], rv[1], 4.f);
        cf s0 = cexp_at(lr[0], rv[0], 256.f), s1 = cexp_at(lr[1], rv[1], 256.f);
        G(2); SCHED();
        cf z0 = cmul(c0, cexp_at(lr[0], rv[0], 512.f)), z1 = cmul(c1, cexp_at(lr[1], rv[1], 512.f));
        put(44, z0, z1); put(45, cmul(z0, f0), cmul(z1, f1));
        z0 = cmul(z0, s0); z1 = cmul(z1, s1);
        put(46, z0, z1); put(47, cmul(z0, f0), cmul(z1, f1));
#pragma unroll
        for (int i = 0; i < 5; i++) { z0 = cmul(z0, s0); z1 = cmul(z1, s1); put(48 + i, z0, z1); if (i == 1) G(3); SCHED(); }
      }
    }
  }
  BAR();
  STAMP(2);
  u32x4 ex[2];
  const char *const exb = ws + WS_EX + (size_t)(cp >> 1) * 32768 + (cp & 1) * 32;
  const u32 exo = (tid >> 2) * 128 + (tid & 2) * 32 + (tid & 1) * 16;
  asm volatile("global_load_dwordx4 %0, %1, %2 sc1" : "=v"(ex[0]) : "v"(exo), "s"(exb));
  asm volatile("global_load_dwordx4 %0, %1, %2 sc1" : "=v"(ex[1]) : "v"(exo), "s"(exb + 16384));
  {
    const u32 rb = job >> 1, cb = job & 1;
    const u32 xa = L_XY + ch * XY_CH + (32 + 16 * rb + (lane & 15)) * XY_STR + g4 * 16;
    const u32 ya = L_XY + ch * XY_CH + (16 * cb + (lane & 15)) * XY_STR + g4 * 16;
    u32x4 fa[8], fb[8];
#pragma unroll
    for (int ks = 0; ks < 8; ks++) { fa[ks] = *lds<u32x4>(xa + ks * 64); fb[ks] = *lds<u32x4>(ya + ks * 64); }
    SCHED();
    const u32 j = 16 * cb + (lane & 15);
    u32 i0, is, i2;
    if (rb == 0) {
      i0 = g4 < 2 ? 4 * g4 + 8 * j : (g4 == 2 ? 256 + 4 * j : 384 + 2 * j); is = 1; i2 = g4 == 3 ? 64 : 2;
    } else {
      i0 = g4 == 0 ? 512 + j : 640 + j; is = g4 == 0 ? 32 : 0; i2 = 2 * is;
    }
    f32x4 a0 = {0, 0, 0, 0}, a1 = {0, 0, 0, 0};
#pragma unroll
    for (int ks = 0; ks < 8; ks += 2) { a0 = mfma16h(fa[ks], fb[ks], a0); a1 = mfma16h(fa[ks + 1], fb[ks + 1], a1); }
    a0 += a1;
    const u32 vb = L_VT + ch * VT_CH;
    if (rb == 0 || g4 == 0) {
#pragma unroll
      for (int r = 0; r < 4; r++) *lds<_Float16>(vb + 2 * (i0 + (r & 1) * is + (r >> 1) * i2)) = (_Float16)a0[r];
    } else if (g4 == 1) {
      *lds<_Float16>(vb + 2 * i0) = (_Float16)a0[0];
    }
  }
  STAMP(4);
  asm volatile("s_waitcnt vmcnt(0)" : "+v"(ex[0]), "+v"(ex[1]), "+v"(junk));
#pragma unroll 1
  for (int spin = 0; spin < SPIN_CAP; spin++) {
    if (__all(ex[0][1] == tag && ex[0][3] == tag && ex[1][1] == tag && ex[1][3] == tag)) break;
    __builtin_amdgcn_s_sleep(1);
    asm volatile("global_load_dwordx4 %0, %2, %3 sc1\n\tglobal_load_dwordx4 %1, %2, %4 sc1\n\ts_waitcnt vmcnt(0)"
                 : "=&v"(ex[0]), "=&v"(ex[1]) : "v"(exo), "s"(exb), "s"(exb + 16384) : "memory");
  }
  BAR();
  STAMP(5);
  if (tid == 0) __hip_atomic_store((GAS u32 *)(ws + WS_FLAG1) + b, tag, __ATOMIC_RELAXED, __HIP_MEMORY_SCOPE_AGENT);
  {
    u32 wr = L_W0 + w1_rd;
    PIN(wr);
#pragma unroll
    for (int ks = 0; ks < 10; ks++) { const u32 a = (ks < 8 ? wr : wr + 65536) + (ks & 7) * 8192; W[ks] = cat(tr_read(a), tr_read(a + 4096)); }
    SCHED();
  }
  {
    const u32 ab = L_UR + 3 * UR_STR + (tid & 1) * UR_CH + (2044 - 1024 - 8 * (tid >> 2) - 2 * (tid & 2)) * 2;
#pragma unroll
    for (int i = 0; i < 2; i++) { u32x2 v; v[0] = ex[i][0]; v[1] = ex[i][2]; *lds<u32x2>(ab + (1 - i) * 2048) = v; }
  }
  {
    u32x4 kv;
    _Float16 t[8];
#pragma unroll
    for (int e = 0; e < 8; e++) t[e] = *lds<_Float16>(vidx[e]);
#pragma unroll
    for (int e = 0; e < 4; e++) { h16x2 t2; t2[0] = t[2 * e]; t2[1] = t[2 * e + 1]; kv[e] = __builtin_bit_cast(u32, t2); }
    const u32 kb = L_K + (tid >> 8) * K_CH + (tid & 255) * 16;
    u32x2 lo, hi; lo[0] = kv[0]; lo[1] = kv[1]; hi[0] = kv[2]; hi[1] = kv[3];
    *lds<u32x2>(kb + 120) = lo; *lds<u32x2>(kb + 128) = hi;
    *lds<u32x4>(kb + 4224 + 112) = kv;
    const u32x2 z2 = {0, 0};
    u32 za;
    if (tid < 64) {
      const u32 c = tid >> 5, i = tid & 31;
      za = L_K + c * K_CH + (i < 15 ? i * 8 : 4224 + (i < 29 ? i - 15 : 0) * 8);
    } else {
      const u32 i = tid < 448 ? tid - 64 : 0, c = i / 48;
      za = L_UR + c * UR_STR + 4096 + (i - c * 48) * 8;
    }
    *lds<u32x2>(za) = z2;
  }
  BAR();
  {
    const u32 ub = L_UR + (tid >> 8) * UR_CH + (tid & 255) * 16;
    const u32x4 a = *lds<u32x4>(ub + 3 * UR_STR);
    const u32x2 c = *lds<u32x2>(ub + 3 * UR_STR + 16);
    SCHED();
#pragma unroll
    for (int i = 0; i < 4; i++) w1_one(10 + i, L_W0 + i * 8192);
    SCHED();
    u32x4 o;
    o[0] = __builtin_amdgcn_alignbit(a[1], a[0], 16); o[1] = __builtin_amdgcn_alignbit(a[2], a[1], 16);
    o[2] = __builtin_amdgcn_alignbit(a[3], a[2], 16); o[3] = __builtin_amdgcn_alignbit(c[0], a[3], 16);
    *lds<u32x4>(ub + 2 * UR_STR) = o;
    u32x4 o1; o1[0] = a[1]; o1[1] = a[2]; o1[2] = a[3]; o1[3] = c[0];
    *lds<u32x4>(ub + UR_STR) = o1;
    u32x4 o0; o0[0] = o[1]; o0[1] = o[2]; o0[2] = o[3]; o0[3] = __builtin_amdgcn_alignbit(c[1], c[0], 16);
    *lds<u32x4>(ub) = o0;
  }
#pragma unroll
  for (int i = 4; i < 8; i++) w1_one(10 + i, L_W0 + i * 8192);
  BAR();
  STAMP(6);
  u32 f1;
  const u32 xp = 255 - 32 * w - (lane >> 1);
  const u32 pg = w & 1, cg = (w >> 1) & 1;
  f32x4 ya[4];
#pragma unroll
  for (int i = 0; i < 4; i++) ya[i] = f32x4{0, 0, 0, 0};
  {
    const u32 c16 = lane & 15;
    const u32 ka = L_K + ch * K_CH + (c16 & 1) * 4224 + (c16 >> 1) * 16 + g4 * 16 + cg * 4 * 64;
    const u32 ua = L_UR + ch * UR_CH + (c16 & 3) * UR_STR + (1984 - 1792 - 64 * (c16 >> 2) + 8 * g4) * 2 + cg * 4 * 64;
    u32x4 kf[2][8], uf[2][8];
    auto rd1 = [&](int s, int j) {
      if (j >= 1 && j <= 8) uf[s & 1][8 - j] = *lds<u32x4>(ua + (j - 1) * 512 + s * 64);
      else kf[s & 1][j ? j - 8 : 0] = *lds<u32x4>(ka + (j ? j - 8 : 0) * 512 + s * 64);
    };
    auto rd = [&](int s, int i) { rd1(s, 2 * i); rd1(s, 2 * i + 1); };
    u32 wr = L_W0 + w1_rd;
    PIN(wr);
    auto side = [&](int s, int i) {
      const int n = s * 8 + i;
      if (n == 5) asm volatile("global_load_dword %0, %1, %2 sc1" : "=v"(f1) : "v"(xp * 4), "s"(ws + WS_FLAG1));
      if (n == 1 || n == 3) w1_one(26 + n / 3, L_W0 + (8 + n / 3) * 8192);
      if (n == 6) { WAIT_VM(3); RAWBAR(); }
      if (n >= 6 && n < 10) {
#pragma unroll
        for (int k = 2 * n - 12; k < 2 * n - 10; k++) W[10 + k] = cat(tr_read(wr + k * 8192), tr_read(wr + k * 8192 + 4096));
      }
      if (n == 11) {
        asm volatile("s_waitcnt vmcnt(0)" : "+v"(f1));
        if (!__all(f1 == tag)) poll(WS_FLAG1 + xp * 4);
        glds_sc1(xp * 128 + (lane & 1) * 16, ws + WS_XT + (size_t)(cp >> 2) * 32768 + (cp & 3) * 32, L_XS + w * 1024);
      }
      if (n == 13) BAR();
      if (n >= 13 && n < 29 && (n & 1)) w1_one(18 + (n - 13) / 2, L_W0 + (n - 13) / 2 * 8192);
    };
#pragma unroll
    for (int i = 0; i < 8; i++) rd(0, i);
    if (pg == 0) {
#pragma unroll
      for (int s = 0; s < 4; s++) {
#pragma unroll
        for (int i = 0; i < 8; i++) {
          if (s < 3) rd(s + 1, i);
          side(s, i);
          ya[3] = mfma16h(uf[s & 1][7 - i], kf[s & 1][i], ya[3]);
          if (i <= 4) ya[2] = mfma16h(uf[s & 1][4 - i], kf[s & 1][i], ya[2]);
          if (i <= 3) ya[1] = mfma16h(uf[s & 1][3 - i], kf[s & 1][i], ya[1]);
          if (i <= 0) ya[0] = mfma16h(uf[s & 1][0 - i], kf[s & 1][i], ya[0]);
          SCHED();
        }
      }
    } else {
#pragma unroll
      for (int s = 0; s < 4; s++) {
#pragma unroll
        for (int i = 0; i < 8; i++) {
          if (s < 3) rd(s + 1, i);
          side(s, i);
          if (i <= 6) ya[3] = mfma16h(uf[s & 1][6 - i], kf[s & 1][i], ya[3]);
          if (i <= 5) ya[2] = mfma16h(uf[s & 1][5 - i], kf[s & 1][i], ya[2]);
          if (i <= 2) ya[1] = mfma16h(uf[s & 1][2 - i], kf[s & 1][i], ya[1]);
          if (i <= 1) ya[0] = mfma16h(uf[s & 1][1 - i], kf[s & 1][i], ya[0]);
          SCHED();
        }
      }
    }
  }
  WAIT_VM(8);
  {
    const u32 yb = L_YP + w * 2048 + lane * 16;
    *lds<f32x4>(yb) = cg ? ya[0] : ya[2];
    *lds<f32x4>(yb + 1024) = cg ? ya[1] : ya[3];
  }
  BAR();
  STAMP(7);
  w1_one(28, L_W2); w1_one(29, L_W2 + 8192);
  {
    const u32 yb = L_YP + (w ^ 2) * 2048 + lane * 16;
    const float ksc = *lds<float>(L_MISC + ch * 4), dsk = ch ? bf_hi(dsk2) : bf_lo(dsk2);
    __amdgpu_buffer_rsrc_t rs = __builtin_amdgcn_make_buffer_rsrc(ws + WS_HT, 0, SEQ * DM * 2, 0x00020000);
    f32x4 o[2]; u32x2 uu[2], xx[2]; u32 t0[2];
#pragma unroll
    for (int i = 0; i < 2; i++) {
      o[i] = *lds<f32x4>(yb + i * 1024);
      t0[i] = 256 * (4 * cg + (i ? 3 - pg : pg)) + 4 * lane;
      uu[i] = *lds<u32x2>(L_UR + ch * UR_CH + 3 * UR_STR + (2044 - t0[i]) * 2);
      xx[i] = *lds<u32x2>(L_XS + ((2044 - t0[i]) >> 3) * 32 + ch * 16 + ((2044 - t0[i]) & 7) * 2);
    }
#pragma unroll
    for (int i = 0; i < 2; i++) {
      const f32x4 own = cg ? (i ? ya[3] : ya[2]) : (i ? ya[1] : ya[0]);
      const u32 uu0 = uu[i][0], uu1 = uu[i][1];
      const h16x2 ua = __builtin_bit_cast(h16x2, uu1), ub = __builtin_bit_cast(h16x2, uu0);
      const float uv[4] = {(float)ua[1], (float)ua[0], (float)ub[1], (float)ub[0]};
      const float xv[4] = {bf_hi(xx[i][1]), bf_lo(xx[i][1]), bf_hi(xx[i][0]), bf_lo(xx[i][0])};
      float hv[4];
#pragma unroll
      for (int r = 0; r < 4; r += 2) {
        const u32 y2 = pk_bf16((o[i][r] + own[r]) * ksc, (o[i][r + 1] + own[r + 1]) * ksc);
        const u32 d2 = pk_bf16(uv[r] * dsk, uv[r + 1] * dsk);
        const u32 z2 = pk_bf16(bf_lo(y2) + bf_lo(d2), bf_hi(y2) + bf_hi(d2));
        hv[r] = xv[r] + bf_lo(z2); hv[r + 1] = xv[r + 1] + bf_hi(z2);
      }
      u32x2 hv2; hv2[0] = pk_bf16(hv[0], hv[1]); hv2[1] = pk_bf16(hv[2], hv[3]);
      const u32 off = ((t0[i] >> 5) * DM + d0 + ch) * 64 + (t0[i] & 31) * 2;
      __builtin_amdgcn_raw_buffer_store_b64(hv2, rs, off, 0, 16);
    }
  }
  SCHED();
  if (b == 0 && tid == 0)
    __hip_atomic_store((GAS u32 *)(ws + WS_EPOCH), tag, __ATOMIC_RELAXED, __HIP_MEMORY_SCOPE_AGENT);
  BAR();
  f32x16 cs;
#pragma unroll
  for (int i = 0; i < 16; i++) cs[i] = 0;
  const u32x4 ones = {0x3f803f80u, 0x3f803f80u, 0x3f803f80u, 0x3f803f80u};
  w1_one(30, L_YP); w1_one(31, L_YP + 8192);
#pragma unroll
  for (int ks = 0; ks < 10; ks++) cs = mfma32(W[ks], ones, cs);
  SCHED();
  WAIT_VM(2);
  RAWBAR();
  if (tid == 0) __hip_atomic_store((GAS u32 *)(ws + WS_FLAG2) + b * 32, tag, __ATOMIC_RELAXED, __HIP_MEMORY_SCOPE_AGENT);
  STAMP(8);
  const u32 t32 = lane & 31;
  const u32 nt = jj & 7, tt = jj >> 3, pn2 = 2 * xcd + (tt >> 1), tgA = tt & 1, tgB = 3 - tgA;
  const u32 lv = lane * 16, rot2 = nt == 7 ? 0 : nt;
  auto ft_of = [&](int i) { const u32 k = (i % 7) + rot2; return 2 * (k >= 7 ? k - 7 : k) + (i < 7 ? tgA ^ 1 : tgA); };
  const u32 d_voff = (lane >> 3) * (DM * 2) + (((lane & 7) ^ (((lane >> 4) & 1) << 2)) * 16);
  const char *const wd_base = (const char *)p.wd + (size_t)(w * 16) * (DM * 2) + nt * 128;
  auto wd_one = [&](int u) {
    const u32 k = __builtin_amdgcn_readfirstlane((ft_of(u >> 1) << 17) + ((u & 1) << 13));
    glds(d_voff, wd_base + k, wd_slot(u) * 8192 + w * 1024);
  };
  u32x4 D[28];
  u32 d_rd[2][3];
#pragma unroll
  for (int h = 0; h < 2; h++) {
    d_rd[h][0] = w * 1024 + (((4 * (g4 >> 1) + q4) * 128 + h * 64 + (g4 & 1) * 32 + p4 * 8) ^ (((q4 >> 1) & 1) * 64));
    d_rd[h][1] = d_rd[h][0] + 65536; d_rd[h][2] = d_rd[h][0] + 131072;
  }
  auto wd_take = [&](int j, int h) {
    const u32 o = wd_slot(2 * j) * 8192;
    D[2 * j + h] = cat(tr_read(d_rd[h][o >> 16] + (o & 65535)), tr_read(d_rd[h][o >> 16] + (o & 65535) + 8192));
  };
  auto lane_again = []() {
    u32 l; asm volatile("v_mbcnt_lo_u32_b32 %0, -1, 0\n\tv_mbcnt_hi_u32_b32 %0, -1, %0" : "=v"(l)); return l;
  };
  auto foA_of = [&]() { return WS_FLAG3 + ((pn2 * 4 + tgA) * 16 + min(lane_again(), 13u)) * 128; };
  __amdgpu_buffer_rsrc_t prs = __builtin_amdgcn_make_buffer_rsrc(ws + WS_P, 0, SEQ * FF * 2, 0x00020000);
  __amdgpu_buffer_rsrc_t frs = __builtin_amdgcn_make_buffer_rsrc(ws + WS_FLAG3, 0, 131072, 0x00020000);
  const u32 OOB = 0x40000000u;
  const u32 p_off = g1 ? (panel * 4 * 112 + ft * 8 + w) * 1024 + lane * 16 : OOB;
  const u32 f_off = g1 && tid == 0 ? (panel * 64 + ft) * 128 : OOB;
  auto tg_of = [&](int g) { return g < 2 ? (u32)g : g ^ (ft & 1); };
  float e_m, e_ne, e_s2, e_g, e_u, e_e, e_o;
  u32 pko[4];
  u32x2 sg;
  const u32 sg_off = WS_ST + (panel * 128 + t32) * 8;
  auto st_take = [&](int tg) {
    PIN(sg);
#pragma unroll 1
    for (int spin = 0; spin < SPIN_CAP; spin++) {
      if (__all(sg[1] == tag)) break;
      __builtin_amdgcn_s_sleep(1);
      asm volatile("global_load_dwordx2 %0, %1, %2 sc1\n\ts_waitcnt vmcnt(0)" : "=&v"(sg) : "v"(sg_off), "s"(ws + tg_of(tg) * 256) : "memory");
    }
    e_m = bf_lo(sg[0]);
    const float sd = bf_hi(sg[0]);
    e_ne = -1.4426950408889634f * __builtin_amdgcn_rcpf(sd); e_s2 = sd * sd;
  };
  auto epi_step = [&](int ks, int tg, const f32x16 &prev) {
    if (ks == 0) st_take(tg);
    else if (ks >= 1 && ks < 25) {
      const int i = (ks - 1) / 3, ph = (ks - 1) % 3;
      if (ph == 0) {
        e_g = prev[i] - e_m * cs[i]; PIN(e_g);
        e_e = __builtin_amdgcn_exp2f(e_g * e_ne);
      } else if (ph == 1) {
        e_u = (prev[i + 8] - e_m * cs[i + 8]) * e_g;
        e_e = e_e * e_s2 + e_s2;
        PIN(e_u); PIN(e_e);
      } else {
        const float o = e_u * __builtin_amdgcn_rcpf(e_e);
        if (i & 1) { pko[i >> 1] = pk_bf16(e_o, o); PIN(pko[i >> 1]); } else { e_o = o; PIN(e_o); }
      }
    } else if (ks == 25) {
      u32x4 v; v[0] = pko[0]; v[1] = pko[1]; v[2] = pko[2]; v[3] = pko[3];
      __builtin_amdgcn_raw_buffer_store_b128(v, prs, p_off + tg_of(tg) * (112 * 1024), 0, 16);
    }
  };
  const char *const pbA = ws + WS_P + ((size_t)(pn2 * 4 + tgA) * 112 + w) * 1024;
  const char *const pbB = ws + WS_P + ((size_t)(pn2 * 4 + tgB) * 112 + w) * 1024;
  u32x4 PF[14], QF[14];
  auto p_one = [&](const char *pb, int i, u32x4 &d) {
    const u32 j = __builtin_amdgcn_readfirstlane(ft_of(i) << 13);
    asm volatile("global_load_dwordx4 %0, %1, %2 sc1" : "=v"(d) : "v"(lv), "s"(pb + j));
  };
  f32x16 acc[2];
  if (g1) {
    {
      u32 wr = L_W0 + w1_rd;
      PIN(wr);
#pragma unroll
      for (int ks = 0; ks < 8; ks++) W[18 + ks] = cat(tr_read(wr + ks * 8192), tr_read(wr + ks * 8192 + 4096));
      wr += 65536;
#pragma unroll
      for (int ks = 0; ks < 2; ks++) W[26 + ks] = cat(tr_read(wr + ks * 8192), tr_read(wr + ks * 8192 + 4096));
      wr = L_W2 + w1_rd;
      PIN(wr);
#pragma unroll
      for (int ks = 0; ks < 2; ks++) W[28 + ks] = cat(tr_read(wr + ks * 8192), tr_read(wr + ks * 8192 + 4096));
#pragma unroll
      for (int ks = 10; ks < 18; ks++) cs = mfma32(W[ks], ones, cs);
      SCHED();
      if (w == 0) WAIT_VM(1); else WAIT_VM(0);
      RAWBAR();
#pragma unroll
      for (int ks = 2; ks < 4; ks++) W[28 + ks] = cat(tr_read(wr + ks * 8192), tr_read(wr + ks * 8192 + 4096));
    }
    STAMP(9);
    poll(WS_FLAG2 + (w * 32 + t32) * 128);
    BAR();
    STAMP(10);
    const char *const a_base = ws + WS_HT + (size_t)panel * 131072;
    auto a_one = [&](int i) { glds_sc1(lv, a_base + tg_of(i >> 2) * 32768 + (((i & 3) * 8 + w + rot1) & 31) * 1024, L_A + w * 1024 + i * 8192); };
#pragma unroll
    for (int i = 0; i < 4; i++) a_one(i);
#pragma unroll
    for (int ks = 18; ks < 32; ks++) { cs = mfma32(W[ks], ones, cs); if (ks % 3 == 0 && ks < 30) { a_one(ks / 3 - 2); SCHED(); } }
    SCHED();
    u32 ab = L_A + (4 * (g4 >> 1) + q4) * 64 + (g4 & 1) * 32 + p4 * 8;
    u32x4 F[LOOK + 1];
    auto frag = [&](u32 a) { return cat(tr_read(a), tr_read(a + 512)); };
    u32 fa;
    wait_vm<g1_younger(-8, 0)>();
    RAWBAR();
    u32 ab2 = ab + 65536;
    PIN(ab); PIN(ab2);
#pragma unroll
    for (int i = 0; i < LOOK; i++) F[i] = frag(ab + i * 1024);
    static_for<0, 128>([&](auto P_) {
      constexpr int P = decltype(P_)::v, tg = P >> 5, ks = P & 31;
      if constexpr (ks == 0) {
#pragma unroll
        for (int i = 0; i < 16; i++) acc[tg & 1][i] = 0;
      }
      if constexpr (P == 8 - LOOK) { wait_vm<g1_younger(-7, P)>(); RAWBAR(); }
      if constexpr (P == 16 - LOOK) { wait_vm<g1_younger(-6, P)>(); RAWBAR(); }
      if constexpr (P == 24 - LOOK) { wait_vm<g1_younger(-5, P)>(); RAWBAR(); }
      if constexpr (P == 32 - LOOK) { wait_vm<g1_younger(-1, P)>(); RAWBAR(); }
      if constexpr (P == 48) { wait_vm<g1_younger(14, P)>(); RAWBAR(); }
      if constexpr (P == 64) { wait_vm<g1_younger(30, P)>(); RAWBAR(); }
      if constexpr (P == 66) wait_vm<g1_younger(54, P)>();
      if constexpr (P == 96) { wait_vm<g1_younger(85, P)>(); RAWBAR(); }
      if constexpr (P == 101) { wait_vm<g1_younger(90, P)>(); RAWBAR(); }
      if constexpr (g1_find(23, 2, P) >= 0) __builtin_amdgcn_raw_buffer_store_b32(tag, frs, f_off, 2048 * g1_find(23, 2, P), 16);
      if constexpr (P == G1_OPS[25]) asm volatile("global_load_dword %0, %1, %2 sc1" : "=v"(fa) : "v"(foA_of()), "s"(ws));
      if constexpr (P == 121) {
        wait_vm<g1_younger(115, P)>();
        PIN(fa);
        if (!__all(fa == tag)) poll(foA_of());
      }
      if constexpr (g1_find(54, 6, P) >= 0) p_one(pbA, g1_find(54, 6, P), PF[g1_find(54, 6, P)]);
      if constexpr (g1_find(8, 8, P) >= 0) a_one(8 + g1_find(8, 8, P));
      if constexpr (g1_find(16, 4, P) >= 0)
        asm volatile("global_load_dwordx2 %0, %1, %2 sc1" : "=v"(sg) : "v"(sg_off), "s"(ws + tg_of(g1_find(16, 4, P)) * 256));
      if constexpr (g1_find(26, 28, P) >= 0) wd_one(g1_find(26, 28, P));
      if constexpr (g1_take(P) >= 0) wd_take(g1_take(P) >> 1, g1_take(P) & 1);
      constexpr int n = P + LOOK;
      if constexpr (n < 128) F[n % (LOOK + 1)] = frag((n < 64 ? ab : ab2) + (n & 63) * 1024);
      acc[tg & 1] = mfma32(W[ks], F[P % (LOOK + 1)], acc[tg & 1]);
      if constexpr (tg > 0 && ks == epi0(tg)) wait_vm<g1_younger(G1_OPS[16 + tg - 1], P + 1)>();
      if constexpr (tg > 0 && ks >= epi0(tg)) epi_step(ks - epi0(tg), tg - 1, acc[(tg - 1) & 1]);
      SCHED();
      if constexpr (P == 31) STAMP(11);
      if constexpr (P == 63) STAMP(19);
      if constexpr (P == 95) STAMP(20);
      if constexpr (P == 127) STAMP(3);
    });
    wait_vm<g1_younger(119, 128)>();
    st_take(3);
    STAMP(12);
  } else {
    poll(WS_FLAG2 + (w * 32 + t32) * 128);
    RAWBAR();
    const u32 tgs = (2 * xcd + ((jj >> 1) & 1)) * 4 + (jj & 1);
    const char *base = ws + WS_HT + (size_t)tgs * 32768 + w * 4096;
#pragma unroll
    for (int i = 0; i < 8; i++) glds_sc1(lv, base + (i >> 2) * 65536 + (i & 3) * 1024, 65536 + (i >> 2) * 32768 + w * 4096 + (i & 3) * 1024);
    const u32 ab = 65536 + w * 4096 + (4 * (g4 >> 1) + q4) * 64 + (g4 & 1) * 32 + p4 * 8;
#pragma unroll
    for (int g = 0; g < 2; g++) {
      if (g == 0) WAIT_VM(4); else WAIT_VM(0);
      float s_s = 0, s_q = 0;
#pragma unroll
      for (int i = 0; i < 4; i++) {
        const u32x4 f = cat(tr_read(ab + g * 32768 + i * 1024), tr_read(ab + g * 32768 + i * 1024 + 512));
#pragma unroll
        for (int k = 0; k < 4; k++) { s_s = dot2bf(f[k], 0x3f803f80u, s_s); s_q = dot2bf(f[k], f[k], s_q); }
      }
      const auto sw = __builtin_amdgcn_permlane32_swap(__float_as_uint(s_s), __float_as_uint(s_q), false, false);
      *lds<float>(g * 2048 + w * 256 + lane * 4) = __uint_as_float(sw[0]) + __uint_as_float(sw[1]);
      BAR();
      float sp = 0;
#pragma unroll
      for (int i = 0; i < 8; i++) sp += *lds<float>(g * 2048 + i * 256 + lane * 4);
      const auto s2 = __builtin_amdgcn_permlane32_swap(__float_as_uint(sp), __float_as_uint(sp), false, false);
      const float mean = __uint_as_float(s2[0]) * (1.f / DM);
      const float var = rbf(fmaxf(__uint_as_float(s2[1]) * (1.f / DM) - mean * mean, 0.f));
      const u64 gr = ((u64)tag << 32) | pk_bf16(mean, __builtin_amdgcn_sqrtf(rbf(var + 1e-5f)));
      if (tid < 32) __hip_atomic_store((GAS u64 *)(ws + WS_ST) + (tgs + 2 * g) * 32 + lane, gr, __ATOMIC_RELAXED, __HIP_MEMORY_SCOPE_AGENT);
    }
    BAR();
#pragma unroll
    for (int u = 0; u < 12; u++) wd_one(u);
    WAIT_VM(0);
#pragma unroll
    for (int j = 0; j < 6; j++) { wd_take(j, 0); wd_take(j, 1); }
    WAIT_LGKM0();
#pragma unroll
    for (int u = 12; u < 28; u++) wd_one(u);
    poll(foA_of());
#pragma unroll
    for (int j = 6; j < 9; j++) { wd_take(j, 0); wd_take(j, 1); }
#pragma unroll
    for (int i = 0; i < 6; i++) p_one(pbA, i, PF[i]);
    asm volatile("s_waitcnt vmcnt(0)" : "+v"(PF[0]), "+v"(PF[1]), "+v"(PF[2]), "+v"(PF[3]), "+v"(PF[4]), "+v"(PF[5]));
#pragma unroll
    for (int i = 0; i < 16; i++) acc[1][i] = 0;
    e_m = 0; e_ne = 0; e_s2 = 1.f;
  }

  {
    const u32 ln = lane_again(), hh = ln >> 5, tl = ln & 31;
    const u32 foB = WS_FLAG3 + ((pn2 * 4 + tgB) * 16 + 2 * min(ln, 6u)) * 128;
    p_one(pbA, 6, PF[6]); p_one(pbA, 7, PF[7]);
    SCHED();
    {
      const f32x2 m2 = {e_m, e_m}, ne2 = {e_ne, e_ne}, s22 = {e_s2, e_s2};
      u32x4 v;
#pragma unroll
      for (int i = 0; i < 8; i += 2) {
        const f32x2 g = f32x2{acc[1][i], acc[1][i + 1]} - m2 * f32x2{cs[i], cs[i + 1]};
        const f32x2 u = (f32x2{acc[1][i + 8], acc[1][i + 9]} - m2 * f32x2{cs[i + 8], cs[i + 9]}) * g;
        f32x2 e = g * ne2;
        e[0] = __builtin_amdgcn_exp2f(e[0]); e[1] = __builtin_amdgcn_exp2f(e[1]);
        e = e * s22 + s22;
        e[0] = __builtin_amdgcn_rcpf(e[0]); e[1] = __builtin_amdgcn_rcpf(e[1]);
        const f32x2 o = u * e;
        v[i >> 1] = pk_bf16(o[0], o[1]);
      }
      __builtin_amdgcn_raw_buffer_store_b128(v, prs, p_off + tg_of(3) * (112 * 1024), 0, 16);
    }
    SCHED();
    STAMP(13);
    wait_vm<8>();
    RAWBAR();
    __builtin_amdgcn_raw_buffer_store_b32(tag, frs, f_off, tg_of(2) * 2048, 16);
    const u32 own_n = nt * 64 + w * 8;
    u32 res[8];
    const u32 roA = WS_HT + ((pn2 * 4 + tgA) * DM + own_n + 4 * hh) * 64 + tl * 2;
    const u32 roB = WS_HT + ((pn2 * 4 + tgB) * DM + own_n + 4 * hh) * 64 + tl * 2;
    f32x16 ac[2];
    f32x4 r[2];
    f32x16 bc[2];
    u32 rw = w * 1024 + lv;
    PIN(rw);
    auto red_put = [&](int t, const f32x16 &a, int o) {
      f32x4 v; v[0] = a[4 * (o & 3)]; v[1] = a[4 * (o & 3) + 1]; v[2] = a[4 * (o & 3) + 2]; v[3] = a[4 * (o & 3) + 3];
      *lds<f32x4>((t ? rw + 65536 : rw) + o * 8192) = v;
    };
    auto red_get = [&](int t) {
      f32x4 v[8];
#pragma unroll
      for (int i = 0; i < 8; i++) v[i] = *lds<f32x4>(t * 65536 + w * 8192 + i * 1024 + lv);
      r[t] = ((v[0] + v[1]) + (v[2] + v[3])) + ((v[4] + v[5]) + (v[6] + v[7]));
    };
#pragma unroll
    for (int i = 0; i < 16; i++) { ac[0][i] = 0; ac[1][i] = 0; }
    u32 fb;
    static_for<0, 14>([&](auto I_) {
      constexpr int i = decltype(I_)::v;
      constexpr int YOUNGER[7] = {0, 12, 13, 12, 12, 11, 10};
      if constexpr (i < 3) { p_one(pbA, 8 + 2 * i, PF[8 + 2 * i]); p_one(pbA, 9 + 2 * i, PF[9 + 2 * i]); }
      if constexpr (i == 4) asm volatile("global_load_dword %0, %1, %2 sc1" : "=v"(fb) : "v"(foB + (tgA ^ 1) * 128), "s"(ws));
      if constexpr (i >= 1 && i < 7) wait_vm<YOUNGER[i < 7 ? i : 0]>();
      if constexpr (i == 7) {
        if (w == 0) wait_vm<8>();
        else {
          wait_vm<0>();
          u32 fc;
          asm volatile("v_mov_b32 %0, %1" : "=v"(fc) : "v"(fb));
          if (!__all(fc == tag)) poll(foB + (tgA ^ 1) * 128);
        }
        BAR();
        __builtin_amdgcn_raw_buffer_store_b32(tag, frs, f_off, tg_of(3) * 2048, 16);
        STAMP(14);
#pragma unroll
        for (int k = 0; k < 7; k++) p_one(pbB, k, QF[k]);
        STAMP(15);
      }
      if constexpr (i >= 8) wait_vm<22 - i>();
      if constexpr (i < 5) { wd_take(9 + i, 0); wd_take(9 + i, 1); }
      PIN(PF[i]);
      ac[0] = mfma32(D[2 * i], PF[i], ac[0]);
      ac[1] = mfma32(D[2 * i + 1], PF[i], ac[1]);
      SCHED();
    });
#define RES_LD(i, ro, o) asm volatile("global_load_ushort %0, %1, %2 offset:" #o " sc1" : "=v"(res[i]) : "v"(ro), "s"(ws));
    RES_LD(0, roA, 0) RES_LD(1, roA, 64) RES_LD(2, roA, 128) RES_LD(3, roA, 192)
    RES_LD(4, roB, 0) RES_LD(5, roB, 64) RES_LD(6, roB, 128) RES_LD(7, roB, 192)
#undef RES_LD
    STAMP(16);
#pragma unroll
    for (int i = 0; i < 16; i++) { bc[0][i] = 0; bc[1][i] = 0; }
    SCHED();
    static_for<0, 14>([&](auto I_) {
      constexpr int i = decltype(I_)::v;
      if constexpr (i == BP) {
        poll(foB + tgA * 128);
        asm volatile("" : "+v"(res[0]), "+v"(res[1]), "+v"(res[2]), "+v"(res[3]), "+v"(res[4]), "+v"(res[5]), "+v"(res[6]), "+v"(res[7]), "+v"(fb));
        STAMP(21);
#pragma unroll
        for (int k = 7; k < 14; k++) p_one(pbB, k, QF[k]);
      }
      if constexpr (i == 7) { BAR(); red_get(0); }
      if constexpr (i < BP || i >= 7) wait_vm<i < 7 ? 14 - i : 13 - i>();
      PIN(QF[i]);
      bc[0] = mfma32(D[2 * i], QF[i], bc[0]);
      if constexpr (i < 4) red_put(0, ac[i >> 1], 2 * i);
      SCHED();
      if constexpr (i < 8) bc[1] = mfma32(D[2 * i + 1], QF[i], bc[1]);
      if constexpr (i < 4) red_put(0, ac[i >> 1], 2 * i + 1);
      SCHED();
    });
#pragma unroll
    for (int i = 8; i < 14; i++) {
      bc[1] = mfma32(D[2 * i + 1], QF[i], bc[1]);
      if (i >= 9 && i < 13) red_put(1, bc[0], i - 9);
      SCHED();
    }
    STAMP(17);
#pragma unroll
    for (int o = 4; o < 8; o++) red_put(1, bc[1], o);
    BAR();
    red_get(1);
    u32 o4[4];
    {
      const u32 y0 = pk_bf16(r[0][0], r[0][1]), y1 = pk_bf16(r[0][2], r[0][3]), y2 = pk_bf16(r[1][0], r[1][1]), y3 = pk_bf16(r[1][2], r[1][3]);
      o4[0] = pk_bf16(bf_lo(res[0]) + bf_lo(y0), bf_lo(res[1]) + bf_hi(y0));
      o4[1] = pk_bf16(bf_lo(res[2]) + bf_lo(y1), bf_lo(res[3]) + bf_hi(y1));
      o4[2] = pk_bf16(bf_lo(res[4]) + bf_lo(y2), bf_lo(res[5]) + bf_hi(y2));
      o4[3] = pk_bf16(bf_lo(res[6]) + bf_lo(y3), bf_lo(res[7]) + bf_hi(y3));
    }
    const auto x0 = __builtin_amdgcn_permlane32_swap(o4[0], o4[2], false, false);
    const auto x1 = __builtin_amdgcn_permlane32_swap(o4[1], o4[3], false, false);
    u32x4 v; v[0] = x0[0]; v[1] = x1[0]; v[2] = x0[1]; v[3] = x1[1];
    *(u32x4 *)(p.out + (size_t)((pn2 * 4 + (hh ? tgB : tgA)) * 32 + tl) * DM + own_n) = v;
    STAMP(18);
  }
  asm volatile(".Lcode_end:");
}

extern "C" void kernel_launch(void *const *d_in, const int *in_sizes, int n_in, void *d_out, int out_size, void *d_ws,
                              size_t ws_size, hipStream_t stream) {
  Params p;
  p.x = (const u16 *)d_in[0]; p.logA = (const u16 *)d_in[1]; p.Aim = (const u16 *)d_in[2]; p.Cr = (const u16 *)d_in[3];
  p.Ci = (const u16 *)d_in[4]; p.logdt = (const u16 *)d_in[5]; p.Dsk = (const u16 *)d_in[6];
  p.wg = (const u16 *)d_in[8]; p.wu = (const u16 *)d_in[9]; p.wd = (const u16 *)d_in[10];
  p.out = (u16 *)d_out; p.ws = (char *)d_ws;
  (void)hipFuncSetAttribute((const void *)fused, hipFuncAttributeMaxDynamicSharedMemorySize, LDS_BYTES);
  fused<<<256, 512, LDS_BYTES, stream>>>(p);
}
```

```cpp
#include <hip/hip_runtime.h>
#include <hip/hip_bf16.h>
#include <stdint.h>

#define STAMPS 0

typedef unsigned short u16;
typedef unsigned int u32;
typedef unsigned long long u64;
typedef float f32x2 __attribute__((ext_vector_type(2)));
typedef float f32x4 __attribute__((ext_vector_type(4)));
typedef float f32x16 __attribute__((ext_vector_type(16)));
typedef _Float16 h16x8 __attribute__((ext_vector_type(8)));
typedef _Float16 h16x2 __attribute__((ext_vector_type(2)));
typedef __bf16 b16x8 __attribute__((ext_vector_type(8)));
typedef __bf16 b16x2 __attribute__((ext_vector_type(2)));
typedef short s16x4 __attribute__((ext_vector_type(4)));
typedef u32 u32x2 __attribute__((ext_vector_type(2)));
typedef u32 u32x4 __attribute__((ext_vector_type(4)));
#define LAS __attribute__((address_space(3)))
#define GAS __attribute__((address_space(1)))
#define CAS __attribute__((address_space(4)))

constexpr int SEQ = 2048, DM = 512, NMODE = 128, FF = 1792;

constexpr u32 WS_EPOCH = 0, WS_ST = 4096, WS_FLAG2 = 32768, WS_FLAG3 = 65536, WS_FLAG1 = 196608;
constexpr u32 WS_HT = 1u << 20, WS_P = 4u << 20, WS_DBG = 16u << 20, WS_EX = 12u << 20, WS_XT = 18u << 20;

constexpr u32 LDS_BYTES = 163840;
constexpr u32 XY_STR = 544, XY_CH = 53 * XY_STR;
constexpr u32 L_XY = 0;
constexpr u32 UR_STR = 4640, UR_CH = 4 * UR_STR;
constexpr u32 L_UR = 0;
constexpr u32 L_XS = 37120;
constexpr u32 K_CH = 8448;
constexpr u32 L_K = 45312;
constexpr u32 VT_CH = 1344;
constexpr u32 L_VT = 62464;
constexpr u32 L_MISC = 9216;
constexpr u32 L_YP = 65536;
constexpr u32 T_STR = 1056, L_T = 57664, L_TX = L_T + 8 * T_STR;
constexpr u32 L_W0 = 81920;
constexpr u32 L_W2 = 49152;
constexpr u32 L_A = 0;
constexpr u32 L_ST = 131072;
constexpr u32 L_RED = 0;
constexpr int SPIN_CAP = 1 << 17;

struct Params {
  const u16 *x, *logA, *Aim, *Cr, *Ci, *logdt, *Dsk, *wg, *wu, *wd;
  u16 *out;
  char *ws;
};

__device__ __forceinline__ float bf_lo(u32 v) { return __uint_as_float(v << 16); }
__device__ __forceinline__ float bf_hi(u32 v) { return __uint_as_float(v & 0xffff0000u); }
__device__ __forceinline__ u32 pk_bf16(float a, float b) {
  b16x2 r; r[0] = (__bf16)a; r[1] = (__bf16)b; return __builtin_bit_cast(u32, r);
}
__device__ __forceinline__ float rbf(float a) { return bf_lo(pk_bf16(a, a)); }
__device__ __forceinline__ u32 pk_f16(float a, float b) {
  h16x2 r; r[0] = (_Float16)a; r[1] = (_Float16)b; return __builtin_bit_cast(u32, r);
}
template <typename T> __device__ __forceinline__ LAS T *lds(u32 off) { return (LAS T *)(size_t)off; }
__device__ __forceinline__ u32x2 tr_read(u32 addr) {
  return __builtin_bit_cast(u32x2, __builtin_amdgcn_ds_read_tr16_b64_v4i16((LAS s16x4 *)(size_t)addr));
}
__device__ __forceinline__ u32x4 cat(u32x2 a, u32x2 b) { u32x4 r; r[0] = a[0]; r[1] = a[1]; r[2] = b[0]; r[3] = b[1]; return r; }
__device__ __forceinline__ f32x16 mfma32(u32x4 a, u32x4 b, f32x16 c) {
  return __builtin_amdgcn_mfma_f32_32x32x16_bf16(__builtin_bit_cast(b16x8, a), __builtin_bit_cast(b16x8, b), c, 0, 0, 0);
}
__device__ __forceinline__ f32x4 mfma16h(u32x4 a, u32x4 b, f32x4 c) {
  return __builtin_amdgcn_mfma_f32_16x16x32_f16(__builtin_bit_cast(h16x8, a), __builtin_bit_cast(h16x8, b), c, 0, 0, 0);
}
__device__ __forceinline__ float dot2bf(u32 a, u32 b, float c) {
  return __builtin_amdgcn_fdot2_f32_bf16(__builtin_bit_cast(b16x2, a), __builtin_bit_cast(b16x2, b), c, false);
}
__device__ __forceinline__ void glds(u32 voff, const void *sbase, u32 ldst) {
  asm volatile("s_mov_b32 m0, %2\n\ts_nop 0\n\tglobal_load_lds_dwordx4 %0, %1" ::"v"(voff), "s"(sbase), "s"(ldst) : "memory");
}
__device__ __forceinline__ void glds_sc1(u32 voff, const void *sbase, u32 ldst) {
  asm volatile("s_mov_b32 m0, %2\n\ts_nop 0\n\tglobal_load_lds_dwordx4 %0, %1 sc1" ::"v"(voff), "s"(sbase), "s"(ldst) : "memory");
}
#define WAIT_VM(n) asm volatile("s_waitcnt vmcnt(" #n ")" ::: "memory")
#define WAIT_LGKM0() asm volatile("s_waitcnt lgkmcnt(0)" ::: "memory")
#define BAR() do { asm volatile("s_waitcnt lgkmcnt(0)\n\ts_barrier" ::: "memory"); } while (0)
#define SCHED() __builtin_amdgcn_sched_barrier(0)
#define PIN(x) asm volatile("" : "+v"(x))
#define RAWBAR() asm volatile("s_barrier" ::: "memory")
template <int N> __device__ __forceinline__ void wait_vm() { asm volatile("s_waitcnt vmcnt(%0)" ::"n"(N) : "memory"); }
template <int I> struct IC { static constexpr int v = I; };
template <int B, int E, typename Fn> __device__ __forceinline__ void static_for(Fn &&f) {
  if constexpr (B < E) { f(IC<B>{}); static_for<B + 1, E>(f); }
}
constexpr int G1_OPS[] = {-8, -7, -6, -5, -4, -3, -2, -1, 2, 6, 10, 14, 18, 22, 26, 30,
                          27, 57, 89, 119,
                          62, 90, 122,
                          97, 101, 115,
                          34, 38, 42, 46, 50, 54, 58, 63, 65, 67, 69, 71, 73, 75, 78, 80, 82, 85,
                          96, 98, 100, 102, 104, 106, 108, 110, 112, 114,
                          121, 123, 124, 125, 126, 127};
constexpr int G1_TAKE[] = {66, 69, 72, 98, 101, 104, 110, 116, 122};
constexpr int g1_take(int p) { for (int i = 0; i < 9; i++) if (G1_TAKE[i] == p || G1_TAKE[i] + 1 == p) return 2 * i + p - G1_TAKE[i]; return -1; }
#define BP 1
#define LOOK 3
constexpr int epi0(int tg) { return tg == 1 ? 5 : 1; }
constexpr int g1_younger(int x, int p) { int n = 0; for (int q : G1_OPS) n += (q > x && q < p); return n; }
constexpr int g1_find(int first, int cnt, int p) { for (int i = 0; i < cnt; i++) if (G1_OPS[first + i] == p) return i; return -1; }
constexpr int wd_slot(int u) { return u < 4 ? 16 + u : u < 12 ? u - 4 : u < 16 ? u + 4 : u < 18 ? u - 16 : u < 22 ? u - 10 : u - 20; }

template <int CTRL> __device__ __forceinline__ float dpp(float v) {
  return __uint_as_float(__builtin_amdgcn_update_dpp(0u, __float_as_uint(v), CTRL, 0xf, 0xf, true));
}
__device__ __forceinline__ float wave_sum(float v) {
  v += dpp<0xB1>(v); v += dpp<0x4E>(v); v += dpp<0x141>(v); v += dpp<0x140>(v);
  auto a = __builtin_amdgcn_permlane16_swap(__float_as_uint(v), __float_as_uint(v), false, false);
  v = __uint_as_float(a[0]) + __uint_as_float(a[1]);
  auto c = __builtin_amdgcn_permlane32_swap(__float_as_uint(v), __float_as_uint(v), false, false);
  return __uint_as_float(c[0]) + __uint_as_float(c[1]);
}
__device__ __forceinline__ float wave_max(float v) {
  v = fmaxf(v, dpp<0xB1>(v)); v = fmaxf(v, dpp<0x4E>(v)); v = fmaxf(v, dpp<0x141>(v)); v = fmaxf(v, dpp<0x140>(v));
  auto a = __builtin_amdgcn_permlane16_swap(__float_as_uint(v), __float_as_uint(v), false, false);
  v = fmaxf(__uint_as_float(a[0]), __uint_as_float(a[1]));
  auto c = __builtin_amdgcn_permlane32_swap(__float_as_uint(v), __float_as_uint(v), false, false);
  return fmaxf(__uint_as_float(c[0]), __uint_as_float(c[1]));
}
struct cf { float r, i; };
__device__ __forceinline__ cf cmul(cf a, cf b) { return {a.r * b.r - a.i * b.i, a.r * b.i + a.i * b.r}; }
__device__ __forceinline__ cf cexp_at(float lr, float rev, float pos) {
  float e = __builtin_amdgcn_exp2f(lr * pos), f = __builtin_amdgcn_fractf(rev * pos);
  return {e * __builtin_amdgcn_cosf(f), e * __builtin_amdgcn_sinf(f)};
}

#define STAMP(i)

__global__ void __launch_bounds__(512, 2) fused(Params p) {
  extern __shared__ __attribute__((aligned(1024))) char smem_[];
  const u32 tid = threadIdx.x, lane = tid & 63;
  const u32 w = __builtin_amdgcn_readfirstlane(tid >> 6);
  const u32 b = blockIdx.x, xcd = b & 7, jj = b >> 3;
  u32 junk;
  {
    u64 pc; u32 sz;
    asm volatile(".Lcode_begin:\n\ts_getpc_b64 %0\n\ts_mov_b32 %1, .Lcode_end-.Lcode_begin" : "=s"(pc), "=s"(sz));
    const u32 co = min((jj * 8 + w) * 256 + lane * 4, sz);
    asm volatile("global_load_dword %0, %1, %2" : "=v"(junk) : "v"(co), "s"(pc));
  }
  char *const ws = p.ws;
  STAMP(0);
  const bool g1 = jj < 28;
  const u32 panel = (xcd & 4) * 2 + (jj < 24 ? jj & 7 : (xcd & 1) * 4 + (jj & 3));
  const u32 ft = 7 * ((xcd >> 1) & 1) + (jj < 24 ? (xcd & 1) * 4 + (jj >> 3) : 3);
  const u32 rot1 = (jj >> 3) * 8;
  u32 tag = 0;

  u32x4 W[32];
  const u32 w1_voff = ((lane >> 4) * FF + ft * 128) * 2 + ((((lane & 15) >> 1) ^ ((lane >> 4) | ((w & 1) << 2))) * 32) + (lane & 1) * 16;
  const char *const w1_base = (const char *)((w & 1) ? p.wu : p.wg) + (size_t)((w >> 1) * 4) * (FF * 2);
  auto w1_one = [&](u32 ks, u32 dst) {
    glds(w1_voff, w1_base + (size_t)((ks + rot1) & 31) * 16 * (FF * 2), dst + w * 1024);
  };
  const u32 g4 = lane >> 4, q4 = (lane >> 2) & 3, p4 = lane & 3;
  const u32 w1_rd = (g4 >> 1) * 2048 + (g4 & 1) * 1024 + q4 * 256 + ((w ^ (q4 | ((g4 & 1) << 2))) * 32) + p4 * 8;

  const u32 cp = xcd * 32 + jj, d0 = 2 * cp;
  const u32 ch = w >> 2, job = w & 3;
  u32x4 xr; u32 pl, pa, pr, pi;
  {
    const u32 o1 = (8 * b + w) * (DM * 2) + lane * 16;
    asm volatile("global_load_dwordx4 %0, %1, %2" : "=v"(xr) : "v"(o1), "s"(p.x));
    const u32 o2 = ((d0 + ch) * NMODE + 2 * lane) * 2;
    asm volatile("global_load_dword %0, %1, %2" : "=v"(pl) : "v"(o2), "s"(p.logA));
    asm volatile("global_load_dword %0, %1, %2" : "=v"(pa) : "v"(o2), "s"(p.Aim));
    asm volatile("global_load_dword %0, %1, %2" : "=v"(pr) : "v"(o2), "s"(p.Cr));
    asm volatile("global_load_dword %0, %1, %2" : "=v"(pi) : "v"(o2), "s"(p.Ci));
  }
  u32 ldt2, dsk2;
  asm volatile("s_load_dword %0, %1, 0x0" : "=s"(tag) : "s"(ws) : "memory");
  asm volatile("s_load_dword %0, %1, 0x0" : "=s"(ldt2) : "s"(p.logdt + d0));
  asm volatile("s_load_dword %0, %1, 0x0" : "=s"(dsk2) : "s"(p.Dsk + d0));
  {
#pragma unroll
    for (int i = 0; i < 4; i++) w1_one(i, L_W0 + i * 8192);
    const u32 gw = b * 8 + w, po = (gw < 1792 ? gw : gw - 1024) * 1024 + lane * 16;
    glds(po, p.wg, L_W0 + 65536 + w * 1024); glds(po, p.wu, L_W0 + 65536 + w * 1024); glds(po, p.wd, L_W0 + 65536 + w * 1024);
  }
  asm volatile("s_waitcnt lgkmcnt(0)" : "+s"(tag), "+s"(ldt2), "+s"(dsk2));
  tag += 1u;
  auto poll = [&](u32 fo) {
#pragma unroll 1
    for (int spin = 0; spin < SPIN_CAP; spin++) {
      u32 f;
      asm volatile("global_load_dword %0, %1, %2 sc1\n\ts_waitcnt vmcnt(0)" : "=&v"(f) : "v"(fo), "s"(ws) : "memory");
      if (__all(f == tag)) break;
      __builtin_amdgcn_s_sleep(4);
    }
  };
  asm volatile("s_waitcnt vmcnt(11)" : "+v"(xr));
  {
    *lds<u32x4>(L_TX + w * T_STR + lane * 16) = xr;
    float v[8];
#pragma unroll
    for (int i = 0; i < 4; i++) { v[2 * i] = bf_lo(xr[i]); v[2 * i + 1] = bf_hi(xr[i]); }
    const float m = wave_sum(((v[0] + v[1]) + (v[2] + v[3])) + ((v[4] + v[5]) + (v[6] + v[7]))) * (1.f / DM);
    float q0 = 0, q1 = 0;
#pragma unroll
    for (int i = 0; i < 4; i++) { const float t0 = v[i] - m, t1 = v[i + 4] - m; q0 += t0 * t0; q1 += t1 * t1; }
    const float var = rbf(wave_sum(q0 + q1) * (1.f / DM));
    const float rs = __builtin_amdgcn_rcpf(rbf(__builtin_amdgcn_sqrtf(rbf(var + 1e-5f)))), mb = rbf(m);
    u32 uu[4];
#pragma unroll
    for (int i = 0; i < 4; i++) {
      const u32 a = pk_bf16(v[2 * i] - mb, v[2 * i + 1] - mb);
      const u32 u = pk_bf16(bf_lo(a) * rs, bf_hi(a) * rs);
      uu[i] = pk_f16(bf_lo(u), bf_hi(u));
    }
    u32x4 o; o[0] = uu[0]; o[1] = uu[1]; o[2] = uu[2]; o[3] = uu[3];
    *lds<u32x4>(L_T + w * T_STR + lane * 16) = o;
  }
  BAR();
  {
    __amdgpu_buffer_rsrc_t rs = __builtin_amdgcn_make_buffer_rsrc(ws + WS_EX, 0, 8u << 20, 0x00020000);
    const u32 ra = L_T + (4 * (p4 & 1) + 3 - q4) * T_STR + (64 * w + 8 * g4 + 4 * (p4 >> 1)) * 2;
    const u32 rx = L_TX + (3 - q4) * T_STR + (64 * w + 16 * g4 + 4 * p4) * 2;
    const u32 so = ((lane >> 3) * 256 + b) * 128 + (lane & 7) * 16;
    u32x2 r[2];
#pragma unroll
    for (int i = 0; i < 2; i++) r[i] = tr_read(ra + i * 64);
    const u32x4 xt = cat(tr_read(rx + 4 * T_STR), tr_read(rx));
#pragma unroll
    for (int i = 0; i < 2; i++) {
      u32x4 o; o[0] = r[i][0]; o[1] = tag; o[2] = r[i][1]; o[3] = tag;
      __builtin_amdgcn_raw_buffer_store_b128(o, rs, so + (16 * w + 8 * i) * 32768, 0, 16);
    }
    __builtin_amdgcn_raw_buffer_store_b128(xt, rs, so + (WS_XT - WS_EX) + 8 * w * 32768, 0, 16);
  }
  STAMP(1);
  u32 vidx[8];
  {
    const u32 l0 = 8 * (tid & 255);
#pragma unroll
    for (int e = 0; e < 8; e++) {
      const u32 l = l0 + e, bits = pk_bf16((float)l, 0.f) & 0xffffu;
      vidx[e] = L_VT + (tid >> 8) * VT_CH + 2 * (l < 256 ? l : bits - (0x4380u - 256u));
    }
#pragma unroll
    for (int e = 0; e < 8; e++) asm volatile("" : "+v"(vidx[e]));
  }
  asm volatile("s_waitcnt vmcnt(10)" : "+v"(pl), "+v"(pa), "+v"(pr), "+v"(pi));
  const float dt = rbf(__expf(ch ? bf_hi(ldt2) : bf_lo(ldt2)));
  {
    const float LOG2E = 1.4426950408889634f, INV2PI = 0.15915494309189535f;
    float lr[2], rv[2];
    lr[0] = -rbf(__expf(bf_lo(pl))) * dt * LOG2E; lr[1] = -rbf(__expf(bf_hi(pl))) * dt * LOG2E;
    rv[0] = bf_lo(pa) * dt * INV2PI; rv[1] = bf_hi(pa) * dt * INV2PI;
    const u32 wb = L_XY + ch * XY_CH + lane * 8;
    auto put = [&](int row, cf a, cf c) {
      u32x2 v; v[0] = pk_f16(a.r, a.i); v[1] = pk_f16(c.r, c.i);
      *lds<u32x2>(wb + row * XY_STR) = v;
    };
    if (job < 2) {
      cf s0 = cexp_at(lr[0], -rv[0], 8.f), s1 = cexp_at(lr[1], -rv[1], 8.f);
      const float p0 = job ? 128.f : 0.f;
      cf z0 = cexp_at(lr[0], -rv[0], p0), z1 = cexp_at(lr[1], -rv[1], p0);
      const u32 wb2 = wb + job * 16 * XY_STR;
#pragma unroll
      for (int i = 0; i < 16; i++) {
        u32x2 v; v[0] = pk_f16(z0.r, z0.i); v[1] = pk_f16(z1.r, z1.i);
        *lds<u32x2>(wb2 + i * XY_STR) = v;
        if (i < 15) { z0 = cmul(z0, s0); z1 = cmul(z1, s1); }
        SCHED();
      }
    } else {
      cf c0 = {bf_lo(pr), bf_lo(pi)}, c1 = {bf_hi(pr), bf_hi(pi)};
      const float mx = wave_max(fmaxf(fmaxf(fabsf(c0.r), fabsf(c0.i)), fmaxf(fabsf(c1.r), fabsf(c1.i))));
      const int ex = __builtin_amdgcn_frexp_expf(mx);
      const float sc = __builtin_amdgcn_ldexpf(1.f, -ex);
      c0.r *= sc; c0.i *= sc; c1.r *= sc; c1.i *= sc;
      if (job == 2) {
        if (lane == 0) *lds<float>(L_MISC + ch * 4) = __builtin_amdgcn_ldexpf(2.f * dt, ex);
        cf s0 = cexp_at(lr[0], rv[0], 1.f), s1 = cexp_at(lr[1], rv[1], 1.f);
        cf z0 = c0, z1 = c1;
#pragma unroll
        for (int i = 0; i < 8; i++) { put(32 + i, z0, z1); if (i < 7) { z0 = cmul(z0, s0); z1 = cmul(z1, s1); } SCHED(); }
        s0 = cmul(s0, s0); s1 = cmul(s1, s1);
        z0 = cmul(c0, cexp_at(lr[0], rv[0], 256.f)); z1 = cmul(c1, cexp_at(lr[1], rv[1], 256.f));
#pragma unroll
        for (int i = 0; i < 4; i++) { put(40 + i, z0, z1); if (i < 3) { z0 = cmul(z0, s0); z1 = cmul(z1, s1); } }
      } else {
        cf f0 = cexp_at(lr[0], rv[0], 4.f), f1 = cexp_at(lr[1], rv[1], 4.f);
        cf s0 = cexp_at(lr[0], rv[0], 256.f), s1 = cexp_at(lr[1], rv[1], 256.f);
        cf z0 = cmul(c0, cexp_at(lr[0], rv[0], 512.f)), z1 = cmul(c1, cexp_at(lr[1], rv[1], 512.f));
        put(44, z0, z1); put(45, cmul(z0, f0), cmul(z1, f1));
        z0 = cmul(z0, s0); z1 = cmul(z1, s1);
        put(46, z0, z1); put(47, cmul(z0, f0), cmul(z1, f1));
#pragma unroll
        for (int i = 0; i < 5; i++) { z0 = cmul(z0, s0); z1 = cmul(z1, s1); put(48 + i, z0, z1); SCHED(); }
      }
    }
  }
  BAR();
  STAMP(2);
  u32x4 ex[2];
  const char *const exb = ws + WS_EX + (size_t)(cp >> 1) * 32768 + (cp & 1) * 32;
  const u32 exo = (tid >> 2) * 128 + (tid & 2) * 32 + (tid & 1) * 16;
  asm volatile("global_load_dwordx4 %0, %1, %2 sc1" : "=v"(ex[0]) : "v"(exo), "s"(exb));
  asm volatile("global_load_dwordx4 %0, %1, %2 sc1" : "=v"(ex[1]) : "v"(exo), "s"(exb + 16384));
  {
    const u32 rb = job >> 1, cb = job & 1;
    const u32 xa = L_XY + ch * XY_CH + (32 + 16 * rb + (lane & 15)) * XY_STR + g4 * 16;
    const u32 ya = L_XY + ch * XY_CH + (16 * cb + (lane & 15)) * XY_STR + g4 * 16;
    u32x4 fa[8], fb[8];
#pragma unroll
    for (int ks = 0; ks < 8; ks++) { fa[ks] = *lds<u32x4>(xa + ks * 64); fb[ks] = *lds<u32x4>(ya + ks * 64); }
    SCHED();
#pragma unroll
    for (int i = 4; i < 10; i++) w1_one(i, L_W0 + i * 8192);
    SCHED();
    const u32 j = 16 * cb + (lane & 15);
    u32 i0, is, i2;
    if (rb == 0) {
      i0 = g4 < 2 ? 4 * g4 + 8 * j : (g4 == 2 ? 256 + 4 * j : 384 + 2 * j); is = 1; i2 = g4 == 3 ? 64 : 2;
    } else {
      i0 = g4 == 0 ? 512 + j : 640 + j; is = g4 == 0 ? 32 : 0; i2 = 2 * is;
    }
    f32x4 a0 = {0, 0, 0, 0}, a1 = {0, 0, 0, 0};
#pragma unroll
    for (int ks = 0; ks < 8; ks += 2) { a0 = mfma16h(fa[ks], fb[ks], a0); a1 = mfma16h(fa[ks + 1], fb[ks + 1], a1); }
    a0 += a1;
    const u32 vb = L_VT + ch * VT_CH;
    if (rb == 0 || g4 == 0) {
#pragma unroll
      for (int r = 0; r < 4; r++) *lds<_Float16>(vb + 2 * (i0 + (r & 1) * is + (r >> 1) * i2)) = (_Float16)a0[r];
    } else if (g4 == 1) {
      *lds<_Float16>(vb + 2 * i0) = (_Float16)a0[0];
    }
  }
  STAMP(4);
  asm volatile("s_waitcnt vmcnt(0)" : "+v"(ex[0]), "+v"(ex[1]), "+v"(junk));
#pragma unroll 1
  for (int spin = 0; spin < SPIN_CAP; spin++) {
    if (__all(ex[0][1] == tag && ex[0][3] == tag && ex[1][1] == tag && ex[1][3] == tag)) break;
    __builtin_amdgcn_s_sleep(1);
    asm volatile("global_load_dwordx4 %0, %2, %3 sc1\n\tglobal_load_dwordx4 %1, %2, %4 sc1\n\ts_waitcnt vmcnt(0)"
                 : "=&v"(ex[0]), "=&v"(ex[1]) : "v"(exo), "s"(exb), "s"(exb + 16384) : "memory");
  }
  BAR();
  STAMP(5);
  if (tid == 0) __hip_atomic_store((GAS u32 *)(ws + WS_FLAG1) + b, tag, __ATOMIC_RELAXED, __HIP_MEMORY_SCOPE_AGENT);
  {
    u32 wr = L_W0 + w1_rd;
    PIN(wr);
#pragma unroll
    for (int ks = 0; ks < 10; ks++) { const u32 a = (ks < 8 ? wr : wr + 65536) + (ks & 7) * 8192; W[ks] = cat(tr_read(a), tr_read(a + 4096)); }
    SCHED();
  }
  {
    const u32 ab = L_UR + 3 * UR_STR + (tid & 1) * UR_CH + (2044 - 1024 - 8 * (tid >> 2) - 2 * (tid & 2)) * 2;
#pragma unroll
    for (int i = 0; i < 2; i++) { u32x2 v; v[0] = ex[i][0]; v[1] = ex[i][2]; *lds<u32x2>(ab + (1 - i) * 2048) = v; }
  }
  {
    u32x4 kv;
    _Float16 t[8];
#pragma unroll
    for (int e = 0; e < 8; e++) t[e] = *lds<_Float16>(vidx[e]);
#pragma unroll
    for (int e = 0; e < 4; e++) { h16x2 t2; t2[0] = t[2 * e]; t2[1] = t[2 * e + 1]; kv[e] = __builtin_bit_cast(u32, t2); }
    const u32 kb = L_K + (tid >> 8) * K_CH + (tid & 255) * 16;
    u32x2 lo, hi; lo[0] = kv[0]; lo[1] = kv[1]; hi[0] = kv[2]; hi[1] = kv[3];
    *lds<u32x2>(kb + 120) = lo; *lds<u32x2>(kb + 128) = hi;
    *lds<u32x4>(kb + 4224 + 112) = kv;
    const u32x2 z2 = {0, 0};
    u32 za;
    if (tid < 64) {
      const u32 c = tid >> 5, i = tid & 31;
      za = L_K + c * K_CH + (i < 15 ? i * 8 : 4224 + (i < 29 ? i - 15 : 0) * 8);
    } else {
      const u32 i = tid < 448 ? tid - 64 : 0, c = i / 48;
      za = L_UR + c * UR_STR + 4096 + (i - c * 48) * 8;
    }
    *lds<u32x2>(za) = z2;
  }
  BAR();
  {
    const u32 ub = L_UR + (tid >> 8) * UR_CH + (tid & 255) * 16;
    const u32x4 a = *lds<u32x4>(ub + 3 * UR_STR);
    const u32x2 c = *lds<u32x2>(ub + 3 * UR_STR + 16);
    SCHED();
#pragma unroll
    for (int i = 0; i < 4; i++) w1_one(10 + i, L_W0 + i * 8192);
    SCHED();
    u32x4 o;
    o[0] = __builtin_amdgcn_alignbit(a[1], a[0], 16); o[1] = __builtin_amdgcn_alignbit(a[2], a[1], 16);
    o[2] = __builtin_amdgcn_alignbit(a[3], a[2], 16); o[3] = __builtin_amdgcn_alignbit(c[0], a[3], 16);
    *lds<u32x4>(ub + 2 * UR_STR) = o;
    u32x4 o1; o1[0] = a[1]; o1[1] = a[2]; o1[2] = a[3]; o1[3] = c[0];
    *lds<u32x4>(ub + UR_STR) = o1;
    u32x4 o0; o0[0] = o[1]; o0[1] = o[2]; o0[2] = o[3]; o0[3] = __builtin_amdgcn_alignbit(c[1], c[0], 16);
    *lds<u32x4>(ub) = o0;
  }
#pragma unroll
  for (int i = 4; i < 8; i++) w1_one(10 + i, L_W0 + i * 8192);
  BAR();
  STAMP(6);
  u32 f1;
  const u32 xp = 255 - 32 * w - (lane >> 1);
  const u32 pg = w & 1, cg = (w >> 1) & 1;
  f32x4 ya[4];
#pragma unroll
  for (int i = 0; i < 4; i++) ya[i] = f32x4{0, 0, 0, 0};
  {
    const u32 c16 = lane & 15;
    const u32 ka = L_K + ch * K_CH + (c16 & 1) * 4224 + (c16 >> 1) * 16 + g4 * 16 + cg * 4 * 64;
    const u32 ua = L_UR + ch * UR_CH + (c16 & 3) * UR_STR + (1984 - 1792 - 64 * (c16 >> 2) + 8 * g4) * 2 + cg * 4 * 64;
    u32x4 kf[2][8], uf[2][8];
    auto rd1 = [&](int s, int j) {
      if (j >= 1 && j <= 8) uf[s & 1][8 - j] = *lds<u32x4>(ua + (j - 1) * 512 + s * 64);
      else kf[s & 1][j ? j - 8 : 0] = *lds<u32x4>(ka + (j ? j - 8 : 0) * 512 + s * 64);
    };
    auto rd = [&](int s, int i) { rd1(s, 2 * i); rd1(s, 2 * i + 1); };
    u32 wr = L_W0 + w1_rd;
    PIN(wr);
    auto side = [&](int s, int i) {
      const int n = s * 8 + i;
      if (n == 5) asm volatile("global_load_dword %0, %1, %2 sc1" : "=v"(f1) : "v"(xp * 4), "s"(ws + WS_FLAG1));
      if (n == 1 || n == 3) w1_one(26 + n / 3, L_W0 + (8 + n / 3) * 8192);
      if (n == 6) { WAIT_VM(3); RAWBAR(); }
      if (n >= 6 && n < 10) {
#pragma unroll
        for (int k = 2 * n - 12; k < 2 * n - 10; k++) W[10 + k] = cat(tr_read(wr + k * 8192), tr_read(wr + k * 8192 + 4096));
      }
      if (n == 11) {
        asm volatile("s_waitcnt vmcnt(0)" : "+v"(f1));
        if (!__all(f1 == tag)) poll(WS_FLAG1 + xp * 4);
        glds_sc1(xp * 128 + (lane & 1) * 16, ws + WS_XT + (size_t)(cp >> 2) * 32768 + (cp & 3) * 32, L_XS + w * 1024);
      }
      if (n == 13) BAR();
      if (n >= 13 && n < 29 && (n & 1)) w1_one(18 + (n - 13) / 2, L_W0 + (n - 13) / 2 * 8192);
    };
#pragma unroll
    for (int i = 0; i < 8; i++) rd(0, i);
    if (pg == 0) {
#pragma unroll
      for (int s = 0; s < 4; s++) {
#pragma unroll
        for (int i = 0; i < 8; i++) {
          if (s < 3) rd(s + 1, i);
          side(s, i);
          ya[3] = mfma16h(uf[s & 1][7 - i], kf[s & 1][i], ya[3]);
          if (i <= 4) ya[2] = mfma16h(uf[s & 1][4 - i], kf[s & 1][i], ya[2]);
          if (i <= 3) ya[1] = mfma16h(uf[s & 1][3 - i], kf[s & 1][i], ya[1]);
          if (i <= 0) ya[0] = mfma16h(uf[s & 1][0 - i], kf[s & 1][i], ya[0]);
          SCHED();
        }
      }
    } else {
#pragma unroll
      for (int s = 0; s < 4; s++) {
#pragma unroll
        for (int i = 0; i < 8; i++) {
          if (s < 3) rd(s + 1, i);
          side(s, i);
          if (i <= 6) ya[3] = mfma16h(uf[s & 1][6 - i], kf[s & 1][i], ya[3]);
          if (i <= 5) ya[2] = mfma16h(uf[s & 1][5 - i], kf[s & 1][i], ya[2]);
          if (i <= 2) ya[1] = mfma16h(uf[s & 1][2 - i], kf[s & 1][i], ya[1]);
          if (i <= 1) ya[0] = mfma16h(uf[s & 1][1 - i], kf[s & 1][i], ya[0]);
          SCHED();
        }
      }
    }
  }
  WAIT_VM(8);
  {
    const u32 yb = L_YP + w * 2048 + lane * 16;
    *lds<f32x4>(yb) = cg ? ya[0] : ya[2];
    *lds<f32x4>(yb + 1024) = cg ? ya[1] : ya[3];
  }
  BAR();
  STAMP(7);
  w1_one(28, L_W2); w1_one(29, L_W2 + 8192);
  {
    const u32 yb = L_YP + (w ^ 2) * 2048 + lane * 16;
    const float ksc = *lds<float>(L_MISC + ch * 4), dsk = ch ? bf_hi(dsk2) : bf_lo(dsk2);
    __amdgpu_buffer_rsrc_t rs = __builtin_amdgcn_make_buffer_rsrc(ws + WS_HT, 0, SEQ * DM * 2, 0x00020000);
    f32x4 o[2]; u32x2 uu[2], xx[2]; u32 t0[2];
#pragma unroll
    for (int i = 0; i < 2; i++) {
      o[i] = *lds<f32x4>(yb + i * 1024);
      t0[i] = 256 * (4 * cg + (i ? 3 - pg : pg)) + 4 * lane;
      uu[i] = *lds<u32x2>(L_UR + ch * UR_CH + 3 * UR_STR + (2044 - t0[i]) * 2);
      xx[i] = *lds<u32x2>(L_XS + ((2044 - t0[i]) >> 3) * 32 + ch * 16 + ((2044 - t0[i]) & 7) * 2);
    }
#pragma unroll
    for (int i = 0; i < 2; i++) {
      const f32x4 own = cg ? (i ? ya[3] : ya[2]) : (i ? ya[1] : ya[0]);
      const u32 uu0 = uu[i][0], uu1 = uu[i][1];
      const h16x2 ua = __builtin_bit_cast(h16x2, uu1), ub = __builtin_bit_cast(h16x2, uu0);
      const float uv[4] = {(float)ua[1], (float)ua[0], (float)ub[1], (float)ub[0]};
      const float xv[4] = {bf_hi(xx[i][1]), bf_lo(xx[i][1]), bf_hi(xx[i][0]), bf_lo(xx[i][0])};
      float hv[4];
#pragma unroll
      for (int r = 0; r < 4; r += 2) {
        const u32 y2 = pk_bf16((o[i][r] + own[r]) * ksc, (o[i][r + 1] + own[r + 1]) * ksc);
        const u32 d2 = pk_bf16(uv[r] * dsk, uv[r + 1] * dsk);
        const u32 z2 = pk_bf16(bf_lo(y2) + bf_lo(d2), bf_hi(y2) + bf_hi(d2));
        hv[r] = xv[r] + bf_lo(z2); hv[r + 1] = xv[r + 1] + bf_hi(z2);
      }
      u32x2 hv2; hv2[0] = pk_bf16(hv[0], hv[1]); hv2[1] = pk_bf16(hv[2], hv[3]);
      const u32 off = ((t0[i] >> 5) * DM + d0 + ch) * 64 + (t0[i] & 31) * 2;
      __builtin_amdgcn_raw_buffer_store_b64(hv2, rs, off, 0, 16);
    }
  }
  SCHED();
  if (b == 0 && tid == 0)
    __hip_atomic_store((GAS u32 *)(ws + WS_EPOCH), tag, __ATOMIC_RELAXED, __HIP_MEMORY_SCOPE_AGENT);
  BAR();
  f32x16 cs;
#pragma unroll
  for (int i = 0; i < 16; i++) cs[i] = 0;
  const u32x4 ones = {0x3f803f80u, 0x3f803f80u, 0x3f803f80u, 0x3f803f80u};
  w1_one(30, L_YP); w1_one(31, L_YP + 8192);
#pragma unroll
  for (int ks = 0; ks < 10; ks++) cs = mfma32(W[ks], ones, cs);
  SCHED();
  WAIT_VM(2);
  RAWBAR();
  if (tid == 0) __hip_atomic_store((GAS u32 *)(ws + WS_FLAG2) + b * 32, tag, __ATOMIC_RELAXED, __HIP_MEMORY_SCOPE_AGENT);
  STAMP(8);
  const u32 t32 = lane & 31;
  const u32 nt = jj & 7, tt = jj >> 3, pn2 = 2 * xcd + (tt >> 1), tgA = tt & 1, tgB = 3 - tgA;
  const u32 lv = lane * 16, rot2 = nt == 7 ? 0 : nt;
  auto ft_of = [&](int i) { const u32 k = (i % 7) + rot2; return 2 * (k >= 7 ? k - 7 : k) + (i < 7 ? tgA ^ 1 : tgA); };
  const u32 d_voff = (lane >> 3) * (DM * 2) + (((lane & 7) ^ (((lane >> 4) & 1) << 2)) * 16);
  const char *const wd_base = (const char *)p.wd + (size_t)(w * 16) * (DM * 2) + nt * 128;
  auto wd_one = [&](int u) {
    const u32 k = __builtin_amdgcn_readfirstlane((ft_of(u >> 1) << 17) + ((u & 1) << 13));
    glds(d_voff, wd_base + k, wd_slot(u) * 8192 + w * 1024);
  };
  u32x4 D[28];
  u32 d_rd[2][3];
#pragma unroll
  for (int h = 0; h < 2; h++) {
    d_rd[h][0] = w * 1024 + (((4 * (g4 >> 1) + q4) * 128 + h * 64 + (g4 & 1) * 32 + p4 * 8) ^ (((q4 >> 1) & 1) * 64));
    d_rd[h][1] = d_rd[h][0] + 65536; d_rd[h][2] = d_rd[h][0] + 131072;
  }
  auto wd_take = [&](int j, int h) {
    const u32 o = wd_slot(2 * j) * 8192;
    D[2 * j + h] = cat(tr_read(d_rd[h][o >> 16] + (o & 65535)), tr_read(d_rd[h][o >> 16] + (o & 65535) + 8192));
  };
  auto lane_again = []() {
    u32 l; asm volatile("v_mbcnt_lo_u32_b32 %0, -1, 0\n\tv_mbcnt_hi_u32_b32 %0, -1, %0" : "=v"(l)); return l;
  };
  auto foA_of = [&]() { return WS_FLAG3 + ((pn2 * 4 + tgA) * 16 + min(lane_again(), 13u)) * 128; };
  __amdgpu_buffer_rsrc_t prs = __builtin_amdgcn_make_buffer_rsrc(ws + WS_P, 0, SEQ * FF * 2, 0x00020000);
  __amdgpu_buffer_rsrc_t frs = __builtin_amdgcn_make_buffer_rsrc(ws + WS_FLAG3, 0, 131072, 0x00020000);
  const u32 OOB = 0x40000000u;
  const u32 p_off = g1 ? (panel * 4 * 112 + ft * 8 + w) * 1024 + lane * 16 : OOB;
  const u32 f_off = g1 && tid == 0 ? (panel * 64 + ft) * 128 : OOB;
  auto tg_of = [&](int g) { return g < 2 ? (u32)g : g ^ (ft & 1); };
  float e_m, e_ne, e_s2, e_g, e_u, e_e, e_o;
  u32 pko[4];
  u32x2 sg;
  const u32 sg_off = WS_ST + (panel * 128 + t32) * 8;
  auto st_take = [&](int tg) {
    PIN(sg);
#pragma unroll 1
    for (int spin = 0; spin < SPIN_CAP; spin++) {
      if (__all(sg[1] == tag)) break;
      __builtin_amdgcn_s_sleep(1);
      asm volatile("global_load_dwordx2 %0, %1, %2 sc1\n\ts_waitcnt vmcnt(0)" : "=&v"(sg) : "v"(sg_off), "s"(ws + tg_of(tg) * 256) : "memory");
    }
    e_m = bf_lo(sg[0]);
    const float sd = bf_hi(sg[0]);
    e_ne = -1.4426950408889634f * __builtin_amdgcn_rcpf(sd); e_s2 = sd * sd;
  };
  auto epi_step = [&](int ks, int tg, const f32x16 &prev) {
    if (ks == 0) st_take(tg);
    else if (ks >= 1 && ks < 25) {
      const int i = (ks - 1) / 3, ph = (ks - 1) % 3;
      if (ph == 0) {
        e_g = prev[i] - e_m * cs[i]; PIN(e_g);
        e_e = __builtin_amdgcn_exp2f(e_g * e_ne);
      } else if (ph == 1) {
        e_u = (prev[i + 8] - e_m * cs[i + 8]) * e_g;
        e_e = e_e * e_s2 + e_s2;
        PIN(e_u); PIN(e_e);
      } else {
        const float o = e_u * __builtin_amdgcn_rcpf(e_e);
        if (i & 1) { pko[i >> 1] = pk_bf16(e_o, o); PIN(pko[i >> 1]); } else { e_o = o; PIN(e_o); }
      }
    } else if (ks == 25) {
      u32x4 v; v[0] = pko[0]; v[1] = pko[1]; v[2] = pko[2]; v[3] = pko[3];
      __builtin_amdgcn_raw_buffer_store_b128(v, prs, p_off + tg_of(tg) * (112 * 1024), 0, 16);
    }
  };
  const char *const pbA = ws + WS_P + ((size_t)(pn2 * 4 + tgA) * 112 + w) * 1024;
  const char *const pbB = ws + WS_P + ((size_t)(pn2 * 4 + tgB) * 112 + w) * 1024;
  u32x4 PF[14], QF[14];
  auto p_one = [&](const char *pb, int i, u32x4 &d) {
    const u32 j = __builtin_amdgcn_readfirstlane(ft_of(i) << 13);
    asm volatile("global_load_dwordx4 %0, %1, %2 sc1" : "=v"(d) : "v"(lv), "s"(pb + j));
  };
  f32x16 acc[2];
  if (g1) {
    {
      u32 wr = L_W0 + w1_rd;
      PIN(wr);
#pragma unroll
      for (int ks = 0; ks < 8; ks++) W[18 + ks] = cat(tr_read(wr + ks * 8192), tr_read(wr + ks * 8192 + 4096));
      wr += 65536;
#pragma unroll
      for (int ks = 0; ks < 2; ks++) W[26 + ks] = cat(tr_read(wr + ks * 8192), tr_read(wr + ks * 8192 + 4096));
      wr = L_W2 + w1_rd;
      PIN(wr);
#pragma unroll
      for (int ks = 0; ks < 2; ks++) W[28 + ks] = cat(tr_read(wr + ks * 8192), tr_read(wr + ks * 8192 + 4096));
#pragma unroll
      for (int ks = 10; ks < 18; ks++) cs = mfma32(W[ks], ones, cs);
      SCHED();
      if (w == 0) WAIT_VM(1); else WAIT_VM(0);
      RAWBAR();
#pragma unroll
      for (int ks = 2; ks < 4; ks++) W[28 + ks] = cat(tr_read(wr + ks * 8192), tr_read(wr + ks * 8192 + 4096));
    }
    STAMP(9);
    poll(WS_FLAG2 + (w * 32 + t32) * 128);
    BAR();
    STAMP(10);
    const char *const a_base = ws + WS_HT + (size_t)panel * 131072;
    auto a_one = [&](int i) { glds_sc1(lv, a_base + tg_of(i >> 2) * 32768 + (((i & 3) * 8 + w + rot1) & 31) * 1024, L_A + w * 1024 + i * 8192); };
#pragma unroll
    for (int i = 0; i < 4; i++) a_one(i);
#pragma unroll
    for (int ks = 18; ks < 32; ks++) { cs = mfma32(W[ks], ones, cs); if (ks % 3 == 0 && ks < 30) { a_one(ks / 3 - 2); SCHED(); } }
    SCHED();
    u32 ab = L_A + (4 * (g4 >> 1) + q4) * 64 + (g4 & 1) * 32 + p4 * 8;
    u32x4 F[LOOK + 1];
    auto frag = [&](u32 a) { return cat(tr_read(a), tr_read(a + 512)); };
    u32 fa;
    wait_vm<g1_younger(-8, 0)>();
    RAWBAR();
    u32 ab2 = ab + 65536;
    PIN(ab); PIN(ab2);
#pragma unroll
    for (int i = 0; i < LOOK; i++) F[i] = frag(ab + i * 1024);
    static_for<0, 128>([&](auto P_) {
      constexpr int P = decltype(P_)::v, tg = P >> 5, ks = P & 31;
      if constexpr (ks == 0) {
#pragma unroll
        for (int i = 0; i < 16; i++) acc[tg & 1][i] = 0;
      }
      if constexpr (P == 8 - LOOK) { wait_vm<g1_younger(-7, P)>(); RAWBAR(); }
      if constexpr (P == 16 - LOOK) { wait_vm<g1_younger(-6, P)>(); RAWBAR(); }
      if constexpr (P == 24 - LOOK) { wait_vm<g1_younger(-5, P)>(); RAWBAR(); }
      if constexpr (P == 32 - LOOK) { wait_vm<g1_younger(-1, P)>(); RAWBAR(); }
      if constexpr (P == 48) { wait_vm<g1_younger(14, P)>(); RAWBAR(); }
      if constexpr (P == 64) { wait_vm<g1_younger(30, P)>(); RAWBAR(); }
      if constexpr (P == 66) wait_vm<g1_younger(54, P)>();
      if constexpr (P == 96) { wait_vm<g1_younger(85, P)>(); RAWBAR(); }
      if constexpr (P == 101) { wait_vm<g1_younger(90, P)>(); RAWBAR(); }
      if constexpr (g1_find(23, 2, P) >= 0) __builtin_amdgcn_raw_buffer_store_b32(tag, frs, f_off, 2048 * g1_find(23, 2, P), 16);
      if constexpr (P == G1_OPS[25]) asm volatile("global_load_dword %0, %1, %2 sc1" : "=v"(fa) : "v"(foA_of()), "s"(ws));
      if constexpr (P == 121) {
        wait_vm<g1_younger(115, P)>();
        PIN(fa);
        if (!__all(fa == tag)) poll(foA_of());
      }
      if constexpr (g1_find(54, 6, P) >= 0) p_one(pbA, g1_find(54, 6, P), PF[g1_find(54, 6, P)]);
      if constexpr (g1_find(8, 8, P) >= 0) a_one(8 + g1_find(8, 8, P));
      if constexpr (g1_find(16, 4, P) >= 0)
        asm volatile("global_load_dwordx2 %0, %1, %2 sc1" : "=v"(sg) : "v"(sg_off), "s"(ws + tg_of(g1_find(16, 4, P)) * 256));
      if constexpr (g1_find(26, 28, P) >= 0) wd_one(g1_find(26, 28, P));
      if constexpr (g1_take(P) >= 0) wd_take(g1_take(P) >> 1, g1_take(P) & 1);
      constexpr int n = P + LOOK;
      if constexpr (n < 128) F[n % (LOOK + 1)] = frag((n < 64 ? ab : ab2) + (n & 63) * 1024);
      acc[tg & 1] = mfma32(W[ks], F[P % (LOOK + 1)], acc[tg & 1]);
      if constexpr (tg > 0 && ks == epi0(tg)) wait_vm<g1_younger(G1_OPS[16 + tg - 1], P + 1)>();
      if constexpr (tg > 0 && ks >= epi0(tg)) epi_step(ks - epi0(tg), tg - 1, acc[(tg - 1) & 1]);
      SCHED();
      if constexpr (P == 31) STAMP(11);
      if constexpr (P == 63) STAMP(19);
      if constexpr (P == 95) STAMP(20);
      if constexpr (P == 127) STAMP(3);
    });
    wait_vm<g1_younger(119, 128)>();
    st_take(3);
    STAMP(12);
  } else {
    poll(WS_FLAG2 + (w * 32 + t32) * 128);
    RAWBAR();
    const u32 tgs = (2 * xcd + ((jj >> 1) & 1)) * 4 + (jj & 1);
    const char *base = ws + WS_HT + (size_t)tgs * 32768 + w * 4096;
#pragma unroll
    for (int i = 0; i < 8; i++) glds_sc1(lv, base + (i >> 2) * 65536 + (i & 3) * 1024, 65536 + (i >> 2) * 32768 + w * 4096 + (i & 3) * 1024);
    const u32 ab = 65536 + w * 4096 + (4 * (g4 >> 1) + q4) * 64 + (g4 & 1) * 32 + p4 * 8;
#pragma unroll
    for (int g = 0; g < 2; g++) {
      if (g == 0) WAIT_VM(4); else WAIT_VM(0);
      float s_s = 0, s_q = 0;
#pragma unroll
      for (int i = 0; i < 4; i++) {
        const u32x4 f = cat(tr_read(ab + g * 32768 + i * 1024), tr_read(ab + g * 32768 + i * 1024 + 512));
#pragma unroll
        for (int k = 0; k < 4; k++) { s_s = dot2bf(f[k], 0x3f803f80u, s_s); s_q = dot2bf(f[k], f[k], s_q); }
      }
      const auto sw = __builtin_amdgcn_permlane32_swap(__float_as_uint(s_s), __float_as_uint(s_q), false, false);
      *lds<float>(g * 2048 + w * 256 + lane * 4) = __uint_as_float(sw[0]) + __uint_as_float(sw[1]);
      BAR();
      float sp = 0;
#pragma unroll
      for (int i = 0; i < 8; i++) sp += *lds<float>(g * 2048 + i * 256 + lane * 4);
      const auto s2 = __builtin_amdgcn_permlane32_swap(__float_as_uint(sp), __float_as_uint(sp), false, false);
      const float mean = __uint_as_float(s2[0]) * (1.f / DM);
      const float var = rbf(fmaxf(__uint_as_float(s2[1]) * (1.f / DM) - mean * mean, 0.f));
      const u64 gr = ((u64)tag << 32) | pk_bf16(mean, __builtin_amdgcn_sqrtf(rbf(var + 1e-5f)));
      if (tid < 32) __hip_atomic_store((GAS u64 *)(ws + WS_ST) + (tgs + 2 * g) * 32 + lane, gr, __ATOMIC_RELAXED, __HIP_MEMORY_SCOPE_AGENT);
    }
    BAR();
#pragma unroll
    for (int u = 0; u < 12; u++) wd_one(u);
    WAIT_VM(0);
#pragma unroll
    for (int j = 0; j < 6; j++) { wd_take(j, 0); wd_take(j, 1); }
    WAIT_LGKM0();
#pragma unroll
    for (int u = 12; u < 28; u++) wd_one(u);
    poll(foA_of());
#pragma unroll
    for (int j = 6; j < 9; j++) { wd_take(j, 0); wd_take(j, 1); }
#pragma unroll
    for (int i = 0; i < 6; i++) p_one(pbA, i, PF[i]);
    asm volatile("s_waitcnt vmcnt(0)" : "+v"(PF[0]), "+v"(PF[1]), "+v"(PF[2]), "+v"(PF[3]), "+v"(PF[4]), "+v"(PF[5]));
#pragma unroll
    for (int i = 0; i < 16; i++) acc[1][i] = 0;
    e_m = 0; e_ne = 0; e_s2 = 1.f;
  }

  {
    const u32 ln = lane_again(), hh = ln >> 5, tl = ln & 31;
    const u32 foB = WS_FLAG3 + ((pn2 * 4 + tgB) * 16 + 2 * min(ln, 6u)) * 128;
    p_one(pbA, 6, PF[6]); p_one(pbA, 7, PF[7]);
    SCHED();
    {
      const f32x2 m2 = {e_m, e_m}, ne2 = {e_ne, e_ne}, s22 = {e_s2, e_s2};
      u32x4 v;
#pragma unroll
      for (int i = 0; i < 8; i += 2) {
        const f32x2 g = f32x2{acc[1][i], acc[1][i + 1]} - m2 * f32x2{cs[i], cs[i + 1]};
        const f32x2 u = (f32x2{acc[1][i + 8], acc[1][i + 9]} - m2 * f32x2{cs[i + 8], cs[i + 9]}) * g;
        f32x2 e = g * ne2;
        e[0] = __builtin_amdgcn_exp2f(e[0]); e[1] = __builtin_amdgcn_exp2f(e[1]);
        e = e * s22 + s22;
        e[0] = __builtin_amdgcn_rcpf(e[0]); e[1] = __builtin_amdgcn_rcpf(e[1]);
        const f32x2 o = u * e;
        v[i >> 1] = pk_bf16(o[0], o[1]);
      }
      __builtin_amdgcn_raw_buffer_store_b128(v, prs, p_off + tg_of(3) * (112 * 1024), 0, 16);
    }
    SCHED();
    STAMP(13);
    wait_vm<8>();
    RAWBAR();
    __builtin_amdgcn_raw_buffer_store_b32(tag, frs, f_off, tg_of(2) * 2048, 16);
    const u32 own_n = nt * 64 + w * 8;
    u32 res[8];
    const u32 roA = WS_HT + ((pn2 * 4 + tgA) * DM + own_n + 4 * hh) * 64 + tl * 2;
    const u32 roB = WS_HT + ((pn2 * 4 + tgB) * DM + own_n + 4 * hh) * 64 + tl * 2;
    f32x16 ac[2];
    f32x4 r[2];
    f32x16 bc[2];
    u32 rw = w * 1024 + lv;
    PIN(rw);
    auto red_put = [&](int t, const f32x16 &a, int o) {
      f32x4 v; v[0] = a[4 * (o & 3)]; v[1] = a[4 * (o & 3) + 1]; v[2] = a[4 * (o & 3) + 2]; v[3] = a[4 * (o & 3) + 3];
      *lds<f32x4>((t ? rw + 65536 : rw) + o * 8192) = v;
    };
    auto red_get = [&](int t) {
      f32x4 v[8];
#pragma unroll
      for (int i = 0; i < 8; i++) v[i] = *lds<f32x4>(t * 65536 + w * 8192 + i * 1024 + lv);
      r[t] = ((v[0] + v[1]) + (v[2] + v[3])) + ((v[4] + v[5]) + (v[6] + v[7]));
    };
#pragma unroll
    for (int i = 0; i < 16; i++) { ac[0][i] = 0; ac[1][i] = 0; }
    u32 fb;
    static_for<0, 14>([&](auto I_) {
      constexpr int i = decltype(I_)::v;
      constexpr int YOUNGER[7] = {0, 12, 13, 12, 12, 11, 10};
      if constexpr (i < 3) { p_one(pbA, 8 + 2 * i, PF[8 + 2 * i]); p_one(pbA, 9 + 2 * i, PF[9 + 2 * i]); }
      if constexpr (i == 4) asm volatile("global_load_dword %0, %1, %2 sc1" : "=v"(fb) : "v"(foB + (tgA ^ 1) * 128), "s"(ws));
      if constexpr (i >= 1 && i < 7) wait_vm<YOUNGER[i < 7 ? i : 0]>();
      if constexpr (i == 7) {
        if (w == 0) wait_vm<8>();
        else {
          wait_vm<0>();
          u32 fc;
          asm volatile("v_mov_b32 %0, %1" : "=v"(fc) : "v"(fb));
          if (!__all(fc == tag)) poll(foB + (tgA ^ 1) * 128);
        }
        BAR();
        __builtin_amdgcn_raw_buffer_store_b32(tag, frs, f_off, tg_of(3) * 2048, 16);
        STAMP(14);
#pragma unroll
        for (int k = 0; k < 7; k++) p_one(pbB, k, QF[k]);
        STAMP(15);
      }
      if constexpr (i >= 8) wait_vm<22 - i>();
      if constexpr (i < 5) { wd_take(9 + i, 0); wd_take(9 + i, 1); }
      PIN(PF[i]);
      ac[0] = mfma32(D[2 * i], PF[i], ac[0]);
      ac[1] = mfma32(D[2 * i + 1], PF[i], ac[1]);
      SCHED();
    });
#define RES_LD(i, ro, o) asm volatile("global_load_ushort %0, %1, %2 offset:" #o " sc1" : "=v"(res[i]) : "v"(ro), "s"(ws));
    RES_LD(0, roA, 0) RES_LD(1, roA, 64) RES_LD(2, roA, 128) RES_LD(3, roA, 192)
    RES_LD(4, roB, 0) RES_LD(5, roB, 64) RES_LD(6, roB, 128) RES_LD(7, roB, 192)
#undef RES_LD
    STAMP(16);
#pragma unroll
    for (int i = 0; i < 16; i++) { bc[0][i] = 0; bc[1][i] = 0; }
    SCHED();
    static_for<0, 14>([&](auto I_) {
      constexpr int i = decltype(I_)::v;
      if constexpr (i == BP) {
        poll(foB + tgA * 128);
        asm volatile("" : "+v"(res[0]), "+v"(res[1]), "+v"(res[2]), "+v"(res[3]), "+v"(res[4]), "+v"(res[5]), "+v"(res[6]), "+v"(res[7]), "+v"(fb));
        STAMP(21);
#pragma unroll
        for (int k = 7; k < 14; k++) p_one(pbB, k, QF[k]);
      }
      if constexpr (i == 7) { BAR(); red_get(0); }
      if constexpr (i < BP || i >= 7) wait_vm<i < 7 ? 14 - i : 13 - i>();
      PIN(QF[i]);
      bc[0] = mfma32(D[2 * i], QF[i], bc[0]);
      if constexpr (i < 4) red_put(0, ac[i >> 1], 2 * i);
      SCHED();
      if constexpr (i < 8) bc[1] = mfma32(D[2 * i + 1], QF[i], bc[1]);
      if constexpr (i < 4) red_put(0, ac[i >> 1], 2 * i + 1);
      SCHED();
    });
#pragma unroll
    for (int i = 8; i < 14; i++) {
      bc[1] = mfma32(D[2 * i + 1], QF[i], bc[1]);
      if (i >= 9 && i < 13) red_put(1, bc[0], i - 9);
      SCHED();
    }
    STAMP(17);
#pragma unroll
    for (int o = 4; o < 8; o++) red_put(1, bc[1], o);
    BAR();
    red_get(1);
    u32 o4[4];
    {
      const u32 y0 = pk_bf16(r[0][0], r[0][1]), y1 = pk_bf16(r[0][2], r[0][3]), y2 = pk_bf16(r[1][0], r[1][1]), y3 = pk_bf16(r[1][2], r[1][3]);
      o4[0] = pk_bf16(bf_lo(res[0]) + bf_lo(y0), bf_lo(res[1]) + bf_hi(y0));
      o4[1] = pk_bf16(bf_lo(res[2]) + bf_lo(y1), bf_lo(res[3]) + bf_hi(y1));
      o4[2] = pk_bf16(bf_lo(res[4]) + bf_lo(y2), bf_lo(res[5]) + bf_hi(y2));
      o4[3] = pk_bf16(bf_lo(res[6]) + bf_lo(y3), bf_lo(res[7]) + bf_hi(y3));
    }
    const auto x0 = __builtin_amdgcn_permlane32_swap(o4[0], o4[2], false, false);
    const auto x1 = __builtin_amdgcn_permlane32_swap(o4[1], o4[3], false, false);
    u32x4 v; v[0] = x0[0]; v[1] = x1[0]; v[2] = x0[1]; v[3] = x1[1];
    *(u32x4 *)(p.out + (size_t)((pn2 * 4 + (hh ? tgB : tgA)) * 32 + tl) * DM + own_n) = v;
    STAMP(18);
  }
  asm volatile(".Lcode_end:");
}

extern "C" void kernel_launch(void *const *d_in, const int *in_sizes, int n_in, void *d_out, int out_size, void *d_ws,
                              size_t ws_size, hipStream_t stream) {
  Params p;
  p.x = (const u16 *)d_in[0]; p.logA = (const u16 *)d_in[1]; p.Aim = (const u16 *)d_in[2]; p.Cr = (const u16 *)d_in[3];
  p.Ci = (const u16 *)d_in[4]; p.logdt = (const u16 *)d_in[5]; p.Dsk = (const u16 *)d_in[6];
  p.wg = (const u16 *)d_in[8]; p.wu = (const u16 *)d_in[9]; p.wd = (const u16 *)d_in[10];
  p.out = (u16 *)d_out; p.ws = (char *)d_ws;
  (void)hipFuncSetAttribute((const void *)fused, hipFuncAttributeMaxDynamicSharedMemorySize, LDS_BYTES);
  fused<<<256, 512, LDS_BYTES, stream>>>(p);
}
```

```cpp
#include <hip/hip_runtime.h>
#include <hip/hip_bf16.h>
#include <stdint.h>

typedef unsigned short u16;
typedef unsigned int u32;
typedef unsigned long long u64;
typedef float f32x2 __attribute__((ext_vector_type(2)));
typedef float f32x4 __attribute__((ext_vector_type(4)));
typedef float f32x16 __attribute__((ext_vector_type(16)));
typedef _Float16 h16x8 __attribute__((ext_vector_type(8)));
typedef _Float16 h16x2 __attribute__((ext_vector_type(2)));
typedef __bf16 b16x8 __attribute__((ext_vector_type(8)));
typedef __bf16 b16x2 __attribute__((ext_vector_type(2)));
typedef short s16x4 __attribute__((ext_vector_type(4)));
typedef u32 u32x2 __attribute__((ext_vector_type(2)));
typedef u32 u32x4 __attribute__((ext_vector_type(4)));
#define LAS __attribute__((address_space(3)))
#define GAS __attribute__((address_space(1)))

constexpr int SEQ = 2048, DM = 512, NMODE = 128, FF = 1792;

constexpr u32 WS_EPOCH = 0, WS_ST = 4096, WS_FLAG2 = 32768, WS_FLAG3 = 65536, WS_FLAG1 = 196608;
constexpr u32 WS_HT = 1u << 20, WS_P = 4u << 20, WS_EX = 12u << 20, WS_XT = 18u << 20;

constexpr u32 LDS_BYTES = 163840;
constexpr u32 XY_STR = 544, XY_CH = 53 * XY_STR;
constexpr u32 L_XY = 0;
constexpr u32 UR_STR = 4640, UR_CH = 4 * UR_STR;
constexpr u32 L_UR = 0;
constexpr u32 L_XS = 37120;
constexpr u32 K_CH = 8448;
constexpr u32 L_K = 45312;
constexpr u32 VT_CH = 1344;
constexpr u32 L_VT = 62464;
constexpr u32 L_MISC = 9216;
constexpr u32 L_YP = 65536;
constexpr u32 T_STR = 1056, L_T = 57664, L_TX = L_T + 8 * T_STR;
constexpr u32 L_W0 = 81920;
constexpr u32 L_W2 = 49152;
constexpr u32 L_A = 0;
constexpr int SPIN_CAP = 1 << 17;

struct Params {
  const u16 *x, *logA, *Aim, *Cr, *Ci, *logdt, *Dsk, *wg, *wu, *wd;
  u16 *out;
  char *ws;
};

__device__ __forceinline__ float bf_lo(u32 v) { return __uint_as_float(v << 16); }
__device__ __forceinline__ float bf_hi(u32 v) { return __uint_as_float(v & 0xffff0000u); }
__device__ __forceinline__ u32 pk_bf16(float a, float b) {
  b16x2 r; r[0] = (__bf16)a; r[1] = (__bf16)b; return __builtin_bit_cast(u32, r);
}
__device__ __forceinline__ float rbf(float a) { return bf_lo(pk_bf16(a, a)); }
__device__ __forceinline__ u32 pk_f16(float a, float b) {
  h16x2 r; r[0] = (_Float16)a; r[1] = (_Float16)b; return __builtin_bit_cast(u32, r);
}
template <typename T> __device__ __forceinline__ LAS T *lds(u32 off) { return (LAS T *)(size_t)off; }
__device__ __forceinline__ u32x2 tr_read(u32 addr) {
  return __builtin_bit_cast(u32x2, __builtin_amdgcn_ds_read_tr16_b64_v4i16((LAS s16x4 *)(size_t)addr));
}
__device__ __forceinline__ u32x4 cat(u32x2 a, u32x2 b) { u32x4 r; r[0] = a[0]; r[1] = a[1]; r[2] = b[0]; r[3] = b[1]; return r; }
__device__ __forceinline__ f32x16 mfma32(u32x4 a, u32x4 b, f32x16 c) {
  return __builtin_amdgcn_mfma_f32_32x32x16_bf16(__builtin_bit_cast(b16x8, a), __builtin_bit_cast(b16x8, b), c, 0, 0, 0);
}
__device__ __forceinline__ f32x4 mfma16h(u32x4 a, u32x4 b, f32x4 c) {
  return __builtin_amdgcn_mfma_f32_16x16x32_f16(__builtin_bit_cast(h16x8, a), __builtin_bit_cast(h16x8, b), c, 0, 0, 0);
}
__device__ __forceinline__ float dot2bf(u32 a, u32 b, float c) {
  return __builtin_amdgcn_fdot2_f32_bf16(__builtin_bit_cast(b16x2, a), __builtin_bit_cast(b16x2, b), c, false);
}
__device__ __forceinline__ void glds(u32 voff, const void *sbase, u32 ldst) {
  asm volatile("s_mov_b32 m0, %2\n\ts_nop 0\n\tglobal_load_lds_dwordx4 %0, %1" ::"v"(voff), "s"(sbase), "s"(ldst) : "memory");
}
__device__ __forceinline__ void glds_sc1(u32 voff, const void *sbase, u32 ldst) {
  asm volatile("s_mov_b32 m0, %2\n\ts_nop 0\n\tglobal_load_lds_dwordx4 %0, %1 sc1" ::"v"(voff), "s"(sbase), "s"(ldst) : "memory");
}
#define WAIT_VM(n) asm volatile("s_waitcnt vmcnt(" #n ")" ::: "memory")
#define WAIT_LGKM0() asm volatile("s_waitcnt lgkmcnt(0)" ::: "memory")
#define BAR() do { asm volatile("s_waitcnt lgkmcnt(0)\n\ts_barrier" ::: "memory"); } while (0)
#define SCHED() __builtin_amdgcn_sched_barrier(0)
#define PIN(x) asm volatile("" : "+v"(x))
#define RAWBAR() asm volatile("s_barrier" ::: "memory")
template <int N> __device__ __forceinline__ void wait_vm() { asm volatile("s_waitcnt vmcnt(%0)" ::"n"(N) : "memory"); }
template <int I> struct IC { static constexpr int v = I; };
template <int B, int E, typename Fn> __device__ __forceinline__ void static_for(Fn &&f) {
  if constexpr (B < E) { f(IC<B>{}); static_for<B + 1, E>(f); }
}
constexpr int G1_OPS[] = {-8, -7, -6, -5, -4, -3, -2, -1, 2, 6, 10, 14, 18, 22, 26, 30,
                          27, 57, 89, 119,
                          62, 90, 122,
                          97, 101, 115,
                          34, 38, 42, 46, 50, 54, 58, 63, 65, 67, 69, 71, 73, 75, 78, 80, 82, 85,
                          96, 98, 100, 102, 104, 106, 108, 110, 112, 114,
                          121, 123, 124, 125, 126, 127};
constexpr int G1_TAKE[] = {66, 69, 72, 98, 101, 104, 110, 116, 122};
constexpr int g1_take(int p) { for (int i = 0; i < 9; i++) if (G1_TAKE[i] == p || G1_TAKE[i] + 1 == p) return 2 * i + p - G1_TAKE[i]; return -1; }
constexpr int LOOK = 3;
constexpr int BP = 1;
constexpr int epi0(int tg) { return tg == 1 ? 5 : 1; }
constexpr int g1_younger(int x, int p) { int n = 0; for (int q : G1_OPS) n += (q > x && q < p); return n; }
constexpr int g1_find(int first, int cnt, int p) { for (int i = 0; i < cnt; i++) if (G1_OPS[first + i] == p) return i; return -1; }
constexpr int wd_slot(int u) { return u < 4 ? 16 + u : u < 12 ? u - 4 : u < 16 ? u + 4 : u < 18 ? u - 16 : u < 22 ? u - 10 : u - 20; }

template <int CTRL> __device__ __forceinline__ float dpp(float v) {
  return __uint_as_float(__builtin_amdgcn_update_dpp(0u, __float_as_uint(v), CTRL, 0xf, 0xf, true));
}
__device__ __forceinline__ float wave_sum(float v) {
  v += dpp<0xB1>(v); v += dpp<0x4E>(v); v += dpp<0x141>(v); v += dpp<0x140>(v);
  auto a = __builtin_amdgcn_permlane16_swap(__float_as_uint(v), __float_as_uint(v), false, false);
  v = __uint_as_float(a[0]) + __uint_as_float(a[1]);
  auto c = __builtin_amdgcn_permlane32_swap(__float_as_uint(v), __float_as_uint(v), false, false);
  return __uint_as_float(c[0]) + __uint_as_float(c[1]);
}
__device__ __forceinline__ float wave_max(float v) {
  v = fmaxf(v, dpp<0xB1>(v)); v = fmaxf(v, dpp<0x4E>(v)); v = fmaxf(v, dpp<0x141>(v)); v = fmaxf(v, dpp<0x140>(v));
  auto a = __builtin_amdgcn_permlane16_swap(__float_as_uint(v), __float_as_uint(v), false, false);
  v = fmaxf(__uint_as_float(a[0]), __uint_as_float(a[1]));
  auto c = __builtin_amdgcn_permlane32_swap(__float_as_uint(v), __float_as_uint(v), false, false);
  return fmaxf(__uint_as_float(c[0]), __uint_as_float(c[1]));
}
struct cf { float r, i; };
__device__ __forceinline__ cf cmul(cf a, cf b) { return {a.r * b.r - a.i * b.i, a.r * b.i + a.i * b.r}; }
__device__ __forceinline__ cf cexp_at(float lr, float rev, float pos) {
  float e = __builtin_amdgcn_exp2f(lr * pos), f = __builtin_amdgcn_fractf(rev * pos);
  return {e * __builtin_amdgcn_cosf(f), e * __builtin_amdgcn_sinf(f)};
}


__global__ void __launch_bounds__(512, 2) fused(Params p) {
  extern __shared__ __attribute__((aligned(1024))) char smem_[];
  const u32 tid = threadIdx.x, lane = tid & 63;
  const u32 w = __builtin_amdgcn_readfirstlane(tid >> 6);
  const u32 b = blockIdx.x, xcd = b & 7, jj = b >> 3;
  u32 junk;
  {
    u64 pc; u32 sz;
    asm volatile(".Lcode_begin:\n\ts_getpc_b64 %0\n\ts_mov_b32 %1, .Lcode_end-.Lcode_begin" : "=s"(pc), "=s"(sz));
    const u32 co = min((jj * 8 + w) * 256 + lane * 4, sz);
    asm volatile("global_load_dword %0, %1, %2" : "=v"(junk) : "v"(co), "s"(pc));
  }
  char *const ws = p.ws;
  const bool g1 = jj < 28;
  const u32 panel = (xcd & 4) * 2 + (jj < 24 ? jj & 7 : (xcd & 1) * 4 + (jj & 3));
  const u32 ft = 7 * ((xcd >> 1) & 1) + (jj < 24 ? (xcd & 1) * 4 + (jj >> 3) : 3);
  const u32 rot1 = (jj >> 3) * 8;
  u32 tag = 0;

  u32x4 W[32];
  const u32 w1_voff = ((lane >> 4) * FF + ft * 128) * 2 + ((((lane & 15) >> 1) ^ ((lane >> 4) | ((w & 1) << 2))) * 32) + (lane & 1) * 16;
  const char *const w1_base = (const char *)((w & 1) ? p.wu : p.wg) + (size_t)((w >> 1) * 4) * (FF * 2);
  auto w1_one = [&](u32 ks, u32 dst) {
    glds(w1_voff, w1_base + (size_t)((ks + rot1) & 31) * 16 * (FF * 2), dst + w * 1024);
  };
  const u32 g4 = lane >> 4, q4 = (lane >> 2) & 3, p4 = lane & 3;
  const u32 w1_rd = (g4 >> 1) * 2048 + (g4 & 1) * 1024 + q4 * 256 + ((w ^ (q4 | ((g4 & 1) << 2))) * 32) + p4 * 8;


  const u32 cp = xcd * 32 + jj, d0 = 2 * cp;
  const u32 ch = w >> 2, job = w & 3;
  u32x4 xr; u32 pl, pa, pr, pi;
  {
    const u32 o1 = (8 * b + w) * (DM * 2) + lane * 16;
    asm volatile("global_load_dwordx4 %0, %1, %2" : "=v"(xr) : "v"(o1), "s"(p.x));
    const u32 o2 = ((d0 + ch) * NMODE + 2 * lane) * 2;
    asm volatile("global_load_dword %0, %1, %2" : "=v"(pl) : "v"(o2), "s"(p.logA));
    asm volatile("global_load_dword %0, %1, %2" : "=v"(pa) : "v"(o2), "s"(p.Aim));
    asm volatile("global_load_dword %0, %1, %2" : "=v"(pr) : "v"(o2), "s"(p.Cr));
    asm volatile("global_load_dword %0, %1, %2" : "=v"(pi) : "v"(o2), "s"(p.Ci));
  }
  u32 ldt2, dsk2;
  asm volatile("s_load_dword %0, %1, 0x0" : "=s"(tag) : "s"(ws) : "memory");
  asm volatile("s_load_dword %0, %1, 0x0" : "=s"(ldt2) : "s"(p.logdt + d0));
  asm volatile("s_load_dword %0, %1, 0x0" : "=s"(dsk2) : "s"(p.Dsk + d0));
  {
#pragma unroll
    for (int i = 0; i < 4; i++) w1_one(i, L_W0 + i * 8192);
    const u32 gw = b * 8 + w, po = (gw < 1792 ? gw : gw - 1024) * 1024 + lane * 16;
    glds(po, p.wg, L_W0 + 65536 + w * 1024); glds(po, p.wu, L_W0 + 65536 + w * 1024); glds(po, p.wd, L_W0 + 65536 + w * 1024);
  }
  asm volatile("s_waitcnt lgkmcnt(0)" : "+s"(tag), "+s"(ldt2), "+s"(dsk2));
  tag += 1u;
  auto poll = [&](u32 fo) {
#pragma unroll 1
    for (int spin = 0; spin < SPIN_CAP; spin++) {
      u32 f;
      asm volatile("global_load_dword %0, %1, %2 sc1\n\ts_waitcnt vmcnt(0)" : "=&v"(f) : "v"(fo), "s"(ws) : "memory");
      if (__all(f == tag)) break;
      __builtin_amdgcn_s_sleep(4);
    }
  };
  asm volatile("s_waitcnt vmcnt(11)" : "+v"(xr));
  {
    *lds<u32x4>(L_TX + w * T_STR + lane * 16) = xr;
    float v[8];
#pragma unroll
    for (int i = 0; i < 4; i++) { v[2 * i] = bf_lo(xr[i]); v[2 * i + 1] = bf_hi(xr[i]); }
    const float m = wave_sum(((v[0] + v[1]) + (v[2] + v[3])) + ((v[4] + v[5]) + (v[6] + v[7]))) * (1.f / DM);
    float q0 = 0, q1 = 0;
#pragma unroll
    for (int i = 0; i < 4; i++) { const float t0 = v[i] - m, t1 = v[i + 4] - m; q0 += t0 * t0; q1 += t1 * t1; }
    const float var = rbf(wave_sum(q0 + q1) * (1.f / DM));
    const float rs = __builtin_amdgcn_rcpf(rbf(__builtin_amdgcn_sqrtf(rbf(var + 1e-5f)))), mb = rbf(m);
    u32 uu[4];
#pragma unroll
    for (int i = 0; i < 4; i++) {
      const u32 a = pk_bf16(v[2 * i] - mb, v[2 * i + 1] - mb);
      const u32 u = pk_bf16(bf_lo(a) * rs, bf_hi(a) * rs);
      uu[i] = pk_f16(bf_lo(u), bf_hi(u));
    }
    u32x4 o; o[0] = uu[0]; o[1] = uu[1]; o[2] = uu[2]; o[3] = uu[3];
    *lds<u32x4>(L_T + w * T_STR + lane * 16) = o;
  }
  BAR();
  {
    __amdgpu_buffer_rsrc_t rs = __builtin_amdgcn_make_buffer_rsrc(ws + WS_EX, 0, 8u << 20, 0x00020000);
    const u32 ra = L_T + (4 * (p4 & 1) + 3 - q4) * T_STR + (64 * w + 8 * g4 + 4 * (p4 >> 1)) * 2;
    const u32 rx = L_TX + (3 - q4) * T_STR + (64 * w + 16 * g4 + 4 * p4) * 2;
    const u32 so = ((lane >> 3) * 256 + b) * 128 + (lane & 7) * 16;
    u32x2 r[2];
#pragma unroll
    for (int i = 0; i < 2; i++) r[i] = tr_read(ra + i * 64);
    const u32x4 xt = cat(tr_read(rx + 4 * T_STR), tr_read(rx));
#pragma unroll
    for (int i = 0; i < 2; i++) {
      u32x4 o; o[0] = r[i][0]; o[1] = tag; o[2] = r[i][1]; o[3] = tag;
      __builtin_amdgcn_raw_buffer_store_b128(o, rs, so + (16 * w + 8 * i) * 32768, 0, 16);
    }
    __builtin_amdgcn_raw_buffer_store_b128(xt, rs, so + (WS_XT - WS_EX) + 8 * w * 32768, 0, 16);
  }
  u32 vidx[8];
  {
    const u32 l0 = 8 * (tid & 255);
#pragma unroll
    for (int e = 0; e < 8; e++) {
      const u32 l = l0 + e, bits = pk_bf16((float)l, 0.f) & 0xffffu;
      vidx[e] = L_VT + (tid >> 8) * VT_CH + 2 * (l < 256 ? l : bits - (0x4380u - 256u));
    }
#pragma unroll
    for (int e = 0; e < 8; e++) asm volatile("" : "+v"(vidx[e]));
  }
  asm volatile("s_waitcnt vmcnt(10)" : "+v"(pl), "+v"(pa), "+v"(pr), "+v"(pi));
  const float dt = rbf(__expf(ch ? bf_hi(ldt2) : bf_lo(ldt2)));
  {
    const float LOG2E = 1.4426950408889634f, INV2PI = 0.15915494309189535f;
    float lr[2], rv[2];
    lr[0] = -rbf(__expf(bf_lo(pl))) * dt * LOG2E; lr[1] = -rbf(__expf(bf_hi(pl))) * dt * LOG2E;
    rv[0] = bf_lo(pa) * dt * INV2PI; rv[1] = bf_hi(pa) * dt * INV2PI;
    const u32 wb = L_XY + ch * XY_CH + lane * 8;
    auto put = [&](int row, cf a, cf c) {
      u32x2 v; v[0] = pk_f16(a.r, a.i); v[1] = pk_f16(c.r, c.i);
      *lds<u32x2>(wb + row * XY_STR) = v;
    };
    auto G = [&](int i) { w1_one(4 + i, L_W0 + (4 + i) * 8192); if (i >= 2) w1_one(6 + i, L_W0 + (6 + i) * 8192); };
    if (job < 2) {
      G(0);
      cf s0 = cexp_at(lr[0], -rv[0], 8.f), s1 = cexp_at(lr[1], -rv[1], 8.f);
      const float p0 = job ? 128.f : 0.f;
      cf z0 = cexp_at(lr[0], -rv[0], p0), z1 = cexp_at(lr[1], -rv[1], p0);
      const u32 wb2 = wb + job * 16 * XY_STR;
#pragma unroll
      for (int i = 0; i < 16; i++) {
        u32x2 v; v[0] = pk_f16(z0.r, z0.i); v[1] = pk_f16(z1.r, z1.i);
        *lds<u32x2>(wb2 + i * XY_STR) = v;
        if (i < 15) { z0 = cmul(z0, s0); z1 = cmul(z1, s1); }
        if (i == 2 || i == 7 || i == 12) G(1 + i / 5);
        SCHED();
      }
    } else {
      G(0);
      cf c0 = {bf_lo(pr), bf_lo(pi)}, c1 = {bf_hi(pr), bf_hi(pi)};
      const float mx = wave_max(fmaxf(fmaxf(fabsf(c0.r), fabsf(c0.i)), fmaxf(fabsf(c1.r), fabsf(c1.i))));
      const int ex = __builtin_amdgcn_frexp_expf(mx);
      const float sc = __builtin_amdgcn_ldexpf(1.f, -ex);
      c0.r *= sc; c0.i *= sc; c1.r *= sc; c1.i *= sc;
      G(1);
      if (job == 2) {
        if (lane == 0) *lds<float>(L_MISC + ch * 4) = __builtin_amdgcn_ldexpf(2.f * dt, ex);
        cf s0 = cexp_at(lr[0], rv[0], 1.f), s1 = cexp_at(lr[1], rv[1], 1.f);
        cf z0 = c0, z1 = c1;
#pragma unroll
        for (int i = 0; i < 8; i++) { put(32 + i, z0, z1); if (i < 7) { z0 = cmul(z0, s0); z1 = cmul(z1, s1); } if (i == 2 || i == 7) G(2 + i / 5); SCHED(); }
        s0 = cmul(s0, s0); s1 = cmul(s1, s1);
        z0 = cmul(c0, cexp_at(lr[0], rv[0], 256.f)); z1 = cmul(c1, cexp_at(lr[1], rv[1], 256.f));
#pragma unroll
        for (int i = 0; i < 4; i++) { put(40 + i, z0, z1); if (i < 3) { z0 = cmul(z0, s0); z1 = cmul(z1, s1); } }
      } else {
        cf f0 = cexp_at(lr[0], rv[0], 4.f), f1 = cexp_at(lr[1], rv[1], 4.f);
        cf s0 = cexp_at(lr[0], rv[0], 256.f), s1 = cexp_at(lr[1], rv[1], 256.f);
        G(2); SCHED();
        cf z0 = cmul(c0, cexp_at(lr[0], rv[0], 512.f)), z1 = cmul(c1, cexp_at(lr[1], rv[1], 512.f));
        put(44, z0, z1); put(45, cmul(z0, f0), cmul(z1, f1));
        z0 = cmul(z0, s0); z1 = cmul(z1, s1);
        put(46, z0, z1); put(47, cmul(z0, f0), cmul(z1, f1));
#pragma unroll
        for (int i = 0; i < 5; i++) { z0 = cmul(z0, s0); z1 = cmul(z1, s1); put(48 + i, z0, z1); if (i == 1) G(3); SCHED(); }
      }
    }
  }
  BAR();
  u32x4 ex[2];
  const char *const exb = ws + WS_EX + (size_t)(cp >> 1) * 32768 + (cp & 1) * 32;
  const u32 exo = (tid >> 2) * 128 + (tid & 2) * 32 + (tid & 1) * 16;
  asm volatile("global_load_dwordx4 %0, %1, %2 sc1" : "=v"(ex[0]) : "v"(exo), "s"(exb));
  asm volatile("global_load_dwordx4 %0, %1, %2 sc1" : "=v"(ex[1]) : "v"(exo), "s"(exb + 16384));
  {
    const u32 rb = job >> 1, cb = job & 1;
    const u32 xa = L_XY + ch * XY_CH + (32 + 16 * rb + (lane & 15)) * XY_STR + g4 * 16;
    const u32 ya = L_XY + ch * XY_CH + (16 * cb + (lane & 15)) * XY_STR + g4 * 16;
    u32x4 fa[8], fb[8];
#pragma unroll
    for (int ks = 0; ks < 8; ks++) { fa[ks] = *lds<u32x4>(xa + ks * 64); fb[ks] = *lds<u32x4>(ya + ks * 64); }
    SCHED();
    const u32 j = 16 * cb + (lane & 15);
    u32 i0, is, i2;
    if (rb == 0) {
      i0 = g4 < 2 ? 4 * g4 + 8 * j : (g4 == 2 ? 256 + 4 * j : 384 + 2 * j); is = 1; i2 = g4 == 3 ? 64 : 2;
    } else {
      i0 = g4 == 0 ? 512 + j : 640 + j; is = g4 == 0 ? 32 : 0; i2 = 2 * is;
    }
    f32x4 a0 = {0, 0, 0, 0}, a1 = {0, 0, 0, 0};
#pragma unroll
    for (int ks = 0; ks < 8; ks += 2) { a0 = mfma16h(fa[ks], fb[ks], a0); a1 = mfma16h(fa[ks + 1], fb[ks + 1], a1); }
    a0 += a1;
    const u32 vb = L_VT + ch * VT_CH;
    if (rb == 0 || g4 == 0) {
#pragma unroll
      for (int r = 0; r < 4; r++) *lds<_Float16>(vb + 2 * (i0 + (r & 1) * is + (r >> 1) * i2)) = (_Float16)a0[r];
    } else if (g4 == 1) {
      *lds<_Float16>(vb + 2 * i0) = (_Float16)a0[0];
    }
  }
  asm volatile("s_waitcnt vmcnt(0)" : "+v"(ex[0]), "+v"(ex[1]), "+v"(junk));
#pragma unroll 1
  for (int spin = 0; spin < SPIN_CAP; spin++) {
    if (__all(ex[0][1] == tag && ex[0][3] == tag && ex[1][1] == tag && ex[1][3] == tag)) break;
    __builtin_amdgcn_s_sleep(1);
    asm volatile("global_load_dwordx4 %0, %2, %3 sc1\n\tglobal_load_dwordx4 %1, %2, %4 sc1\n\ts_waitcnt vmcnt(0)"
                 : "=&v"(ex[0]), "=&v"(ex[1]) : "v"(exo), "s"(exb), "s"(exb + 16384) : "memory");
  }
  BAR();
  if (tid == 0) __hip_atomic_store((GAS u32 *)(ws + WS_FLAG1) + b, tag, __ATOMIC_RELAXED, __HIP_MEMORY_SCOPE_AGENT);
  {
    u32 wr = L_W0 + w1_rd;
    PIN(wr);
#pragma unroll
    for (int ks = 0; ks < 10; ks++) { const u32 a = (ks < 8 ? wr : wr + 65536) + (ks & 7) * 8192; W[ks] = cat(tr_read(a), tr_read(a + 4096)); }
    SCHED();
  }
  {
    const u32 ab = L_UR + 3 * UR_STR + (tid & 1) * UR_CH + (2044 - 1024 - 8 * (tid >> 2) - 2 * (tid & 2)) * 2;
#pragma unroll
    for (int i = 0; i < 2; i++) { u32x2 v; v[0] = ex[i][0]; v[1] = ex[i][2]; *lds<u32x2>(ab + (1 - i) * 2048) = v; }
  }
  {
    u32x4 kv;
    _Float16 t[8];
#pragma unroll
    for (int e = 0; e < 8; e++) t[e] = *lds<_Float16>(vidx[e]);
#pragma unroll
    for (int e = 0; e < 4; e++) { h16x2 t2; t2[0] = t[2 * e]; t2[1] = t[2 * e + 1]; kv[e] = __builtin_bit_cast(u32, t2); }
    const u32 kb = L_K + (tid >> 8) * K_CH + (tid & 255) * 16;
    u32x2 lo, hi; lo[0] = kv[0]; lo[1] = kv[1]; hi[0] = kv[2]; hi[1] = kv[3];
    *lds<u32x2>(kb + 120) = lo; *lds<u32x2>(kb + 128) = hi;
    *lds<u32x4>(kb + 4224 + 112) = kv;
    const u32x2 z2 = {0, 0};
    u32 za;
    if (tid < 64) {
      const u32 c = tid >> 5, i = tid & 31;
      za = L_K + c * K_CH + (i < 15 ? i * 8 : 4224 + (i < 29 ? i - 15 : 0) * 8);
    } else {
      const u32 i = tid < 448 ? tid - 64 : 0, c = i / 48;
      za = L_UR + c * UR_STR + 4096 + (i - c * 48) * 8;
    }
    *lds<u32x2>(za) = z2;
  }
  BAR();
  {
    const u32 ub = L_UR + (tid >> 8) * UR_CH + (tid & 255) * 16;
    const u32x4 a = *lds<u32x4>(ub + 3 * UR_STR);
    const u32x2 c = *lds<u32x2>(ub + 3 * UR_STR + 16);
    SCHED();
#pragma unroll
    for (int i = 0; i < 4; i++) w1_one(10 + i, L_W0 + i * 8192);
    SCHED();
    u32x4 o;
    o[0] = __builtin_amdgcn_alignbit(a[1], a[0], 16); o[1] = __builtin_amdgcn_alignbit(a[2], a[1], 16);
    o[2] = __builtin_amdgcn_alignbit(a[3], a[2], 16); o[3] = __builtin_amdgcn_alignbit(c[0], a[3], 16);
    *lds<u32x4>(ub + 2 * UR_STR) = o;
    u32x4 o1; o1[0] = a[1]; o1[1] = a[2]; o1[2] = a[3]; o1[3] = c[0];
    *lds<u32x4>(ub + UR_STR) = o1;
    u32x4 o0; o0[0] = o[1]; o0[1] = o[2]; o0[2] = o[3]; o0[3] = __builtin_amdgcn_alignbit(c[1], c[0], 16);
    *lds<u32x4>(ub) = o0;
  }
#pragma unroll
  for (int i = 4; i < 8; i++) w1_one(10 + i, L_W0 + i * 8192);
  BAR();
  u32 f1;
  const u32 xp = 255 - 32 * w - (lane >> 1);
  const u32 pg = w & 1, cg = (w >> 1) & 1;
  f32x4 ya[4];
#pragma unroll
  for (int i = 0; i < 4; i++) ya[i] = f32x4{0, 0, 0, 0};
  {
    const u32 c16 = lane & 15;
    const u32 ka = L_K + ch * K_CH + (c16 & 1) * 4224 + (c16 >> 1) * 16 + g4 * 16 + cg * 4 * 64;
    const u32 ua = L_UR + ch * UR_CH + (c16 & 3) * UR_STR + (1984 - 1792 - 64 * (c16 >> 2) + 8 * g4) * 2 + cg * 4 * 64;
    u32x4 kf[2][8], uf[2][8];
    auto rd1 = [&](int s, int j) {
      if (j >= 1 && j <= 8) uf[s & 1][8 - j] = *lds<u32x4>(ua + (j - 1) * 512 + s * 64);
      else kf[s & 1][j ? j - 8 : 0] = *lds<u32x4>(ka + (j ? j - 8 : 0) * 512 + s * 64);
    };
    auto rd = [&](int s, int i) { rd1(s, 2 * i); rd1(s, 2 * i + 1); };
    u32 wr = L_W0 + w1_rd;
    PIN(wr);
    auto side = [&](int s, int i) {
      const int n = s * 8 + i;
      if (n == 5) asm volatile("global_load_dword %0, %1, %2 sc1" : "=v"(f1) : "v"(xp * 4), "s"(ws + WS_FLAG1));
      if (n == 1 || n == 3) w1_one(26 + n / 3, L_W0 + (8 + n / 3) * 8192);
      if (n == 6) { WAIT_VM(3); RAWBAR(); }
      if (n >= 6 && n < 10) {
#pragma unroll
        for (int k = 2 * n - 12; k < 2 * n - 10; k++) W[10 + k] = cat(tr_read(wr + k * 8192), tr_read(wr + k * 8192 + 4096));
      }
      if (n == 11) {
        asm volatile("s_waitcnt vmcnt(0)" : "+v"(f1));
        if (!__all(f1 == tag)) poll(WS_FLAG1 + xp * 4);
        glds_sc1(xp * 128 + (lane & 1) * 16, ws + WS_XT + (size_t)(cp >> 2) * 32768 + (cp & 3) * 32, L_XS + w * 1024);
      }
      if (n == 13) BAR();
      if (n >= 13 && n < 29 && (n & 1)) w1_one(18 + (n - 13) / 2, L_W0 + (n - 13) / 2 * 8192);
    };
#pragma unroll
    for (int i = 0; i < 8; i++) rd(0, i);
    if (pg == 0) {
#pragma unroll
      for (int s = 0; s < 4; s++) {
#pragma unroll
        for (int i = 0; i < 8; i++) {
          if (s < 3) rd(s + 1, i);
          side(s, i);
          ya[3] = mfma16h(uf[s & 1][7 - i], kf[s & 1][i], ya[3]);
          if (i <= 4) ya[2] = mfma16h(uf[s & 1][4 - i], kf[s & 1][i], ya[2]);
          if (i <= 3) ya[1] = mfma16h(uf[s & 1][3 - i], kf[s & 1][i], ya[1]);
          if (i <= 0) ya[0] = mfma16h(uf[s & 1][0 - i], kf[s & 1][i], ya[0]);
          SCHED();
        }
      }
    } else {
#pragma unroll
      for (int s = 0; s < 4; s++) {
#pragma unroll
        for (int i = 0; i < 8; i++) {
          if (s < 3) rd(s + 1, i);
          side(s, i);
          if (i <= 6) ya[3] = mfma16h(uf[s & 1][6 - i], kf[s & 1][i], ya[3]);
          if (i <= 5) ya[2] = mfma16h(uf[s & 1][5 - i], kf[s & 1][i], ya[2]);
          if (i <= 2) ya[1] = mfma16h(uf[s & 1][2 - i], kf[s & 1][i], ya[1]);
          if (i <= 1) ya[0] = mfma16h(uf[s & 1][1 - i], kf[s & 1][i], ya[0]);
          SCHED();
        }
      }
    }
  }
  WAIT_VM(8);
  {
    const u32 yb = L_YP + w * 2048 + lane * 16;
    *lds<f32x4>(yb) = cg ? ya[0] : ya[2];
    *lds<f32x4>(yb + 1024) = cg ? ya[1] : ya[3];
  }
  BAR();
  w1_one(28, L_W2); w1_one(29, L_W2 + 8192);
  {
    const u32 yb = L_YP + (w ^ 2) * 2048 + lane * 16;
    const float ksc = *lds<float>(L_MISC + ch * 4), dsk = ch ? bf_hi(dsk2) : bf_lo(dsk2);
    __amdgpu_buffer_rsrc_t rs = __builtin_amdgcn_make_buffer_rsrc(ws + WS_HT, 0, SEQ * DM * 2, 0x00020000);
    f32x4 o[2]; u32x2 uu[2], xx[2]; u32 t0[2];
#pragma unroll
    for (int i = 0; i < 2; i++) {
      o[i] = *lds<f32x4>(yb + i * 1024);
      t0[i] = 256 * (4 * cg + (i ? 3 - pg : pg)) + 4 * lane;
      uu[i] = *lds<u32x2>(L_UR + ch * UR_CH + 3 * UR_STR + (2044 - t0[i]) * 2);
      xx[i] = *lds<u32x2>(L_XS + ((2044 - t0[i]) >> 3) * 32 + ch * 16 + ((2044 - t0[i]) & 7) * 2);
    }
#pragma unroll
    for (int i = 0; i < 2; i++) {
      const f32x4 own = cg ? (i ? ya[3] : ya[2]) : (i ? ya[1] : ya[0]);
      const u32 uu0 = uu[i][0], uu1 = uu[i][1];
      const h16x2 ua = __builtin_bit_cast(h16x2, uu1), ub = __builtin_bit_cast(h16x2, uu0);
      const float uv[4] = {(float)ua[1], (float)ua[0], (float)ub[1], (float)ub[0]};
      const float xv[4] = {bf_hi(xx[i][1]), bf_lo(xx[i][1]), bf_hi(xx[i][0]), bf_lo(xx[i][0])};
      float hv[4];
#pragma unroll
      for (int r = 0; r < 4; r += 2) {
        const u32 y2 = pk_bf16((o[i][r] + own[r]) * ksc, (o[i][r + 1] + own[r + 1]) * ksc);
        const u32 d2 = pk_bf16(uv[r] * dsk, uv[r + 1] * dsk);
        const u32 z2 = pk_bf16(bf_lo(y2) + bf_lo(d2), bf_hi(y2) + bf_hi(d2));
        hv[r] = xv[r] + bf_lo(z2); hv[r + 1] = xv[r + 1] + bf_hi(z2);
      }
      u32x2 hv2; hv2[0] = pk_bf16(hv[0], hv[1]); hv2[1] = pk_bf16(hv[2], hv[3]);
      const u32 off = ((t0[i] >> 5) * DM + d0 + ch) * 64 + (t0[i] & 31) * 2;
      __builtin_amdgcn_raw_buffer_store_b64(hv2, rs, off, 0, 16);
    }
  }
  SCHED();
  if (b == 0 && tid == 0)
    __hip_atomic_store((GAS u32 *)(ws + WS_EPOCH), tag, __ATOMIC_RELAXED, __HIP_MEMORY_SCOPE_AGENT);
  BAR();
  f32x16 cs;
#pragma unroll
  for (int i = 0; i < 16; i++) cs[i] = 0;
  const u32x4 ones = {0x3f803f80u, 0x3f803f80u, 0x3f803f80u, 0x3f803f80u};
  w1_one(30, L_YP); w1_one(31, L_YP + 8192);
#pragma unroll
  for (int ks = 0; ks < 10; ks++) cs = mfma32(W[ks], ones, cs);
  SCHED();
  WAIT_VM(2);
  RAWBAR();
  if (tid == 0) __hip_atomic_store((GAS u32 *)(ws + WS_FLAG2) + b * 32, tag, __ATOMIC_RELAXED, __HIP_MEMORY_SCOPE_AGENT);
  const u32 t32 = lane & 31;
  const u32 nt = jj & 7, tt = jj >> 3, pn2 = 2 * xcd + (tt >> 1), tgA = tt & 1, tgB = 3 - tgA;
  const u32 lv = lane * 16, rot2 = nt == 7 ? 0 : nt;
  auto ft_of = [&](int i) { const u32 k = (i % 7) + rot2; return 2 * (k >= 7 ? k - 7 : k) + (i < 7 ? tgA ^ 1 : tgA); };
  const u32 d_voff = (lane >> 3) * (DM * 2) + (((lane & 7) ^ (((lane >> 4) & 1) << 2)) * 16);
  const char *const wd_base = (const char *)p.wd + (size_t)(w * 16) * (DM * 2) + nt * 128;
  auto wd_one = [&](int u) {
    const u32 k = __builtin_amdgcn_readfirstlane((ft_of(u >> 1) << 17) + ((u & 1) << 13));
    glds(d_voff, wd_base + k, wd_slot(u) * 8192 + w * 1024);
  };
  u32x4 D[28];
  u32 d_rd[2][3];
#pragma unroll
  for (int h = 0; h < 2; h++) {
    d_rd[h][0] = w * 1024 + (((4 * (g4 >> 1) + q4) * 128 + h * 64 + (g4 & 1) * 32 + p4 * 8) ^ (((q4 >> 1) & 1) * 64));
    d_rd[h][1] = d_rd[h][0] + 65536; d_rd[h][2] = d_rd[h][0] + 131072;
  }
  auto wd_take = [&](int j, int h) {
    const u32 o = wd_slot(2 * j) * 8192;
    D[2 * j + h] = cat(tr_read(d_rd[h][o >> 16] + (o & 65535)), tr_read(d_rd[h][o >> 16] + (o & 65535) + 8192));
  };
  auto lane_again = []() {
    u32 l; asm volatile("v_mbcnt_lo_u32_b32 %0, -1, 0\n\tv_mbcnt_hi_u32_b32 %0, -1, %0" : "=v"(l)); return l;
  };
  auto foA_of = [&]() { return WS_FLAG3 + ((pn2 * 4 + tgA) * 16 + min(lane_again(), 13u)) * 128; };
  __amdgpu_buffer_rsrc_t prs = __builtin_amdgcn_make_buffer_rsrc(ws + WS_P, 0, SEQ * FF * 2, 0x00020000);
  __amdgpu_buffer_rsrc_t frs = __builtin_amdgcn_make_buffer_rsrc(ws + WS_FLAG3, 0, 131072, 0x00020000);
  const u32 OOB = 0x40000000u;
  const u32 p_off = g1 ? (panel * 4 * 112 + ft * 8 + w) * 1024 + lane * 16 : OOB;
  const u32 f_off = g1 && tid == 0 ? (panel * 64 + ft) * 128 : OOB;
  auto tg_of = [&](int g) { return g < 2 ? (u32)g : g ^ (ft & 1); };
  float e_m, e_ne, e_s2, e_g, e_u, e_e, e_o;
  u32 pko[4];
  u32x2 sg;
  const u32 sg_off = WS_ST + (panel * 128 + t32) * 8;
  auto st_take = [&](int tg) {
    PIN(sg);
#pragma unroll 1
    for (int spin = 0; spin < SPIN_CAP; spin++) {
      if (__all(sg[1] == tag)) break;
      __builtin_amdgcn_s_sleep(1);
      asm volatile("global_load_dwordx2 %0, %1, %2 sc1\n\ts_waitcnt vmcnt(0)" : "=&v"(sg) : "v"(sg_off), "s"(ws + tg_of(tg) * 256) : "memory");
    }
    e_m = bf_lo(sg[0]);
    const float sd = bf_hi(sg[0]);
    e_ne = -1.4426950408889634f * __builtin_amdgcn_rcpf(sd); e_s2 = sd * sd;
  };
  auto epi_step = [&](int ks, int tg, const f32x16 &prev) {
    if (ks == 0) st_take(tg);
    else if (ks >= 1 && ks < 25) {
      const int i = (ks - 1) / 3, ph = (ks - 1) % 3;
      if (ph == 0) {
        e_g = prev[i] - e_m * cs[i]; PIN(e_g);
        e_e = __builtin_amdgcn_exp2f(e_g * e_ne);
      } else if (ph == 1) {
        e_u = (prev[i + 8] - e_m * cs[i + 8]) * e_g;
        e_e = e_e * e_s2 + e_s2;
        PIN(e_u); PIN(e_e);
      } else {
        const float o = e_u * __builtin_amdgcn_rcpf(e_e);
        if (i & 1) { pko[i >> 1] = pk_bf16(e_o, o); PIN(pko[i >> 1]); } else { e_o = o; PIN(e_o); }
      }
    } else if (ks == 25) {
      u32x4 v; v[0] = pko[0]; v[1] = pko[1]; v[2] = pko[2]; v[3] = pko[3];
      __builtin_amdgcn_raw_buffer_store_b128(v, prs, p_off + tg_of(tg) * (112 * 1024), 0, 16);
    }
  };
  const char *const pbA = ws + WS_P + ((size_t)(pn2 * 4 + tgA) * 112 + w) * 1024;
  const char *const pbB = ws + WS_P + ((size_t)(pn2 * 4 + tgB) * 112 + w) * 1024;
  u32x4 PF[14], QF[14];
  auto p_one = [&](const char *pb, int i, u32x4 &d) {
    const u32 j = __builtin_amdgcn_readfirstlane(ft_of(i) << 13);
    asm volatile("global_load_dwordx4 %0, %1, %2 sc1" : "=v"(d) : "v"(lv), "s"(pb + j));
  };
  f32x16 acc[2];
  if (g1) {
    {
      u32 wr = L_W0 + w1_rd;
      PIN(wr);
#pragma unroll
      for (int ks = 0; ks < 8; ks++) W[18 + ks] = cat(tr_read(wr + ks * 8192), tr_read(wr + ks * 8192 + 4096));
      wr += 65536;
#pragma unroll
      for (int ks = 0; ks < 2; ks++) W[26 + ks] = cat(tr_read(wr + ks * 8192), tr_read(wr + ks * 8192 + 4096));
      wr = L_W2 + w1_rd;
      PIN(wr);
#pragma unroll
      for (int ks = 0; ks < 2; ks++) W[28 + ks] = cat(tr_read(wr + ks * 8192), tr_read(wr + ks * 8192 + 4096));
#pragma unroll
      for (int ks = 10; ks < 18; ks++) cs = mfma32(W[ks], ones, cs);
      SCHED();
      if (w == 0) WAIT_VM(1); else WAIT_VM(0);
      RAWBAR();
#pragma unroll
      for (int ks = 2; ks < 4; ks++) W[28 + ks] = cat(tr_read(wr + ks * 8192), tr_read(wr + ks * 8192 + 4096));
    }
    poll(WS_FLAG2 + (w * 32 + t32) * 128);
    BAR();
    const char *const a_base = ws + WS_HT + (size_t)panel * 131072;
    auto a_one = [&](int i) { glds_sc1(lv, a_base + tg_of(i >> 2) * 32768 + (((i & 3) * 8 + w + rot1) & 31) * 1024, L_A + w * 1024 + i * 8192); };
#pragma unroll
    for (int i = 0; i < 4; i++) a_one(i);
#pragma unroll
    for (int ks = 18; ks < 32; ks++) { cs = mfma32(W[ks], ones, cs); if (ks % 3 == 0 && ks < 30) { a_one(ks / 3 - 2); SCHED(); } }
    SCHED();
    u32 ab = L_A + (4 * (g4 >> 1) + q4) * 64 + (g4 & 1) * 32 + p4 * 8;
    u32x4 F[LOOK + 1];
    auto frag = [&](u32 a) { return cat(tr_read(a), tr_read(a + 512)); };
    u32 fa;
    wait_vm<g1_younger(-8, 0)>();
    RAWBAR();
    u32 ab2 = ab + 65536;
    PIN(ab); PIN(ab2);
#pragma unroll
    for (int i = 0; i < LOOK; i++) F[i] = frag(ab + i * 1024);
    static_for<0, 128>([&](auto P_) {
      constexpr int P = decltype(P_)::v, tg = P >> 5, ks = P & 31;
      if constexpr (ks == 0) {
#pragma unroll
        for (int i = 0; i < 16; i++) acc[tg & 1][i] = 0;
      }
      if constexpr (P == 8 - LOOK) { wait_vm<g1_younger(-7, P)>(); RAWBAR(); }
      if constexpr (P == 16 - LOOK) { wait_vm<g1_younger(-6, P)>(); RAWBAR(); }
      if constexpr (P == 24 - LOOK) { wait_vm<g1_younger(-5, P)>(); RAWBAR(); }
      if constexpr (P == 32 - LOOK) { wait_vm<g1_younger(-1, P)>(); RAWBAR(); }
      if constexpr (P == 48) { wait_vm<g1_younger(14, P)>(); RAWBAR(); }
      if constexpr (P == 64) { wait_vm<g1_younger(30, P)>(); RAWBAR(); }
      if constexpr (P == 66) wait_vm<g1_younger(54, P)>();
      if constexpr (P == 96) { wait_vm<g1_younger(85, P)>(); RAWBAR(); }
      if constexpr (P == 101) { wait_vm<g1_younger(90, P)>(); RAWBAR(); }
      if constexpr (g1_find(23, 2, P) >= 0) __builtin_amdgcn_raw_buffer_store_b32(tag, frs, f_off, 2048 * g1_find(23, 2, P), 16);
      if constexpr (P == G1_OPS[25]) asm volatile("global_load_dword %0, %1, %2 sc1" : "=v"(fa) : "v"(foA_of()), "s"(ws));
      if constexpr (P == 121) {
        wait_vm<g1_younger(115, P)>();
        PIN(fa);
        if (!__all(fa == tag)) poll(foA_of());
      }
      if constexpr (g1_find(54, 6, P) >= 0) p_one(pbA, g1_find(54, 6, P), PF[g1_find(54, 6, P)]);
      if constexpr (g1_find(8, 8, P) >= 0) a_one(8 + g1_find(8, 8, P));
      if constexpr (g1_find(16, 4, P) >= 0)
        asm volatile("global_load_dwordx2 %0, %1, %2 sc1" : "=v"(sg) : "v"(sg_off), "s"(ws + tg_of(g1_find(16, 4, P)) * 256));
      if constexpr (g1_find(26, 28, P) >= 0) wd_one(g1_find(26, 28, P));
      if constexpr (g1_take(P) >= 0) wd_take(g1_take(P) >> 1, g1_take(P) & 1);
      constexpr int n = P + LOOK;
      if constexpr (n < 128) F[n % (LOOK + 1)] = frag((n < 64 ? ab : ab2) + (n & 63) * 1024);
      acc[tg & 1] = mfma32(W[ks], F[P % (LOOK + 1)], acc[tg & 1]);
      if constexpr (tg > 0 && ks == epi0(tg)) wait_vm<g1_younger(G1_OPS[16 + tg - 1], P + 1)>();
      if constexpr (tg > 0 && ks >= epi0(tg)) epi_step(ks - epi0(tg), tg - 1, acc[(tg - 1) & 1]);
      SCHED();
    });
    wait_vm<g1_younger(119, 128)>();
    st_take(3);
  } else {
    poll(WS_FLAG2 + (w * 32 + t32) * 128);
    RAWBAR();
    const u32 tgs = (2 * xcd + ((jj >> 1) & 1)) * 4 + (jj & 1);
    const char *base = ws + WS_HT + (size_t)tgs * 32768 + w * 4096;
#pragma unroll
    for (int i = 0; i < 8; i++) glds_sc1(lv, base + (i >> 2) * 65536 + (i & 3) * 1024, 65536 + (i >> 2) * 32768 + w * 4096 + (i & 3) * 1024);
    const u32 ab = 65536 + w * 4096 + (4 * (g4 >> 1) + q4) * 64 + (g4 & 1) * 32 + p4 * 8;
#pragma unroll
    for (int g = 0; g < 2; g++) {
      if (g == 0) WAIT_VM(4); else WAIT_VM(0);
      float s_s = 0, s_q = 0;
#pragma unroll
      for (int i = 0; i < 4; i++) {
        const u32x4 f = cat(tr_read(ab + g * 32768 + i * 1024), tr_read(ab + g * 32768 + i * 1024 + 512));
#pragma unroll
        for (int k = 0; k < 4; k++) { s_s = dot2bf(f[k], 0x3f803f80u, s_s); s_q = dot2bf(f[k], f[k], s_q); }
      }
      const auto sw = __builtin_amdgcn_permlane32_swap(__float_as_uint(s_s), __float_as_uint(s_q), false, false);
      *lds<float>(g * 2048 + w * 256 + lane * 4) = __uint_as_float(sw[0]) + __uint_as_float(sw[1]);
      BAR();
      float sp = 0;
#pragma unroll
      for (int i = 0; i < 8; i++) sp += *lds<float>(g * 2048 + i * 256 + lane * 4);
      const auto s2 = __builtin_amdgcn_permlane32_swap(__float_as_uint(sp), __float_as_uint(sp), false, false);
      const float mean = __uint_as_float(s2[0]) * (1.f / DM);
      const float var = rbf(fmaxf(__uint_as_float(s2[1]) * (1.f / DM) - mean * mean, 0.f));
      const u64 gr = ((u64)tag << 32) | pk_bf16(mean, __builtin_amdgcn_sqrtf(rbf(var + 1e-5f)));
      if (tid < 32) __hip_atomic_store((GAS u64 *)(ws + WS_ST) + (tgs + 2 * g) * 32 + lane, gr, __ATOMIC_RELAXED, __HIP_MEMORY_SCOPE_AGENT);
    }
    BAR();
#pragma unroll
    for (int u = 0; u < 12; u++) wd_one(u);
    WAIT_VM(0);
#pragma unroll
    for (int j = 0; j < 6; j++) { wd_take(j, 0); wd_take(j, 1); }
    WAIT_LGKM0();
#pragma unroll
    for (int u = 12; u < 28; u++) wd_one(u);
    poll(foA_of());
#pragma unroll
    for (int j = 6; j < 9; j++) { wd_take(j, 0); wd_take(j, 1); }
#pragma unroll
    for (int i = 0; i < 6; i++) p_one(pbA, i, PF[i]);
    asm volatile("s_waitcnt vmcnt(0)" : "+v"(PF[0]), "+v"(PF[1]), "+v"(PF[2]), "+v"(PF[3]), "+v"(PF[4]), "+v"(PF[5]));
#pragma unroll
    for (int i = 0; i < 16; i++) acc[1][i] = 0;
    e_m = 0; e_ne = 0; e_s2 = 1.f;
  }

  {
    const u32 ln = lane_again(), hh = ln >> 5, tl = ln & 31;
    const u32 foB = WS_FLAG3 + ((pn2 * 4 + tgB) * 16 + 2 * min(ln, 6u)) * 128;
    p_one(pbA, 6, PF[6]); p_one(pbA, 7, PF[7]);
    SCHED();
    {
      const f32x2 m2 = {e_m, e_m}, ne2 = {e_ne, e_ne}, s22 = {e_s2, e_s2};
      u32x4 v;
#pragma unroll
      for (int i = 0; i < 8; i += 2) {
        const f32x2 g = f32x2{acc[1][i], acc[1][i + 1]} - m2 * f32x2{cs[i], cs[i + 1]};
        const f32x2 u = (f32x2{acc[1][i + 8], acc[1][i + 9]} - m2 * f32x2{cs[i + 8], cs[i + 9]}) * g;
        f32x2 e = g * ne2;
        e[0] = __builtin_amdgcn_exp2f(e[0]); e[1] = __builtin_amdgcn_exp2f(e[1]);
        e = e * s22 + s22;
        e[0] = __builtin_amdgcn_rcpf(e[0]); e[1] = __builtin_amdgcn_rcpf(e[1]);
        const f32x2 o = u * e;
        v[i >> 1] = pk_bf16(o[0], o[1]);
      }
      __builtin_amdgcn_raw_buffer_store_b128(v, prs, p_off + tg_of(3) * (112 * 1024), 0, 16);
    }
    SCHED();
    wait_vm<8>();
    RAWBAR();
    __builtin_amdgcn_raw_buffer_store_b32(tag, frs, f_off, tg_of(2) * 2048, 16);
    const u32 own_n = nt * 64 + w * 8;
    u32 res[8];
    const u32 roA = WS_HT + ((pn2 * 4 + tgA) * DM + own_n + 4 * hh) * 64 + tl * 2;
    const u32 roB = WS_HT + ((pn2 * 4 + tgB) * DM + own_n + 4 * hh) * 64 + tl * 2;
    f32x16 ac[2];
    f32x4 r[2];
    f32x16 bc[2];
    u32 rw = w * 1024 + lv;
    PIN(rw);
    auto red_put = [&](int t, const f32x16 &a, int o) {
      f32x4 v; v[0] = a[4 * (o & 3)]; v[1] = a[4 * (o & 3) + 1]; v[2] = a[4 * (o & 3) + 2]; v[3] = a[4 * (o & 3) + 3];
      *lds<f32x4>((t ? rw + 65536 : rw) + o * 8192) = v;
    };
    auto red_get = [&](int t) {
      f32x4 v[8];
#pragma unroll
      for (int i = 0; i < 8; i++) v[i] = *lds<f32x4>(t * 65536 + w * 8192 + i * 1024 + lv);
      r[t] = ((v[0] + v[1]) + (v[2] + v[3])) + ((v[4] + v[5]) + (v[6] + v[7]));
    };
#pragma unroll
    for (int i = 0; i < 16; i++) { ac[0][i] = 0; ac[1][i] = 0; }
    u32 fb;
    static_for<0, 14>([&](auto I_) {
      constexpr int i = decltype(I_)::v;
      constexpr int YOUNGER[7] = {0, 12, 13, 12, 12, 11, 10};
      if constexpr (i < 3) { p_one(pbA, 8 + 2 * i, PF[8 + 2 * i]); p_one(pbA, 9 + 2 * i, PF[9 + 2 * i]); }
      if constexpr (i == 4) asm volatile("global_load_dword %0, %1, %2 sc1" : "=v"(fb) : "v"(foB + (tgA ^ 1) * 128), "s"(ws));
      if constexpr (i >= 1 && i < 7) wait_vm<YOUNGER[i < 7 ? i : 0]>();
      if constexpr (i == 7) {
        if (w == 0) wait_vm<8>();
        else {
          wait_vm<0>();
          u32 fc;
          asm volatile("v_mov_b32 %0, %1" : "=v"(fc) : "v"(fb));
          if (!__all(fc == tag)) poll(foB + (tgA ^ 1) * 128);
        }
        BAR();
        __builtin_amdgcn_raw_buffer_store_b32(tag, frs, f_off, tg_of(3) * 2048, 16);
#pragma unroll
        for (int k = 0; k < 7; k++) p_one(pbB, k, QF[k]);
      }
      if constexpr (i >= 8) wait_vm<22 - i>();
      if constexpr (i < 5) { wd_take(9 + i, 0); wd_take(9 + i, 1); }
      PIN(PF[i]);
      ac[0] = mfma32(D[2 * i], PF[i], ac[0]);
      ac[1] = mfma32(D[2 * i + 1], PF[i], ac[1]);
      SCHED();
    });
#define RES_LD(i, ro, o) asm volatile("global_load_ushort %0, %1, %2 offset:" #o " sc1" : "=v"(res[i]) : "v"(ro), "s"(ws));
    RES_LD(0, roA, 0) RES_LD(1, roA, 64) RES_LD(2, roA, 128) RES_LD(3, roA, 192)
    RES_LD(4, roB, 0) RES_LD(5, roB, 64) RES_LD(6, roB, 128) RES_LD(7, roB, 192)
#undef RES_LD
#pragma unroll
    for (int i = 0; i < 16; i++) { bc[0][i] = 0; bc[1][i] = 0; }
    SCHED();
    static_for<0, 14>([&](auto I_) {
      constexpr int i = decltype(I_)::v;
      if constexpr (i == BP) {
        poll(foB + tgA * 128);
        asm volatile("" : "+v"(res[0]), "+v"(res[1]), "+v"(res[2]), "+v"(res[3]), "+v"(res[4]), "+v"(res[5]), "+v"(res[6]), "+v"(res[7]), "+v"(fb));
#pragma unroll
        for (int k = 7; k < 14; k++) p_one(pbB, k, QF[k]);
      }
      if constexpr (i == 7) { BAR(); red_get(0); }
      if constexpr (i < BP || i >= 7) wait_vm<i < 7 ? 14 - i : 13 - i>();
      PIN(QF[i]);
      bc[0] = mfma32(D[2 * i], QF[i], bc[0]);
      if constexpr (i < 4) red_put(0, ac[i >> 1], 2 * i);
      SCHED();
      if constexpr (i < 8) bc[1] = mfma32(D[2 * i + 1], QF[i], bc[1]);
      if constexpr (i < 4) red_put(0, ac[i >> 1], 2 * i + 1);
      SCHED();
    });
#pragma unroll
    for (int i = 8; i < 14; i++) {
      bc[1] = mfma32(D[2 * i + 1], QF[i], bc[1]);
      if (i >= 9 && i < 13) red_put(1, bc[0], i - 9);
      SCHED();
    }
#pragma unroll
    for (int o = 4; o < 8; o++) red_put(1, bc[1], o);
    BAR();
    red_get(1);
    u32 o4[4];
    {
      const u32 y0 = pk_bf16(r[0][0], r[0][1]), y1 = pk_bf16(r[0][2], r[0][3]), y2 = pk_bf16(r[1][0], r[1][1]), y3 = pk_bf16(r[1][2], r[1][3]);
      o4[0] = pk_bf16(bf_lo(res[0]) + bf_lo(y0), bf_lo(res[1]) + bf_hi(y0));
      o4[1] = pk_bf16(bf_lo(res[2]) + bf_lo(y1), bf_lo(res[3]) + bf_hi(y1));
      o4[2] = pk_bf16(bf_lo(res[4]) + bf_lo(y2), bf_lo(res[5]) + bf_hi(y2));
      o4[3] = pk_bf16(bf_lo(res[6]) + bf_lo(y3), bf_lo(res[7]) + bf_hi(y3));
    }
    const auto x0 = __builtin_amdgcn_permlane32_swap(o4[0], o4[2], false, false);
    const auto x1 = __builtin_amdgcn_permlane32_swap(o4[1], o4[3], false, false);
    u32x4 v; v[0] = x0[0]; v[1] = x1[0]; v[2] = x0[1]; v[3] = x1[1];
    *(u32x4 *)(p.out + (size_t)((pn2 * 4 + (hh ? tgB : tgA)) * 32 + tl) * DM + own_n) = v;
  }
  asm volatile(".Lcode_end:");
}

extern "C" void kernel_launch(void *const *d_in, const int *in_sizes, int n_in, void *d_out, int out_size, void *d_ws,
                              size_t ws_size, hipStream_t stream) {
  Params p;
  p.x = (const u16 *)d_in[0]; p.logA = (const u16 *)d_in[1]; p.Aim = (const u16 *)d_in[2]; p.Cr = (const u16 *)d_in[3];
  p.Ci = (const u16 *)d_in[4]; p.logdt = (const u16 *)d_in[5]; p.Dsk = (const u16 *)d_in[6];
  p.wg = (const u16 *)d_in[8]; p.wu = (const u16 *)d_in[9]; p.wd = (const u16 *)d_in[10];
  p.out = (u16 *)d_out; p.ws = (char *)d_ws;
  (void)hipFuncSetAttribute((const void *)fused, hipFuncAttributeMaxDynamicSharedMemorySize, LDS_BYTES);
  fused<<<256, 512, LDS_BYTES, stream>>>(p);
}
```
